```python
import torch

def _softmax(x, axis=-1):
    return torch.softmax(x, dim=axis)
def _gelu(x):
    return torch.nn.functional.gelu(x, approximate="tanh")
def _silu(x):
    return torch.nn.functional.silu(x)
def _sigmoid(x):
    return torch.sigmoid(x)
def _layernorm(x, axis=-1, eps=1e-5):
    m = x.mean(dim=axis, keepdim=True)
    v = x.var(dim=axis, keepdim=True, unbiased=False)
    return (x - m) / torch.sqrt(v + eps)
def _scatter_add(tgt, idx, src, axis=0):
    return tgt.clone().index_add_(axis, idx, src)
def _segment_sum(src, seg, n):
    out = torch.zeros((n,) + src.shape[1:], dtype=src.dtype)
    return out.index_add_(0, seg, src)
def _pad(x, pw):
    flat = tuple(v for pair in reversed(pw) for v in pair)
    return torch.nn.functional.pad(x, flat)

kernel_name = 'standalone__mha_softmax__near_ridge'

def setup_inputs():

    _w = (lambda a: (lambda t: t.to(torch.bfloat16) if t.is_floating_point() else t)(torch.as_tensor(a)))
    B, H, Sq, Sk, D = 32, 32, 256, 512, 128
    return {
        "q": _w((lambda *s: torch.randn(*s, dtype=torch.bfloat16) * 0.8)(B, H, Sq, D)),
        "k": _w((lambda *s: torch.randn(*s, dtype=torch.bfloat16) * 0.8)(B, H, Sk, D)),
        "v": _w((lambda *s: torch.randn(*s, dtype=torch.bfloat16) * 0.05)(B, H, Sk, D)),
    }

def reference(q, k, v):
    scale = q.shape[-1] ** -0.5
    scores = torch.einsum("bhqd,bhkd->bhqk", q, k) * scale
    p = _softmax(scores, axis=-1)
    return torch.einsum("bhqk,bhkd->bhqd", p, v)
```

```cpp
#include <hip/hip_runtime.h>
#include <hip/hip_bf16.h>
#include <cstdio>

using bf16 = __hip_bfloat16;
constexpr int   D = 128, NW = 8, QBLK = 32, KVBLK = 64;
constexpr float SCALE = 0.088388347648318440f;
#define ATTN_THR 8.f
constexpr float THR = ATTN_THR;
#define ATTN_LAYOUT 0
constexpr int LAYOUT = ATTN_LAYOUT;
#define ATTN_SDEPTH 2
constexpr int SDEPTH = ATTN_SDEPTH;
static_assert(SDEPTH == 1 || SDEPTH == 2, "SDEPTH is 1 or 2");
#define ATTN_B 32
#define ATTN_HQ 32
#define ATTN_HKV 32
#define ATTN_S 256
#define ATTN_SKV 512
constexpr int B = ATTN_B, HQ = ATTN_HQ, HKV = ATTN_HKV, S = ATTN_S, SKV = ATTN_SKV;
constexpr int LDQ = LAYOUT ? HQ * D : D, LDK = LAYOUT ? HKV * D : D, LDO = LDQ;
static_assert(S % (QBLK * NW) == 0 && SKV % (2 * KVBLK) == 0 && HQ % HKV == 0, "S % 256 == 0, SKV % 128 == 0 (even tile count), HQ % HKV == 0");
constexpr size_t SHM_V = KVBLK * D * 2, SHM_K = KVBLK * D * 2, SHM_ATTN = 2 * SHM_V + 2 * SHM_K + NW * 64 * 4;

using bf16x8 = __attribute__((ext_vector_type(8))) short;
using s16x4  = __attribute__((ext_vector_type(4))) short;
using f32x16 = __attribute__((ext_vector_type(16))) float;
using f32x8  = __attribute__((ext_vector_type(8))) float;
using u32x4  = __attribute__((ext_vector_type(4))) unsigned;
#define KSWZ(row, colB) ((row) * 256 + ((colB) ^ (((row) & 7) << 4)))
#define SBAR() __builtin_amdgcn_sched_barrier(0)
__device__ __forceinline__ int crow(int r, int hi) { return (r & 3) + 8 * (r >> 2) + 4 * hi; }
__device__ __forceinline__ unsigned cvtpk(float lo, float hi) {
  unsigned r; asm volatile("v_cvt_pk_bf16_f32 %0, %1, %2" : "=v"(r) : "v"(lo), "v"(hi)); return r;
}
template <typename TIn> struct Stage;
template <> struct Stage<bf16>  { using T = bf16x8;
  __device__ static __forceinline__ T ld8(const bf16* p) { return *reinterpret_cast<const bf16x8*>(p); }
  __device__ static __forceinline__ bf16x8 tobf(T x) { return x; } };
template <> struct Stage<float> { using T = f32x8;
  __device__ static __forceinline__ T ld8(const float* p) { return *reinterpret_cast<const f32x8*>(p); }
  __device__ static __forceinline__ bf16x8 tobf(T x) {
    u32x4 w = {cvtpk(x[0], x[1]), cvtpk(x[2], x[3]), cvtpk(x[4], x[5]), cvtpk(x[6], x[7])}; return *reinterpret_cast<bf16x8*>(&w); } };

__device__ __forceinline__ void partialSM(f32x16& p0, f32x16& p1, float& m_reg, float& mn, float& alpha) {
  constexpr float C = SCALE * 1.4426950408889634f;
  float pmax = p0[0]; for (int r = 1; r < 16; ++r) pmax = fmaxf(pmax, p0[r]); for (int r = 0; r < 16; ++r) pmax = fmaxf(pmax, p1[r]);
  { auto rr = __builtin_amdgcn_permlane32_swap(__float_as_uint(pmax), __float_as_uint(pmax), false, false);
    pmax = fmaxf(__uint_as_float(rr[0]), __uint_as_float(rr[1])); }
  if (__builtin_expect(__all(pmax - m_reg <= THR / SCALE), 1)) { mn = m_reg; alpha = 1.f; }
  else { mn = fmaxf(m_reg, pmax); alpha = __builtin_amdgcn_exp2f((m_reg - mn) * C); m_reg = mn; }
  float mnC = -mn * C;
  for (int r = 0; r < 16; ++r) p0[r] = fmaf(p0[r], C, mnC); for (int r = 0; r < 16; ++r) p1[r] = fmaf(p1[r], C, mnC);
  for (int r = 0; r < 16; ++r) p0[r] = __builtin_amdgcn_exp2f(p0[r]);
}
__device__ __forceinline__ void finishSM(f32x16& p0, f32x16& p1, float alpha, float& l_reg, bf16x8& pa0, bf16x8& pa1, bf16x8& pa2, bf16x8& pa3) {
  for (int r = 0; r < 16; ++r) p1[r] = __builtin_amdgcn_exp2f(p1[r]);
  float ps = 0; for (int r = 0; r < 16; ++r) ps += p0[r]; for (int r = 0; r < 16; ++r) ps += p1[r];
  { auto rr = __builtin_amdgcn_permlane32_swap(__float_as_uint(ps), __float_as_uint(ps), false, false);
    ps = __uint_as_float(rr[0]) + __uint_as_float(rr[1]); }
  l_reg = l_reg * alpha + ps;
#define PK4(P, BASE, OUT) do { unsigned a0 = cvtpk(P[BASE + 0], P[BASE + 1]), a1 = cvtpk(P[BASE + 2], P[BASE + 3]);   \
    unsigned b0 = cvtpk(P[BASE + 4], P[BASE + 5]), b1 = cvtpk(P[BASE + 6], P[BASE + 7]);                              \
    auto r0 = __builtin_amdgcn_permlane32_swap(a0, b0, false, false); auto r1 = __builtin_amdgcn_permlane32_swap(a1, b1, false, false); \
    u32x4 w = {r0[0], r1[0], r0[1], r1[1]}; OUT = *reinterpret_cast<bf16x8*>(&w); } while (0)
  PK4(p0, 0, pa0); PK4(p0, 8, pa1); PK4(p1, 0, pa2); PK4(p1, 8, pa3);
#undef PK4
}
__device__ __forceinline__ void qkt(f32x16& p0, f32x16& p1, const bf16* Ks, const bf16x8* qr, int r32, int hi) {
  p0 = f32x16{}; p1 = f32x16{};
  for (int d0 = 0; d0 < 8; ++d0) { int cb = (d0 * 16 + hi * 8) * 2;
    bf16x8 b0 = *reinterpret_cast<const bf16x8*>((const char*)Ks + KSWZ(r32, cb));
    bf16x8 b1 = *reinterpret_cast<const bf16x8*>((const char*)Ks + KSWZ(32 + r32, cb));
    p0 = __builtin_amdgcn_mfma_f32_32x32x16_bf16(b0, qr[d0], p0, 0, 0, 0);
    p1 = __builtin_amdgcn_mfma_f32_32x32x16_bf16(b1, qr[d0], p1, 0, 0, 0); }
}
__device__ __forceinline__ int v_st(int k, int c) { const int kk = (k & ~0xC) | ((k & 4) << 1) | ((k & 8) >> 1); return ((kk >> 3) * 4 + (c >> 5)) * 512 + ((kk & 7) * 32 + (c & 31)) * 2; }
__device__ __forceinline__ int v_rd_base(int lane) { return ((lane & 3) << 3) | (((lane >> 2) & 3) << 6) | (((lane >> 4) & 1) << 5) | (((lane >> 5) & 1) << 8); }
constexpr int v_rd_off(int d0, int ks, int half) { return d0 * 512 + ks * 4096 + half * 2048; }
template <int OFF> __device__ __forceinline__ s16x4 tr_read(int vb) {
  s16x4 r; asm volatile("ds_read_b64_tr_b16 %0, %1 offset:%2" : "=&v"(r) : "v"(vb), "i"(OFF) : "memory"); return r;
}
template <int D0> __device__ __forceinline__ void pv_one(f32x16& od, int vb, bf16x8 pa0, bf16x8 pa1, bf16x8 pa2, bf16x8 pa3) {
  const s16x4 l0 = tr_read<v_rd_off(D0, 0, 0)>(vb), h0 = tr_read<v_rd_off(D0, 0, 1)>(vb), l1 = tr_read<v_rd_off(D0, 1, 0)>(vb), h1 = tr_read<v_rd_off(D0, 1, 1)>(vb);
  const s16x4 l2 = tr_read<v_rd_off(D0, 2, 0)>(vb), h2 = tr_read<v_rd_off(D0, 2, 1)>(vb), l3 = tr_read<v_rd_off(D0, 3, 0)>(vb), h3 = tr_read<v_rd_off(D0, 3, 1)>(vb);
  asm volatile("s_waitcnt lgkmcnt(0)" ::: "memory"); SBAR();
#define PK(L, H) (bf16x8){L[0], L[1], L[2], L[3], H[0], H[1], H[2], H[3]}
  od = __builtin_amdgcn_mfma_f32_32x32x16_bf16(pa0, PK(l0, h0), od, 0, 0, 0);
  od = __builtin_amdgcn_mfma_f32_32x32x16_bf16(pa1, PK(l1, h1), od, 0, 0, 0);
  od = __builtin_amdgcn_mfma_f32_32x32x16_bf16(pa2, PK(l2, h2), od, 0, 0, 0);
  od = __builtin_amdgcn_mfma_f32_32x32x16_bf16(pa3, PK(l3, h3), od, 0, 0, 0);
#undef PK
}
__device__ __forceinline__ void pv_d0(f32x16* o, int vb, bf16x8 pa0, bf16x8 pa1, bf16x8 pa2, bf16x8 pa3) {
  pv_one<0>(o[0], vb, pa0, pa1, pa2, pa3); pv_one<1>(o[1], vb, pa0, pa1, pa2, pa3); pv_one<2>(o[2], vb, pa0, pa1, pa2, pa3); pv_one<3>(o[3], vb, pa0, pa1, pa2, pa3);
}

template <typename TQ>
__device__ __forceinline__ void attn_dense_body(const TQ* __restrict__ Qb, const bf16* __restrict__ Kh, const bf16* __restrict__ Vh,
                                                bf16* __restrict__ Ob, int seq, char* lds) {
  using St = Stage<bf16>; using SQ = Stage<TQ>;
  const int tid = threadIdx.x, wid = tid >> 6, lane = tid & 63, r32 = lane & 31, hi = lane >> 5;
  bf16* V_lds = (bf16*)lds; bf16* K_lds = (bf16*)(lds + 2 * SHM_V);
  float* ws = (float*)(lds + 2 * SHM_V + 2 * SHM_K) + wid * 64; float* li_l = ws; float* al_l = ws + 32;
  float m_reg = -1e30f, l_reg = 0; f32x16 o[4] = {}; bf16x8 qr[8];
  const TQ* Qw = Qb + (long)(wid * QBLK + r32) * LDQ + hi * 8;
#pragma unroll
  for (int d0 = 0; d0 < 8; ++d0) qr[d0] = SQ::tobf(SQ::ld8(Qw + d0 * 16));
  const int sr = tid >> 4, sc = (tid & 15) * 8, vst0 = v_st(sr, sc), vst1 = v_st(32 + sr, sc);
  const int vb0 = (int)(uintptr_t)V_lds + v_rd_base(lane);
  struct { typename St::T vs0, vs1, ks0, ks1; } sr_[SDEPTH];
#define SLOAD(i, k0) do { sr_[i].vs0 = St::ld8(&Vh[(long)((k0) + sr) * LDK + sc]); sr_[i].vs1 = St::ld8(&Vh[(long)((k0) + 32 + sr) * LDK + sc]); \
    sr_[i].ks0 = St::ld8(&Kh[(long)((k0) + sr) * LDK + sc]); sr_[i].ks1 = St::ld8(&Kh[(long)((k0) + 32 + sr) * LDK + sc]); } while (0)
#define SWRITE(b, i) do { *(bf16x8*)((char*)V_lds + (b) * SHM_V + vst0) = St::tobf(sr_[i].vs0);          \
    *(bf16x8*)((char*)V_lds + (b) * SHM_V + vst1) = St::tobf(sr_[i].vs1); int kc = sc * 2;               \
    *(bf16x8*)((char*)K_lds + (b) * SHM_K + KSWZ(sr, kc)) = St::tobf(sr_[i].ks0);                       \
    *(bf16x8*)((char*)K_lds + (b) * SHM_K + KSWZ(32 + sr, kc)) = St::tobf(sr_[i].ks1); } while (0)
#define SWAIT() do { if constexpr (SDEPTH == 2) asm volatile("s_waitcnt vmcnt(4)" ::: "memory"); else asm volatile("s_waitcnt vmcnt(0)" ::: "memory"); } while (0)
#define RESC(a) do { if (__any((a) < 1.f)) { if (hi == 0) al_l[r32] = (a); asm volatile("s_waitcnt lgkmcnt(0)" ::: "memory"); \
    for (int d = 0; d < 4; ++d) for (int r = 0; r < 16; ++r) o[d][r] *= al_l[crow(r, hi)]; } } while (0)
  f32x16 pA0, pA1, pB0, pB1; float mnA, mnB, alA, alB; bf16x8 pa0, pa1, pa2, pa3; const int NT = seq / KVBLK;
  constexpr int SE = 0, SO = SDEPTH - 1;
  SLOAD(SE, 0); asm volatile("s_waitcnt vmcnt(0)" ::: "memory"); SWRITE(0, SE); __syncthreads();
  qkt(pA0, pA1, K_lds, qr, r32, hi); partialSM(pA0, pA1, m_reg, mnA, alA);
  SLOAD(SO, KVBLK); if constexpr (SDEPTH == 2) { if (2 < NT) SLOAD(SE, 2 * KVBLK); }
  SWAIT(); SWRITE(1, SO); __syncthreads();
  for (int j = 1; j + 1 < NT; j += 2) {
    SBAR(); qkt(pB0, pB1, (bf16*)((char*)K_lds + SHM_K), qr, r32, hi);
    finishSM(pA0, pA1, alA, l_reg, pa0, pa1, pa2, pa3); SBAR();
    SLOAD(SO, (j + SDEPTH) * KVBLK); SBAR();
    pv_d0(o, vb0, pa0, pa1, pa2, pa3); partialSM(pB0, pB1, m_reg, mnB, alB);
    __syncthreads(); SWAIT(); SWRITE(0, SE);
    RESC(alB); __syncthreads();
    SBAR(); qkt(pA0, pA1, K_lds, qr, r32, hi);
    finishSM(pB0, pB1, alB, l_reg, pa0, pa1, pa2, pa3); SBAR();
    if (SDEPTH == 1 || j + 3 < NT) SLOAD(SE, (j + 1 + SDEPTH) * KVBLK); SBAR();
    pv_d0(o, vb0 + (int)SHM_V, pa0, pa1, pa2, pa3); partialSM(pA0, pA1, m_reg, mnA, alA);
    __syncthreads(); SWAIT(); SWRITE(1, SO);
    RESC(alA); __syncthreads();
  }
  SBAR(); qkt(pB0, pB1, (bf16*)((char*)K_lds + SHM_K), qr, r32, hi);
  finishSM(pA0, pA1, alA, l_reg, pa0, pa1, pa2, pa3); SBAR();
  pv_d0(o, vb0, pa0, pa1, pa2, pa3); partialSM(pB0, pB1, m_reg, mnB, alB);
  __syncthreads(); RESC(alB);
  finishSM(pB0, pB1, alB, l_reg, pa0, pa1, pa2, pa3); SBAR();
  pv_d0(o, vb0 + (int)SHM_V, pa0, pa1, pa2, pa3);
  if (hi == 0) li_l[r32] = l_reg; asm volatile("s_waitcnt lgkmcnt(0)" ::: "memory");
  float rli[16];
#pragma unroll
  for (int r = 0; r < 16; ++r) rli[r] = __builtin_amdgcn_rcpf(li_l[crow(r, hi)]);
  bf16* Ow = Ob + (long)(wid * QBLK) * LDO;
#pragma unroll
  for (int r = 0; r < 16; ++r) { int orow = crow(r, hi);
    for (int d0 = 0; d0 < 4; ++d0) Ow[(long)orow * LDO + d0 * 32 + r32] = __float2bfloat16(o[d0][r] * rli[r]); }
#undef SLOAD
#undef SWRITE
#undef SWAIT
#undef RESC
}

template <typename TQ>
__global__ __launch_bounds__(64 * NW, 1)
void attn_fwd_dense(const TQ* __restrict__ Q, const bf16* __restrict__ K, const bf16* __restrict__ V, bf16* __restrict__ O, int sq, int skv, int GQA) {
  extern __shared__ __attribute__((aligned(16))) char shm[];
  const long b = blockIdx.z, h = blockIdx.y, Hq = gridDim.y, Hkv = Hq / GQA, kvh = h / GQA, i0 = (long)blockIdx.x * (QBLK * NW);
  if constexpr (LAYOUT == 0) {
    const long q0 = ((b * Hq + h) * sq + i0) * D, k0 = (b * Hkv + kvh) * skv * (long)D;
    attn_dense_body<TQ>(Q + q0, K + k0, V + k0, O + q0, skv, shm);
  } else {
    const long q0 = ((b * sq + i0) * HQ + h) * (long)D, k0 = (b * skv * HKV + kvh) * (long)D;
    attn_dense_body<TQ>(Q + q0, K + k0, V + k0, O + q0, skv, shm);
  }
}
__global__ __launch_bounds__(256) void cvt_f32_bf16(const float* __restrict__ a, bf16* __restrict__ ao, long na8,
                                                   const float* __restrict__ b, bf16* __restrict__ bo, long nb8) {
  for (long i = blockIdx.x * 256L + threadIdx.x; i < na8 + nb8; i += gridDim.x * 256L) {
    const bool inA = i < na8; const long j = inA ? i : i - na8;
    const f32x8 x = *reinterpret_cast<const f32x8*>((inA ? a : b) + j * 8);
    *reinterpret_cast<bf16x8*>((inA ? ao : bo) + j * 8) = Stage<float>::tobf(x);
  }
}

constexpr long   NK = (long)B * HKV * SKV * D;
constexpr size_t WS_KB = 0, WS_VB = ((size_t)NK * 2 + 255) / 256 * 256, WS_END = WS_VB + (size_t)NK * 2;
constexpr int    CVT_GRID = (int)((2 * NK / 8 + 255) / 256 < 4096 ? (2 * NK / 8 + 255) / 256 : 4096);
extern "C" void kernel_launch(void* const* d_in, const int* in_sizes, int n_in, void* d_out, int out_size, void* d_ws, size_t ws_size, hipStream_t stream) {
  hipFuncSetAttribute((const void*)attn_fwd_dense<bf16>, hipFuncAttributeMaxDynamicSharedMemorySize, (int)SHM_ATTN);
  hipLaunchKernelGGL(attn_fwd_dense<bf16>, dim3(S / (QBLK * NW), HQ, B), dim3(64 * NW), SHM_ATTN, stream,
                     (const bf16*)d_in[0], (const bf16*)d_in[1], (const bf16*)d_in[2], (bf16*)d_out, S, SKV, HQ / HKV);
}
```

```cpp
#include <hip/hip_runtime.h>
#include <stdint.h>

namespace {
constexpr int SQ = 256, SK = 512, D = 128, NHEADS = 1024, GRID = 256, LDS_BYTES = 163840;

__device__ __forceinline__ float bf_lo(unsigned x) { return __uint_as_float(x << 16); }
__device__ __forceinline__ float bf_hi(unsigned x) { return __uint_as_float(x & 0xffff0000u); }
__device__ __forceinline__ unsigned pack_bf16(float a, float b) {
  typedef float f32x2 __attribute__((ext_vector_type(2)));
  typedef __bf16 bf16x2 __attribute__((ext_vector_type(2)));
  f32x2 v = {a, b};
  return __builtin_bit_cast(unsigned, __builtin_convertvector(v, bf16x2));
}

__device__ __forceinline__ void slow_rows(const unsigned* __restrict__ q, const unsigned* __restrict__ k, const unsigned* __restrict__ v,
                                       unsigned* __restrict__ o, unsigned head, unsigned wave) {
  const unsigned lane = __builtin_amdgcn_mbcnt_hi(~0u, __builtin_amdgcn_mbcnt_lo(~0u, 0u));
  const unsigned* kh = k + (size_t)head * (SK * D / 2);
  const unsigned* vh = v + (size_t)head * (SK * D / 2);
  for (unsigned r = 0; r < 64; ++r) {
    const size_t row = (size_t)head * SQ + wave * 64 + r;
    const unsigned* qr = q + row * (D / 2);
    float s[8];
#pragma unroll
    for (int t = 0; t < 8; ++t) s[t] = 0.f;
    for (unsigned d2 = 0; d2 < D / 2; ++d2) {
      const unsigned qq = qr[d2];
      const float q0 = bf_lo(qq), q1 = bf_hi(qq);
#pragma unroll
      for (int t = 0; t < 8; ++t) {
        const unsigned kk = kh[(size_t)(lane + 64 * t) * (D / 2) + d2];
        s[t] = fmaf(q1, bf_hi(kk), fmaf(q0, bf_lo(kk), s[t]));
      }
    }
    float m = -INFINITY;
#pragma unroll
    for (int t = 0; t < 8; ++t) { s[t] *= 0.08838834764831845f; m = fmaxf(m, s[t]); }
    for (int x = 32; x >= 1; x >>= 1) m = fmaxf(m, __shfl_xor(m, x, 64));
    float l = 0.f;
#pragma unroll
    for (int t = 0; t < 8; ++t) { s[t] = __builtin_amdgcn_exp2f((s[t] - m) * 1.4426950408889634f); l += s[t]; }
    for (int x = 32; x >= 1; x >>= 1) l += __shfl_xor(l, x, 64);
    float a0 = 0.f, a1 = 0.f;
#pragma unroll
    for (int t = 0; t < 8; ++t) {
      for (unsigned j = 0; j < 64; ++j) {
        const float p = __shfl(s[t], (int)j, 64);
        const unsigned vv = vh[(size_t)(64 * t + j) * (D / 2) + lane];
        a0 = fmaf(p, bf_lo(vv), a0); a1 = fmaf(p, bf_hi(vv), a1);
      }
    }
    const float il = 1.0f / l;
    o[row * (D / 2) + lane] = pack_bf16(a0 * il, a1 * il);
  }
}
}

__global__ __launch_bounds__(256, 1) void mha_fwd_kernel(const unsigned* __restrict__ q, const unsigned* __restrict__ k, const unsigned* __restrict__ v, unsigned* __restrict__ o) {
  extern __shared__ __attribute__((aligned(1024))) char lds[];
  const unsigned wave = __builtin_amdgcn_readfirstlane(threadIdx.x >> 6);
  const unsigned head0 = blockIdx.x;
  const unsigned ldsb = (unsigned)(size_t)lds;
  unsigned flags;
  asm volatile(
      "s_load_dwordx8 s[92:99], %1, 0x0\n"
      "s_mov_b32 s69, %6\n"
      "v_mbcnt_lo_u32_b32 v232, -1, 0\n"
      "v_mbcnt_hi_u32_b32 v232, -1, v232\n"
      "s_getpc_b64 s[70:71]\n"
      "L_PFBASE:\n"
      "s_lshl_b32 s67, s69, 14\n"
      "s_mov_b32 s68, L_CODEEND-L_PFBASE-4\n"
      "v_lshlrev_b32 v233, 7, v232\n"
      "v_add_u32 v233, s67, v233\n"
      "v_min_u32 v234, s68, v233\n"
      "global_load_dword a255, v234, s[70:71]\n"
      "v_add_u32 v233, 0x2000, v233\n"
      "v_min_u32 v234, s68, v233\n"
      "global_load_dword a255, v234, s[70:71]\n"
      "v_lshrrev_b32 v233, 2, v232\n"
      "v_and_b32 v233, 7, v233\n"
      "v_lshlrev_b32 v212, 8, v233\n"
      "v_lshrrev_b32 v233, 5, v232\n"
      "v_lshl_add_u32 v212, v233, 6, v212\n"
      "v_lshrrev_b32 v233, 3, v232\n"
      "v_and_b32 v233, 2, v233\n"
      "v_and_b32 v234, 3, v232\n"
      "v_xor_b32 v233, v233, v234\n"
      "v_lshl_add_u32 v212, v233, 4, v212\n"
      "v_add_u32 v213, 0x80, v212\n"
      "v_add_u32 v212, 0x400, v212\n"
      "s_lshl_b32 s56, %5, 16\n"
      "s_lshl_b32 s70, s69, 14\n"
      "s_add_u32 s56, s56, s70\n"
      "s_mul_i32 s59, s69, 24576\n"
      "s_add_u32 s59, s59, %7\n"
      "s_add_u32 s59, s59, 0x10000\n"
      "s_lshl_b32 s57, %5, 17\n"
      "s_lshl_b32 s70, s69, 11\n"
      "s_add_u32 s57, s57, s70\n"
      "s_add_u32 s58, s70, %7\n"
      "s_mov_b32 s42, 0x4000400\n"
      "s_mov_b32 s43, 0x20000\n"
      "s_mov_b32 s46, 0x8000400\n"
      "s_mov_b32 s47, 0x20000\n"
      "s_mov_b32 s50, 0x8000400\n"
      "s_mov_b32 s51, 0x20000\n"
      "s_mov_b32 s54, 0x0\n"
      "s_mov_b32 s55, 0x20000\n"
      "s_waitcnt lgkmcnt(0)\n"
      "s_mov_b64 s[40:41], s[92:93]\n"
      "s_sub_u32 s40, s40, 1024\n"
      "s_subb_u32 s41, s41, 0\n"
      "s_and_b32 s41, s41, 0xffff\n"
      "s_add_u32 m0, s59, 0x2000\n"
      "s_mov_b32 s68, s56\n"
      "buffer_load_dwordx4 v212, s[40:43], s68 offen nt lds\n"
      "buffer_load_dwordx4 v213, s[40:43], s68 offen offset:1024 nt lds\n"
      "s_add_u32 m0, s59, 0x2800\n"
      "s_add_u32 s67, s56, 0x800\n"
      "buffer_load_dwordx4 v212, s[40:43], s67 offen nt lds\n"
      "buffer_load_dwordx4 v213, s[40:43], s67 offen offset:1024 nt lds\n"
      "s_mov_b64 s[44:45], s[94:95]\n"
      "s_sub_u32 s44, s44, 1024\n"
      "s_subb_u32 s45, s45, 0\n"
      "s_and_b32 s45, s45, 0xffff\n"
      "s_mov_b32 m0, s58\n"
      "s_mov_b32 s68, s57\n"
      "buffer_load_dwordx4 v212, s[44:47], s68 offen nt lds\n"
      "buffer_load_dwordx4 v213, s[44:47], s68 offen offset:1024 nt lds\n"
      "s_add_u32 m0, s59, 0x3000\n"
      "s_add_u32 s67, s56, 0x1000\n"
      "buffer_load_dwordx4 v212, s[40:43], s67 offen nt lds\n"
      "buffer_load_dwordx4 v213, s[40:43], s67 offen offset:1024 nt lds\n"
      "s_add_u32 m0, s59, 0x3800\n"
      "s_add_u32 s68, s56, 0x1800\n"
      "buffer_load_dwordx4 v212, s[40:43], s68 offen nt lds\n"
      "buffer_load_dwordx4 v213, s[40:43], s68 offen offset:1024 nt lds\n"
      "s_mov_b64 s[48:49], s[96:97]\n"
      "s_sub_u32 s48, s48, 1024\n"
      "s_subb_u32 s49, s49, 0\n"
      "s_and_b32 s49, s49, 0xffff\n"
      "s_mov_b64 s[52:53], s[98:99]\n"
      "s_and_b32 s53, s53, 0xffff\n"
      "s_mov_b32 s61, 0x3e0293ee\n"
      "s_mov_b32 s62, 0xffff0000\n"
      "s_mov_b32 s63, 0x53800000\n"
      "s_mov_b32 s64, 0\n"
      "s_mov_b32 s65, 1\n"
      "s_mov_b32 s66, 4\n"
      "s_mov_b32 s76, 0\n"
      "s_mov_b32 s77, 0\n"
      "s_mov_b32 s78, 0\n"
      "s_mov_b32 s79, 0\n"
      "v_lshlrev_b32 v214, 4, v232\n"
      "v_and_b32 v234, 15, v232\n"
      "v_lshrrev_b32 v236, 4, v232\n"
      "v_lshrrev_b32 v233, 3, v234\n"
      "v_lshlrev_b32 v208, 11, v233\n"
      "v_and_b32 v233, 7, v234\n"
      "v_lshl_add_u32 v208, v233, 6, v208\n"
      "v_lshrrev_b32 v233, 1, v234\n"
      "v_and_b32 v233, 2, v233\n"
      "v_xor_b32 v233, v233, v236\n"
      "v_lshl_add_u32 v208, v233, 4, v208\n"
      "v_add_u32 v209, s59, v208\n"
      "v_add_u32 v208, %7, v208\n"
      "v_and_b32 v233, 7, v234\n"
      "v_lshrrev_b32 v235, 1, v236\n"
      "v_xor_b32 v233, v233, v235\n"
      "v_lshlrev_b32 v233, 4, v233\n"
      "v_lshl_add_u32 v215, v234, 8, v233\n"
      "v_and_b32 v233, 1, v236\n"
      "v_lshl_add_u32 v215, v233, 3, v215\n"
      "v_add_u32 v215, s59, v215\n"
      "v_xor_b32 v216, 32, v215\n"
      "v_xor_b32 v217, 64, v215\n"
      "v_xor_b32 v218, 0x60, v215\n"
      "v_and_b32 v234, 7, v232\n"
      "v_xor_b32 v234, v236, v234\n"
      "v_and_b32 v219, 8, v232\n"
      "v_add_u32 v234, v219, v234\n"
      "v_lshlrev_b32 v234, 4, v234\n"
      "v_lshl_add_u32 v219, v236, 8, v234\n"
      "v_add_u32 v219, s59, v219\n"
      "v_xor_b32 v220, 64, v219\n"
      "v_lshrrev_b32 v233, 1, v236\n"
      "v_lshlrev_b32 v210, 11, v233\n"
      "v_and_b32 v235, 1, v236\n"
      "v_lshrrev_b32 v233, 2, v232\n"
      "v_and_b32 v233, 3, v233\n"
      "v_lshl_add_u32 v233, v235, 2, v233\n"
      "v_lshl_add_u32 v210, v233, 6, v210\n"
      "v_lshrrev_b32 v233, 1, v232\n"
      "v_and_b32 v233, 1, v233\n"
      "v_lshlrev_b32 v235, 1, v235\n"
      "v_xor_b32 v233, v233, v235\n"
      "v_lshl_add_u32 v210, v233, 4, v210\n"
      "v_and_b32 v233, 1, v232\n"
      "v_lshl_add_u32 v210, v233, 3, v210\n"
      "v_add_u32 v210, %7, v210\n"
      "v_xor_b32 v211, 32, v210\n"
      "v_mov_b32 v233, 0x3f803f80\n"
      "v_accvgpr_write_b32 a144, v233\n"
      "v_accvgpr_write_b32 a145, v233\n"
      "v_accvgpr_write_b32 a146, v233\n"
      "v_accvgpr_write_b32 a147, v233\n"
      "s_waitcnt vmcnt(6)\n"
      "ds_read_b128 v[160:163], v209 offset:8192\n"
      "ds_read_b128 v[164:167], v209 offset:8704\n"
      "ds_read_b128 v[168:171], v209 offset:9216\n"
      "ds_read_b128 v[172:175], v209 offset:9728\n"
      "s_waitcnt lgkmcnt(0)\n"
      "v_lshlrev_b32 v221, 16, v160\n"
      "v_and_b32 v160, s62, v160\n"
      "v_mul_f32 v221, s61, v221\n"
      "v_mul_f32 v160, s61, v160\n"
      "v_cvt_pk_bf16_f32 v160, v221, v160\n"
      "v_accvgpr_write_b32 a0, v160\n"
      "v_lshlrev_b32 v222, 16, v161\n"
      "v_and_b32 v161, s62, v161\n"
      "v_mul_f32 v222, s61, v222\n"
      "v_mul_f32 v161, s61, v161\n"
      "v_cvt_pk_bf16_f32 v161, v222, v161\n"
      "v_accvgpr_write_b32 a1, v161\n"
      "v_lshlrev_b32 v221, 16, v162\n"
      "v_and_b32 v162, s62, v162\n"
      "v_mul_f32 v221, s61, v221\n"
      "v_mul_f32 v162, s61, v162\n"
      "v_cvt_pk_bf16_f32 v162, v221, v162\n"
      "v_accvgpr_write_b32 a2, v162\n"
      "v_lshlrev_b32 v222, 16, v163\n"
      "v_and_b32 v163, s62, v163\n"
      "v_mul_f32 v222, s61, v222\n"
      "v_mul_f32 v163, s61, v163\n"
      "v_cvt_pk_bf16_f32 v163, v222, v163\n"
      "v_accvgpr_write_b32 a3, v163\n"
      "v_lshlrev_b32 v221, 16, v164\n"
      "v_and_b32 v164, s62, v164\n"
      "v_mul_f32 v221, s61, v221\n"
      "v_mul_f32 v164, s61, v164\n"
      "v_cvt_pk_bf16_f32 v164, v221, v164\n"
      "v_accvgpr_write_b32 a4, v164\n"
      "v_lshlrev_b32 v222, 16, v165\n"
      "v_and_b32 v165, s62, v165\n"
      "v_mul_f32 v222, s61, v222\n"
      "v_mul_f32 v165, s61, v165\n"
      "v_cvt_pk_bf16_f32 v165, v222, v165\n"
      "v_accvgpr_write_b32 a5, v165\n"
      "v_lshlrev_b32 v221, 16, v166\n"
      "v_and_b32 v166, s62, v166\n"
      "v_mul_f32 v221, s61, v221\n"
      "v_mul_f32 v166, s61, v166\n"
      "v_cvt_pk_bf16_f32 v166, v221, v166\n"
      "v_accvgpr_write_b32 a6, v166\n"
      "v_lshlrev_b32 v222, 16, v167\n"
      "v_and_b32 v167, s62, v167\n"
      "v_mul_f32 v222, s61, v222\n"
      "v_mul_f32 v167, s61, v167\n"
      "v_cvt_pk_bf16_f32 v167, v222, v167\n"
      "v_accvgpr_write_b32 a7, v167\n"
      "s_add_u32 m0, s59, 0x4000\n"
      "s_add_u32 s67, s56, 0x2000\n"
      "buffer_load_dwordx4 v212, s[40:43], s67 offen nt lds\n"
      "buffer_load_dwordx4 v213, s[40:43], s67 offen offset:1024 nt lds\n"
      "v_lshlrev_b32 v221, 16, v168\n"
      "v_and_b32 v168, s62, v168\n"
      "v_mul_f32 v221, s61, v221\n"
      "v_mul_f32 v168, s61, v168\n"
      "v_cvt_pk_bf16_f32 v168, v221, v168\n"
      "v_accvgpr_write_b32 a8, v168\n"
      "v_lshlrev_b32 v222, 16, v169\n"
      "v_and_b32 v169, s62, v169\n"
      "v_mul_f32 v222, s61, v222\n"
      "v_mul_f32 v169, s61, v169\n"
      "v_cvt_pk_bf16_f32 v169, v222, v169\n"
      "v_accvgpr_write_b32 a9, v169\n"
      "v_lshlrev_b32 v221, 16, v170\n"
      "v_and_b32 v170, s62, v170\n"
      "v_mul_f32 v221, s61, v221\n"
      "v_mul_f32 v170, s61, v170\n"
      "v_cvt_pk_bf16_f32 v170, v221, v170\n"
      "v_accvgpr_write_b32 a10, v170\n"
      "v_lshlrev_b32 v222, 16, v171\n"
      "v_and_b32 v171, s62, v171\n"
      "v_mul_f32 v222, s61, v222\n"
      "v_mul_f32 v171, s61, v171\n"
      "v_cvt_pk_bf16_f32 v171, v222, v171\n"
      "v_accvgpr_write_b32 a11, v171\n"
      "v_lshlrev_b32 v221, 16, v172\n"
      "v_and_b32 v172, s62, v172\n"
      "v_mul_f32 v221, s61, v221\n"
      "v_mul_f32 v172, s61, v172\n"
      "v_cvt_pk_bf16_f32 v172, v221, v172\n"
      "v_accvgpr_write_b32 a12, v172\n"
      "v_lshlrev_b32 v222, 16, v173\n"
      "v_and_b32 v173, s62, v173\n"
      "v_mul_f32 v222, s61, v222\n"
      "v_mul_f32 v173, s61, v173\n"
      "v_cvt_pk_bf16_f32 v173, v222, v173\n"
      "v_accvgpr_write_b32 a13, v173\n"
      "v_lshlrev_b32 v221, 16, v174\n"
      "v_and_b32 v174, s62, v174\n"
      "v_mul_f32 v221, s61, v221\n"
      "v_mul_f32 v174, s61, v174\n"
      "v_cvt_pk_bf16_f32 v174, v221, v174\n"
      "v_accvgpr_write_b32 a14, v174\n"
      "v_lshlrev_b32 v222, 16, v175\n"
      "v_and_b32 v175, s62, v175\n"
      "v_mul_f32 v222, s61, v222\n"
      "v_mul_f32 v175, s61, v175\n"
      "v_cvt_pk_bf16_f32 v175, v222, v175\n"
      "v_accvgpr_write_b32 a15, v175\n"
      "s_add_u32 m0, s59, 0x4800\n"
      "s_add_u32 s68, s56, 0x2800\n"
      "buffer_load_dwordx4 v212, s[40:43], s68 offen nt lds\n"
      "buffer_load_dwordx4 v213, s[40:43], s68 offen offset:1024 nt lds\n"
      "s_waitcnt vmcnt(4)\n"
      "s_barrier\n"
      "ds_read_b128 a[64:67], v208 offset:0\n"
      "ds_read_b128 a[68:71], v208 offset:512\n"
      "ds_read_b128 a[72:75], v208 offset:1024\n"
      "ds_read_b128 a[76:79], v208 offset:1536\n"
      "ds_read_b128 a[80:83], v208 offset:4096\n"
      "ds_read_b128 a[84:87], v208 offset:4608\n"
      "ds_read_b128 a[88:91], v208 offset:5120\n"
      "ds_read_b128 a[92:95], v208 offset:5632\n"
      "ds_read_b128 v[176:179], v209 offset:12288\n"
      "ds_read_b128 v[180:183], v209 offset:12800\n"
      "ds_read_b128 v[184:187], v209 offset:13312\n"
      "ds_read_b128 v[188:191], v209 offset:13824\n"
      "s_waitcnt lgkmcnt(0)\n"
      "v_lshlrev_b32 v221, 16, v176\n"
      "v_and_b32 v176, s62, v176\n"
      "v_mul_f32 v221, s61, v221\n"
      "v_mul_f32 v176, s61, v176\n"
      "v_cvt_pk_bf16_f32 v176, v221, v176\n"
      "v_mfma_f32_16x16x32_bf16 v[128:131], a[64:67], a[0:3], 0\n"
      "v_accvgpr_write_b32 a16, v176\n"
      "v_lshlrev_b32 v222, 16, v177\n"
      "v_and_b32 v177, s62, v177\n"
      "v_mul_f32 v222, s61, v222\n"
      "v_mul_f32 v177, s61, v177\n"
      "v_mfma_f32_16x16x32_bf16 v[128:131], a[68:71], a[4:7], v[128:131]\n"
      "v_cvt_pk_bf16_f32 v177, v222, v177\n"
      "v_accvgpr_write_b32 a17, v177\n"
      "v_lshlrev_b32 v221, 16, v178\n"
      "v_and_b32 v178, s62, v178\n"
      "v_mul_f32 v221, s61, v221\n"
      "v_mfma_f32_16x16x32_bf16 v[128:131], a[72:75], a[8:11], v[128:131]\n"
      "v_mul_f32 v178, s61, v178\n"
      "v_cvt_pk_bf16_f32 v178, v221, v178\n"
      "v_accvgpr_write_b32 a18, v178\n"
      "v_lshlrev_b32 v222, 16, v179\n"
      "v_and_b32 v179, s62, v179\n"
      "s_add_u32 m0, s59, 0x5000\n"
      "v_mul_f32 v222, s61, v222\n"
      "v_mul_f32 v179, s61, v179\n"
      "v_cvt_pk_bf16_f32 v179, v222, v179\n"
      "v_accvgpr_write_b32 a19, v179\n"
      "s_add_u32 s67, s56, 0x3000\n"
      "v_lshlrev_b32 v221, 16, v180\n"
      "v_and_b32 v180, s62, v180\n"
      "v_mul_f32 v221, s61, v221\n"
      "v_mul_f32 v180, s61, v180\n"
      "v_cvt_pk_bf16_f32 v180, v221, v180\n"
      "buffer_load_dwordx4 v212, s[40:43], s67 offen nt lds\n"
      "v_accvgpr_write_b32 a20, v180\n"
      "v_lshlrev_b32 v222, 16, v181\n"
      "v_and_b32 v181, s62, v181\n"
      "v_mul_f32 v222, s61, v222\n"
      "v_mul_f32 v181, s61, v181\n"
      "buffer_load_dwordx4 v213, s[40:43], s67 offen offset:1024 nt lds\n"
      "v_cvt_pk_bf16_f32 v181, v222, v181\n"
      "v_accvgpr_write_b32 a21, v181\n"
      "v_lshlrev_b32 v221, 16, v182\n"
      "v_and_b32 v182, s62, v182\n"
      "v_mul_f32 v221, s61, v221\n"
      "v_mfma_f32_16x16x32_bf16 v[128:131], a[76:79], a[12:15], v[128:131]\n"
      "v_mul_f32 v182, s61, v182\n"
      "v_cvt_pk_bf16_f32 v182, v221, v182\n"
      "v_accvgpr_write_b32 a22, v182\n"
      "v_lshlrev_b32 v222, 16, v183\n"
      "v_and_b32 v183, s62, v183\n"
      "v_mfma_f32_16x16x32_bf16 v[132:135], a[80:83], a[0:3], 0\n"
      "v_mul_f32 v222, s61, v222\n"
      "v_mul_f32 v183, s61, v183\n"
      "v_cvt_pk_bf16_f32 v183, v222, v183\n"
      "v_accvgpr_write_b32 a23, v183\n"
      "v_mfma_f32_16x16x32_bf16 v[132:135], a[84:87], a[4:7], v[132:135]\n"
      "v_lshlrev_b32 v221, 16, v184\n"
      "v_and_b32 v184, s62, v184\n"
      "v_mul_f32 v221, s61, v221\n"
      "v_mul_f32 v184, s61, v184\n"
      "v_cvt_pk_bf16_f32 v184, v221, v184\n"
      "s_add_u32 m0, s59, 0x5800\n"
      "v_accvgpr_write_b32 a24, v184\n"
      "v_lshlrev_b32 v222, 16, v185\n"
      "v_and_b32 v185, s62, v185\n"
      "v_mul_f32 v222, s61, v222\n"
      "v_mul_f32 v185, s61, v185\n"
      "s_add_u32 s68, s56, 0x3800\n"
      "v_cvt_pk_bf16_f32 v185, v222, v185\n"
      "v_accvgpr_write_b32 a25, v185\n"
      "v_lshlrev_b32 v221, 16, v186\n"
      "v_and_b32 v186, s62, v186\n"
      "v_mul_f32 v221, s61, v221\n"
      "buffer_load_dwordx4 v212, s[40:43], s68 offen nt lds\n"
      "v_mul_f32 v186, s61, v186\n"
      "v_cvt_pk_bf16_f32 v186, v221, v186\n"
      "v_accvgpr_write_b32 a26, v186\n"
      "v_lshlrev_b32 v222, 16, v187\n"
      "v_and_b32 v187, s62, v187\n"
      "buffer_load_dwordx4 v213, s[40:43], s68 offen offset:1024 nt lds\n"
      "v_mul_f32 v222, s61, v222\n"
      "v_mul_f32 v187, s61, v187\n"
      "v_cvt_pk_bf16_f32 v187, v222, v187\n"
      "v_accvgpr_write_b32 a27, v187\n"
      "v_mfma_f32_16x16x32_bf16 v[132:135], a[88:91], a[8:11], v[132:135]\n"
      "v_lshlrev_b32 v221, 16, v188\n"
      "v_and_b32 v188, s62, v188\n"
      "v_mul_f32 v221, s61, v221\n"
      "v_mul_f32 v188, s61, v188\n"
      "v_cvt_pk_bf16_f32 v188, v221, v188\n"
      "v_mfma_f32_16x16x32_bf16 v[132:135], a[92:95], a[12:15], v[132:135]\n"
      "v_accvgpr_write_b32 a28, v188\n"
      "v_lshlrev_b32 v222, 16, v189\n"
      "v_and_b32 v189, s62, v189\n"
      "v_mul_f32 v222, s61, v222\n"
      "v_mul_f32 v189, s61, v189\n"
      "s_add_u32 m0, s58, 0x2000\n"
      "v_cvt_pk_bf16_f32 v189, v222, v189\n"
      "v_accvgpr_write_b32 a29, v189\n"
      "v_lshlrev_b32 v221, 16, v190\n"
      "v_and_b32 v190, s62, v190\n"
      "v_mul_f32 v221, s61, v221\n"
      "s_add_u32 s67, s57, 0x2000\n"
      "v_mul_f32 v190, s61, v190\n"
      "v_cvt_pk_bf16_f32 v190, v221, v190\n"
      "v_accvgpr_write_b32 a30, v190\n"
      "v_lshlrev_b32 v222, 16, v191\n"
      "v_and_b32 v191, s62, v191\n"
      "buffer_load_dwordx4 v212, s[44:47], s67 offen nt lds\n"
      "v_mul_f32 v222, s61, v222\n"
      "v_mul_f32 v191, s61, v191\n"
      "v_cvt_pk_bf16_f32 v191, v222, v191\n"
      "v_accvgpr_write_b32 a31, v191\n"
      "buffer_load_dwordx4 v213, s[44:47], s67 offen offset:1024 nt lds\n"
      "s_waitcnt vmcnt(6)\n"
      "ds_read_b128 v[160:163], v209 offset:16384\n"
      "ds_read_b128 v[164:167], v209 offset:16896\n"
      "ds_read_b128 v[168:171], v209 offset:17408\n"
      "ds_read_b128 v[172:175], v209 offset:17920\n"
      "s_waitcnt lgkmcnt(0)\n"
      "v_lshlrev_b32 v221, 16, v160\n"
      "v_and_b32 v160, s62, v160\n"
      "v_mul_f32 v221, s61, v221\n"
      "v_mul_f32 v160, s61, v160\n"
      "v_cvt_pk_bf16_f32 v160, v221, v160\n"
      "v_mfma_f32_16x16x32_bf16 v[136:139], a[64:67], a[16:19], 0\n"
      "v_accvgpr_write_b32 a32, v160\n"
      "v_lshlrev_b32 v222, 16, v161\n"
      "v_and_b32 v161, s62, v161\n"
      "v_mul_f32 v222, s61, v222\n"
      "v_mul_f32 v161, s61, v161\n"
      "v_mfma_f32_16x16x32_bf16 v[136:139], a[68:71], a[20:23], v[136:139]\n"
      "v_cvt_pk_bf16_f32 v161, v222, v161\n"
      "v_accvgpr_write_b32 a33, v161\n"
      "v_lshlrev_b32 v221, 16, v162\n"
      "v_and_b32 v162, s62, v162\n"
      "v_mul_f32 v221, s61, v221\n"
      "v_mfma_f32_16x16x32_bf16 v[136:139], a[72:75], a[24:27], v[136:139]\n"
      "v_mul_f32 v162, s61, v162\n"
      "v_cvt_pk_bf16_f32 v162, v221, v162\n"
      "v_accvgpr_write_b32 a34, v162\n"
      "v_lshlrev_b32 v222, 16, v163\n"
      "v_and_b32 v163, s62, v163\n"
      "s_add_u32 m0, s58, 0x8000\n"
      "v_mul_f32 v222, s61, v222\n"
      "v_mul_f32 v163, s61, v163\n"
      "v_cvt_pk_bf16_f32 v163, v222, v163\n"
      "v_accvgpr_write_b32 a35, v163\n"
      "s_mov_b32 s68, s57\n"
      "v_lshlrev_b32 v221, 16, v164\n"
      "v_and_b32 v164, s62, v164\n"
      "v_mul_f32 v221, s61, v221\n"
      "v_mul_f32 v164, s61, v164\n"
      "v_cvt_pk_bf16_f32 v164, v221, v164\n"
      "buffer_load_dwordx4 v212, s[48:51], s68 offen nt lds\n"
      "v_accvgpr_write_b32 a36, v164\n"
      "v_lshlrev_b32 v222, 16, v165\n"
      "v_and_b32 v165, s62, v165\n"
      "v_mul_f32 v222, s61, v222\n"
      "v_mul_f32 v165, s61, v165\n"
      "buffer_load_dwordx4 v213, s[48:51], s68 offen offset:1024 nt lds\n"
      "v_cvt_pk_bf16_f32 v165, v222, v165\n"
      "v_accvgpr_write_b32 a37, v165\n"
      "v_lshlrev_b32 v221, 16, v166\n"
      "v_and_b32 v166, s62, v166\n"
      "v_mul_f32 v221, s61, v221\n"
      "v_mfma_f32_16x16x32_bf16 v[136:139], a[76:79], a[28:31], v[136:139]\n"
      "v_mul_f32 v166, s61, v166\n"
      "v_cvt_pk_bf16_f32 v166, v221, v166\n"
      "v_accvgpr_write_b32 a38, v166\n"
      "v_lshlrev_b32 v222, 16, v167\n"
      "v_and_b32 v167, s62, v167\n"
      "v_mfma_f32_16x16x32_bf16 v[140:143], a[80:83], a[16:19], 0\n"
      "v_mul_f32 v222, s61, v222\n"
      "v_mul_f32 v167, s61, v167\n"
      "v_cvt_pk_bf16_f32 v167, v222, v167\n"
      "v_accvgpr_write_b32 a39, v167\n"
      "v_mfma_f32_16x16x32_bf16 v[140:143], a[84:87], a[20:23], v[140:143]\n"
      "v_lshlrev_b32 v221, 16, v168\n"
      "v_and_b32 v168, s62, v168\n"
      "v_mul_f32 v221, s61, v221\n"
      "v_mul_f32 v168, s61, v168\n"
      "v_cvt_pk_bf16_f32 v168, v221, v168\n"
      "s_add_u32 m0, s58, 0x4000\n"
      "v_accvgpr_write_b32 a40, v168\n"
      "v_lshlrev_b32 v222, 16, v169\n"
      "v_and_b32 v169, s62, v169\n"
      "v_mul_f32 v222, s61, v222\n"
      "v_mul_f32 v169, s61, v169\n"
      "s_add_u32 s67, s57, 0x4000\n"
      "v_cvt_pk_bf16_f32 v169, v222, v169\n"
      "v_accvgpr_write_b32 a41, v169\n"
      "v_lshlrev_b32 v221, 16, v170\n"
      "v_and_b32 v170, s62, v170\n"
      "v_mul_f32 v221, s61, v221\n"
      "buffer_load_dwordx4 v212, s[44:47], s67 offen nt lds\n"
      "v_mul_f32 v170, s61, v170\n"
      "v_cvt_pk_bf16_f32 v170, v221, v170\n"
      "v_accvgpr_write_b32 a42, v170\n"
      "v_lshlrev_b32 v222, 16, v171\n"
      "v_and_b32 v171, s62, v171\n"
      "buffer_load_dwordx4 v213, s[44:47], s67 offen offset:1024 nt lds\n"
      "v_mul_f32 v222, s61, v222\n"
      "v_mul_f32 v171, s61, v171\n"
      "v_cvt_pk_bf16_f32 v171, v222, v171\n"
      "v_accvgpr_write_b32 a43, v171\n"
      "v_mfma_f32_16x16x32_bf16 v[140:143], a[88:91], a[24:27], v[140:143]\n"
      "v_lshlrev_b32 v221, 16, v172\n"
      "v_and_b32 v172, s62, v172\n"
      "v_mul_f32 v221, s61, v221\n"
      "v_mul_f32 v172, s61, v172\n"
      "v_cvt_pk_bf16_f32 v172, v221, v172\n"
      "v_mfma_f32_16x16x32_bf16 v[140:143], a[92:95], a[28:31], v[140:143]\n"
      "v_accvgpr_write_b32 a44, v172\n"
      "v_lshlrev_b32 v222, 16, v173\n"
      "v_and_b32 v173, s62, v173\n"
      "v_mul_f32 v222, s61, v222\n"
      "v_mul_f32 v173, s61, v173\n"
      "s_mov_b32 m0, s59\n"
      "v_cvt_pk_bf16_f32 v173, v222, v173\n"
      "v_accvgpr_write_b32 a45, v173\n"
      "v_lshlrev_b32 v221, 16, v174\n"
      "v_and_b32 v174, s62, v174\n"
      "v_mul_f32 v221, s61, v221\n"
      "s_add_u32 s68, s56, 0x1000000\n"
      "v_mul_f32 v174, s61, v174\n"
      "v_cvt_pk_bf16_f32 v174, v221, v174\n"
      "v_accvgpr_write_b32 a46, v174\n"
      "v_lshlrev_b32 v222, 16, v175\n"
      "v_and_b32 v175, s62, v175\n"
      "buffer_load_dwordx4 v212, s[40:43], s68 offen nt lds\n"
      "v_mul_f32 v222, s61, v222\n"
      "v_mul_f32 v175, s61, v175\n"
      "v_cvt_pk_bf16_f32 v175, v222, v175\n"
      "v_accvgpr_write_b32 a47, v175\n"
      "buffer_load_dwordx4 v213, s[40:43], s68 offen offset:1024 nt lds\n"
      "s_waitcnt vmcnt(8)\n"
      "ds_read_b128 v[176:179], v209 offset:20480\n"
      "ds_read_b128 v[180:183], v209 offset:20992\n"
      "ds_read_b128 v[184:187], v209 offset:21504\n"
      "ds_read_b128 v[188:191], v209 offset:22016\n"
      "s_waitcnt lgkmcnt(0)\n"
      "v_lshlrev_b32 v221, 16, v176\n"
      "v_and_b32 v176, s62, v176\n"
      "v_mul_f32 v221, s61, v221\n"
      "v_mul_f32 v176, s61, v176\n"
      "v_cvt_pk_bf16_f32 v176, v221, v176\n"
      "v_mfma_f32_16x16x32_bf16 v[144:147], a[64:67], a[32:35], 0\n"
      "v_accvgpr_write_b32 a48, v176\n"
      "v_lshlrev_b32 v222, 16, v177\n"
      "v_and_b32 v177, s62, v177\n"
      "v_mul_f32 v222, s61, v222\n"
      "v_mul_f32 v177, s61, v177\n"
      "v_mfma_f32_16x16x32_bf16 v[144:147], a[68:71], a[36:39], v[144:147]\n"
      "v_cvt_pk_bf16_f32 v177, v222, v177\n"
      "v_accvgpr_write_b32 a49, v177\n"
      "v_lshlrev_b32 v221, 16, v178\n"
      "v_and_b32 v178, s62, v178\n"
      "v_mul_f32 v221, s61, v221\n"
      "v_mfma_f32_16x16x32_bf16 v[144:147], a[72:75], a[40:43], v[144:147]\n"
      "v_mul_f32 v178, s61, v178\n"
      "v_cvt_pk_bf16_f32 v178, v221, v178\n"
      "v_accvgpr_write_b32 a50, v178\n"
      "v_lshlrev_b32 v222, 16, v179\n"
      "v_and_b32 v179, s62, v179\n"
      "s_add_u32 m0, s59, 0x800\n"
      "v_mul_f32 v222, s61, v222\n"
      "v_mul_f32 v179, s61, v179\n"
      "v_cvt_pk_bf16_f32 v179, v222, v179\n"
      "v_accvgpr_write_b32 a51, v179\n"
      "s_add_u32 s67, s56, 0x1000800\n"
      "v_lshlrev_b32 v221, 16, v180\n"
      "v_and_b32 v180, s62, v180\n"
      "v_mul_f32 v221, s61, v221\n"
      "v_mul_f32 v180, s61, v180\n"
      "v_cvt_pk_bf16_f32 v180, v221, v180\n"
      "buffer_load_dwordx4 v212, s[40:43], s67 offen nt lds\n"
      "v_accvgpr_write_b32 a52, v180\n"
      "v_lshlrev_b32 v222, 16, v181\n"
      "v_and_b32 v181, s62, v181\n"
      "v_mul_f32 v222, s61, v222\n"
      "v_mul_f32 v181, s61, v181\n"
      "buffer_load_dwordx4 v213, s[40:43], s67 offen offset:1024 nt lds\n"
      "v_cvt_pk_bf16_f32 v181, v222, v181\n"
      "v_accvgpr_write_b32 a53, v181\n"
      "v_lshlrev_b32 v221, 16, v182\n"
      "v_and_b32 v182, s62, v182\n"
      "v_mul_f32 v221, s61, v221\n"
      "v_mfma_f32_16x16x32_bf16 v[144:147], a[76:79], a[44:47], v[144:147]\n"
      "v_mul_f32 v182, s61, v182\n"
      "v_cvt_pk_bf16_f32 v182, v221, v182\n"
      "v_accvgpr_write_b32 a54, v182\n"
      "v_lshlrev_b32 v222, 16, v183\n"
      "v_and_b32 v183, s62, v183\n"
      "v_mfma_f32_16x16x32_bf16 v[148:151], a[80:83], a[32:35], 0\n"
      "v_mul_f32 v222, s61, v222\n"
      "v_mul_f32 v183, s61, v183\n"
      "v_cvt_pk_bf16_f32 v183, v222, v183\n"
      "v_accvgpr_write_b32 a55, v183\n"
      "v_mfma_f32_16x16x32_bf16 v[148:151], a[84:87], a[36:39], v[148:151]\n"
      "v_lshlrev_b32 v221, 16, v184\n"
      "v_and_b32 v184, s62, v184\n"
      "v_mul_f32 v221, s61, v221\n"
      "v_mul_f32 v184, s61, v184\n"
      "v_cvt_pk_bf16_f32 v184, v221, v184\n"
      "s_add_u32 m0, s58, 0xa000\n"
      "v_accvgpr_write_b32 a56, v184\n"
      "v_lshlrev_b32 v222, 16, v185\n"
      "v_and_b32 v185, s62, v185\n"
      "v_mul_f32 v222, s61, v222\n"
      "v_mul_f32 v185, s61, v185\n"
      "s_add_u32 s68, s57, 0x2000\n"
      "v_cvt_pk_bf16_f32 v185, v222, v185\n"
      "v_accvgpr_write_b32 a57, v185\n"
      "v_lshlrev_b32 v221, 16, v186\n"
      "v_and_b32 v186, s62, v186\n"
      "v_mul_f32 v221, s61, v221\n"
      "buffer_load_dwordx4 v212, s[48:51], s68 offen nt lds\n"
      "v_mul_f32 v186, s61, v186\n"
      "v_cvt_pk_bf16_f32 v186, v221, v186\n"
      "v_accvgpr_write_b32 a58, v186\n"
      "v_lshlrev_b32 v222, 16, v187\n"
      "v_and_b32 v187, s62, v187\n"
      "buffer_load_dwordx4 v213, s[48:51], s68 offen offset:1024 nt lds\n"
      "v_mul_f32 v222, s61, v222\n"
      "v_mul_f32 v187, s61, v187\n"
      "v_cvt_pk_bf16_f32 v187, v222, v187\n"
      "v_accvgpr_write_b32 a59, v187\n"
      "v_mfma_f32_16x16x32_bf16 v[148:151], a[88:91], a[40:43], v[148:151]\n"
      "v_lshlrev_b32 v221, 16, v188\n"
      "v_and_b32 v188, s62, v188\n"
      "v_mul_f32 v221, s61, v221\n"
      "v_mul_f32 v188, s61, v188\n"
      "v_cvt_pk_bf16_f32 v188, v221, v188\n"
      "v_mfma_f32_16x16x32_bf16 v[148:151], a[92:95], a[44:47], v[148:151]\n"
      "v_accvgpr_write_b32 a60, v188\n"
      "v_lshlrev_b32 v222, 16, v189\n"
      "v_and_b32 v189, s62, v189\n"
      "v_mul_f32 v222, s61, v222\n"
      "v_mul_f32 v189, s61, v189\n"
      "s_add_u32 m0, s58, 0x6000\n"
      "v_cvt_pk_bf16_f32 v189, v222, v189\n"
      "v_accvgpr_write_b32 a61, v189\n"
      "v_lshlrev_b32 v221, 16, v190\n"
      "v_and_b32 v190, s62, v190\n"
      "v_mul_f32 v221, s61, v221\n"
      "s_add_u32 s67, s57, 0x6000\n"
      "v_mul_f32 v190, s61, v190\n"
      "v_cvt_pk_bf16_f32 v190, v221, v190\n"
      "v_accvgpr_write_b32 a62, v190\n"
      "v_lshlrev_b32 v222, 16, v191\n"
      "v_and_b32 v191, s62, v191\n"
      "buffer_load_dwordx4 v212, s[44:47], s67 offen nt lds\n"
      "v_mul_f32 v222, s61, v222\n"
      "v_mul_f32 v191, s61, v191\n"
      "v_cvt_pk_bf16_f32 v191, v222, v191\n"
      "v_accvgpr_write_b32 a63, v191\n"
      "buffer_load_dwordx4 v213, s[44:47], s67 offen offset:1024 nt lds\n"
      "v_max3_f32 v232, v128, v129, v130\n"
      "v_max3_f32 v233, v131, v132, v133\n"
      "v_max3_f32 v232, v232, v134, v135\n"
      "v_mfma_f32_16x16x32_bf16 v[152:155], a[64:67], a[48:51], 0\n"
      "v_max_f32 v232, v232, v233\n"
      "v_max3_f32 v234, v136, v137, v138\n"
      "v_mfma_f32_16x16x32_bf16 v[152:155], a[68:71], a[52:55], v[152:155]\n"
      "v_max3_f32 v235, v139, v140, v141\n"
      "v_max3_f32 v234, v234, v142, v143\n"
      "v_mfma_f32_16x16x32_bf16 v[152:155], a[72:75], a[56:59], v[152:155]\n"
      "v_max_f32 v234, v234, v235\n"
      "v_mov_b32 v233, v232\n"
      "s_add_u32 m0, s58, 0xc000\n"
      "v_mov_b32 v235, v234\n"
      "v_permlane32_swap_b32 v232, v233\n"
      "s_add_u32 s68, s57, 0x4000\n"
      "v_permlane32_swap_b32 v234, v235\n"
      "v_max_f32 v232, v232, v233\n"
      "v_max_f32 v234, v234, v235\n"
      "buffer_load_dwordx4 v212, s[48:51], s68 offen nt lds\n"
      "v_mov_b32 v233, v232\n"
      "v_mov_b32 v235, v234\n"
      "buffer_load_dwordx4 v213, s[48:51], s68 offen offset:1024 nt lds\n"
      "v_permlane16_swap_b32 v232, v233\n"
      "v_permlane16_swap_b32 v234, v235\n"
      "v_mfma_f32_16x16x32_bf16 v[152:155], a[76:79], a[60:63], v[152:155]\n"
      "v_max_f32 v232, v232, v233\n"
      "v_max_f32 v234, v234, v235\n"
      "v_mfma_f32_16x16x32_bf16 v[156:159], a[80:83], a[48:51], 0\n"
      "v_sub_f32 v128, v128, v232\n"
      "v_sub_f32 v129, v129, v232\n"
      "v_mfma_f32_16x16x32_bf16 v[156:159], a[84:87], a[52:55], v[156:159]\n"
      "v_sub_f32 v130, v130, v232\n"
      "v_sub_f32 v131, v131, v232\n"
      "v_sub_f32 v132, v132, v232\n"
      "s_add_u32 m0, s59, 0x1000\n"
      "v_sub_f32 v133, v133, v232\n"
      "v_sub_f32 v134, v134, v232\n"
      "s_add_u32 s67, s56, 0x1001000\n"
      "v_sub_f32 v135, v135, v232\n"
      "v_mul_f32 v192, -1.0, v232\n"
      "buffer_load_dwordx4 v212, s[40:43], s67 offen nt lds\n"
      "v_mul_f32 v193, -1.0, v232\n"
      "v_mul_f32 v194, -1.0, v232\n"
      "buffer_load_dwordx4 v213, s[40:43], s67 offen offset:1024 nt lds\n"
      "v_mul_f32 v195, -1.0, v232\n"
      "v_sub_f32 v136, v136, v234\n"
      "v_mfma_f32_16x16x32_bf16 v[156:159], a[88:91], a[56:59], v[156:159]\n"
      "v_sub_f32 v137, v137, v234\n"
      "v_sub_f32 v138, v138, v234\n"
      "v_sub_f32 v139, v139, v234\n"
      "v_mfma_f32_16x16x32_bf16 v[156:159], a[92:95], a[60:63], v[156:159]\n"
      "v_sub_f32 v140, v140, v234\n"
      "v_sub_f32 v141, v141, v234\n"
      "s_add_u32 m0, s59, 0x1800\n"
      "v_sub_f32 v142, v142, v234\n"
      "v_sub_f32 v143, v143, v234\n"
      "s_add_u32 s68, s56, 0x1001800\n"
      "v_mul_f32 v196, -1.0, v234\n"
      "v_mul_f32 v197, -1.0, v234\n"
      "buffer_load_dwordx4 v212, s[40:43], s68 offen nt lds\n"
      "v_mul_f32 v198, -1.0, v234\n"
      "v_mul_f32 v199, -1.0, v234\n"
      "buffer_load_dwordx4 v213, s[40:43], s68 offen offset:1024 nt lds\n"
      "s_waitcnt vmcnt(18)\n"
      "s_barrier\n"
      "s_mov_b32 m0, s58\n"
      "s_add_u32 s67, s57, 0x8000\n"
      "buffer_load_dwordx4 v212, s[44:47], s67 offen nt lds\n"
      "buffer_load_dwordx4 v213, s[44:47], s67 offen offset:1024 nt lds\n"
      "ds_read_b128 a[64:67], v208 offset:8192\n"
      "ds_read_b128 a[68:71], v208 offset:8704\n"
      "ds_read_b128 a[72:75], v208 offset:9216\n"
      "ds_read_b128 a[76:79], v208 offset:9728\n"
      "ds_read_b128 a[80:83], v208 offset:12288\n"
      "ds_read_b128 a[84:87], v208 offset:12800\n"
      "ds_read_b128 a[88:91], v208 offset:13312\n"
      "ds_read_b128 a[92:95], v208 offset:13824\n"
      "s_waitcnt vmcnt(12)\n"
      "ds_read_b128 v[224:227], v209 offset:0\n"
      "ds_read_b128 v[228:231], v209 offset:512\n"
      "v_max3_f32 v236, v144, v145, v146\n"
      "v_max3_f32 v237, v147, v148, v149\n"
      "v_max3_f32 v236, v236, v150, v151\n"
      "v_max_f32 v236, v236, v237\n"
      "v_max3_f32 v238, v152, v153, v154\n"
      "v_max3_f32 v239, v155, v156, v157\n"
      "v_max3_f32 v238, v238, v158, v159\n"
      "v_max_f32 v238, v238, v239\n"
      "v_mov_b32 v237, v236\n"
      "v_mov_b32 v239, v238\n"
      "s_nop 0\n"
      "v_permlane32_swap_b32 v236, v237\n"
      "v_permlane32_swap_b32 v238, v239\n"
      "v_max_f32 v236, v236, v237\n"
      "v_max_f32 v238, v238, v239\n"
      "v_mov_b32 v237, v236\n"
      "v_mov_b32 v239, v238\n"
      "s_nop 0\n"
      "v_permlane16_swap_b32 v236, v237\n"
      "v_permlane16_swap_b32 v238, v239\n"
      "v_max_f32 v236, v236, v237\n"
      "v_max_f32 v238, v238, v239\n"
      "v_sub_f32 v144, v144, v236\n"
      "v_sub_f32 v145, v145, v236\n"
      "v_sub_f32 v146, v146, v236\n"
      "v_sub_f32 v147, v147, v236\n"
      "v_sub_f32 v148, v148, v236\n"
      "v_sub_f32 v149, v149, v236\n"
      "v_sub_f32 v150, v150, v236\n"
      "v_sub_f32 v151, v151, v236\n"
      "v_mul_f32 v200, -1.0, v236\n"
      "v_mul_f32 v201, -1.0, v236\n"
      "v_mul_f32 v202, -1.0, v236\n"
      "v_mul_f32 v203, -1.0, v236\n"
      "v_sub_f32 v152, v152, v238\n"
      "v_sub_f32 v153, v153, v238\n"
      "v_sub_f32 v154, v154, v238\n"
      "v_sub_f32 v155, v155, v238\n"
      "v_sub_f32 v156, v156, v238\n"
      "v_sub_f32 v157, v157, v238\n"
      "v_sub_f32 v158, v158, v238\n"
      "v_sub_f32 v159, v159, v238\n"
      "v_mul_f32 v204, -1.0, v238\n"
      "v_mul_f32 v205, -1.0, v238\n"
      "v_mul_f32 v206, -1.0, v238\n"
      "v_mul_f32 v207, -1.0, v238\n"
      "v_exp_f32 v128, v128\n"
      "v_exp_f32 v129, v129\n"
      "v_exp_f32 v130, v130\n"
      "v_exp_f32 v131, v131\n"
      "v_exp_f32 v132, v132\n"
      "v_exp_f32 v133, v133\n"
      "v_exp_f32 v134, v134\n"
      "v_exp_f32 v135, v135\n"
      "v_exp_f32 v136, v136\n"
      "v_exp_f32 v137, v137\n"
      "v_exp_f32 v138, v138\n"
      "v_exp_f32 v139, v139\n"
      "v_exp_f32 v140, v140\n"
      "v_exp_f32 v141, v141\n"
      "v_exp_f32 v142, v142\n"
      "v_exp_f32 v143, v143\n"
      "v_exp_f32 v144, v144\n"
      "v_exp_f32 v145, v145\n"
      "v_exp_f32 v146, v146\n"
      "v_exp_f32 v147, v147\n"
      "v_exp_f32 v148, v148\n"
      "v_exp_f32 v149, v149\n"
      "v_exp_f32 v150, v150\n"
      "v_exp_f32 v151, v151\n"
      "v_exp_f32 v152, v152\n"
      "v_exp_f32 v153, v153\n"
      "v_exp_f32 v154, v154\n"
      "v_cvt_pk_bf16_f32 v128, v128, v129\n"
      "v_cvt_pk_bf16_f32 v129, v130, v131\n"
      "v_cvt_pk_bf16_f32 v130, v132, v133\n"
      "v_cvt_pk_bf16_f32 v131, v134, v135\n"
      "v_cvt_pk_bf16_f32 v136, v136, v137\n"
      "v_cvt_pk_bf16_f32 v137, v138, v139\n"
      "v_cvt_pk_bf16_f32 v138, v140, v141\n"
      "v_cvt_pk_bf16_f32 v139, v142, v143\n"
      "v_cvt_pk_bf16_f32 v144, v144, v145\n"
      "v_cvt_pk_bf16_f32 v145, v146, v147\n"
      "v_cvt_pk_bf16_f32 v146, v148, v149\n"
      "v_cvt_pk_bf16_f32 v147, v150, v151\n"
      "v_cvt_pk_bf16_f32 v152, v152, v153\n"
      "s_waitcnt lgkmcnt(0)\n"
      "v_lshlrev_b32 v221, 16, v224\n"
      "v_and_b32 v224, s62, v224\n"
      "v_mul_f32 v221, s61, v221\n"
      "v_mul_f32 v224, s61, v224\n"
      "v_cvt_pk_bf16_f32 v224, v221, v224\n"
      "v_accvgpr_write_b32 a160, v224\n"
      "v_lshlrev_b32 v222, 16, v225\n"
      "v_and_b32 v225, s62, v225\n"
      "v_mul_f32 v222, s61, v222\n"
      "v_mul_f32 v225, s61, v225\n"
      "v_cvt_pk_bf16_f32 v225, v222, v225\n"
      "v_accvgpr_write_b32 a161, v225\n"
      "v_lshlrev_b32 v221, 16, v226\n"
      "v_and_b32 v226, s62, v226\n"
      "v_mul_f32 v221, s61, v221\n"
      "v_mul_f32 v226, s61, v226\n"
      "v_cvt_pk_bf16_f32 v226, v221, v226\n"
      "v_accvgpr_write_b32 a162, v226\n"
      "v_lshlrev_b32 v222, 16, v227\n"
      "v_and_b32 v227, s62, v227\n"
      "v_mul_f32 v222, s61, v222\n"
      "v_mul_f32 v227, s61, v227\n"
      "v_cvt_pk_bf16_f32 v227, v222, v227\n"
      "v_accvgpr_write_b32 a163, v227\n"
      "s_mov_b32 s86, 0\n"
      "s_mov_b32 s87, 168\n"
      "s_memrealtime s[84:85]\n"
      "L_HEAD:\n"
      "s_waitcnt lgkmcnt(0)\n"
      "s_sub_u32 s85, s84, s86\n"
      "s_cmp_ge_u32 s85, s87\n"
      "s_cbranch_scc1 L_PACED0_0\n"
      "L_PACE0_0:\n"
      "s_memrealtime s[84:85]\n"
      "s_waitcnt lgkmcnt(0)\n"
      "s_sub_u32 s85, s84, s86\n"
      "s_cmp_lt_u32 s85, s87\n"
      "s_cbranch_scc1 L_PACE0_0\n"
      "L_PACED0_0:\n"
      "s_mov_b32 s86, s84\n"
      "v_mfma_f32_16x16x32_bf16 v[160:163], a[64:67], a[0:3], v[192:195]\n"
      "s_waitcnt vmcnt(14)\n"
      "s_barrier\n"
      "v_lshlrev_b32 v221, 16, v228\n"
      "v_and_b32 v228, s62, v228\n"
      "v_mfma_f32_16x16x32_bf16 v[160:163], a[68:71], a[4:7], v[160:163]\n"
      "v_exp_f32 v155, v155\n"
      "v_mfma_f32_16x16x32_bf16 v[160:163], a[72:75], a[8:11], v[160:163]\n"
      "ds_read_b128 a[148:151], v219 offset:8192\n"
      "v_cvt_pk_bf16_f32 v153, v154, v155\n"
      "v_mfma_f32_16x16x32_bf16 v[160:163], a[76:79], a[12:15], v[160:163]\n"
      "v_exp_f32 v156, v156\n"
      "v_mfma_f32_16x16x32_bf16 v[164:167], a[80:83], a[0:3], v[192:195]\n"
      "ds_read_b64_tr_b16 a[96:97], v210 offset:32768\n"
      "ds_read_b64_tr_b16 a[98:99], v210 offset:36864\n"
      "v_mfma_f32_16x16x32_bf16 v[164:167], a[84:87], a[4:7], v[164:167]\n"
      "v_exp_f32 v157, v157\n"
      "v_mfma_f32_16x16x32_bf16 v[164:167], a[88:91], a[8:11], v[164:167]\n"
      "ds_read_b64_tr_b16 a[100:101], v211 offset:32768\n"
      "v_cvt_pk_bf16_f32 v154, v156, v157\n"
      "v_mfma_f32_16x16x32_bf16 v[164:167], a[92:95], a[12:15], v[164:167]\n"
      "v_exp_f32 v158, v158\n"
      "v_mfma_f32_16x16x32_bf16 v[168:171], a[64:67], a[16:19], v[196:199]\n"
      "ds_read_b64_tr_b16 a[102:103], v211 offset:36864\n"
      "ds_read_b64_tr_b16 a[104:105], v210 offset:33280\n"
      "v_mfma_f32_16x16x32_bf16 v[168:171], a[68:71], a[20:23], v[168:171]\n"
      "v_exp_f32 v159, v159\n"
      "v_mfma_f32_16x16x32_bf16 v[168:171], a[72:75], a[24:27], v[168:171]\n"
      "ds_read_b64_tr_b16 a[106:107], v210 offset:37376\n"
      "v_cvt_pk_bf16_f32 v155, v158, v159\n"
      "v_mfma_f32_16x16x32_bf16 v[168:171], a[76:79], a[28:31], v[168:171]\n"
      "v_exp_f32 v160, v160\n"
      "v_mfma_f32_16x16x32_bf16 v[172:175], a[80:83], a[16:19], v[196:199]\n"
      "ds_read_b64_tr_b16 a[108:109], v211 offset:33280\n"
      "ds_read_b64_tr_b16 a[110:111], v211 offset:37376\n"
      "v_mfma_f32_16x16x32_bf16 v[172:175], a[84:87], a[20:23], v[172:175]\n"
      "v_exp_f32 v161, v161\n"
      "v_mfma_f32_16x16x32_bf16 v[172:175], a[88:91], a[24:27], v[172:175]\n"
      "ds_read_b64_tr_b16 a[112:113], v210 offset:33792\n"
      "v_cvt_pk_bf16_f32 v160, v160, v161\n"
      "v_mfma_f32_16x16x32_bf16 v[172:175], a[92:95], a[28:31], v[172:175]\n"
      "v_exp_f32 v162, v162\n"
      "v_mfma_f32_16x16x32_bf16 v[176:179], a[64:67], a[32:35], v[200:203]\n"
      "ds_read_b64_tr_b16 a[114:115], v210 offset:37888\n"
      "ds_read_b64_tr_b16 a[116:117], v211 offset:33792\n"
      "v_mfma_f32_16x16x32_bf16 v[176:179], a[68:71], a[36:39], v[176:179]\n"
      "v_exp_f32 v163, v163\n"
      "v_mfma_f32_16x16x32_bf16 v[176:179], a[72:75], a[40:43], v[176:179]\n"
      "ds_read_b64_tr_b16 a[118:119], v211 offset:37888\n"
      "v_cvt_pk_bf16_f32 v161, v162, v163\n"
      "v_mfma_f32_16x16x32_bf16 v[176:179], a[76:79], a[44:47], v[176:179]\n"
      "v_exp_f32 v164, v164\n"
      "v_mfma_f32_16x16x32_bf16 v[180:183], a[80:83], a[32:35], v[200:203]\n"
      "ds_read_b64_tr_b16 a[120:121], v210 offset:34304\n"
      "ds_read_b64_tr_b16 a[122:123], v210 offset:38400\n"
      "v_mfma_f32_16x16x32_bf16 v[180:183], a[84:87], a[36:39], v[180:183]\n"
      "v_exp_f32 v165, v165\n"
      "v_mfma_f32_16x16x32_bf16 v[180:183], a[88:91], a[40:43], v[180:183]\n"
      "ds_read_b64_tr_b16 a[124:125], v211 offset:34304\n"
      "v_cvt_pk_bf16_f32 v162, v164, v165\n"
      "v_mfma_f32_16x16x32_bf16 v[180:183], a[92:95], a[44:47], v[180:183]\n"
      "v_exp_f32 v166, v166\n"
      "v_mfma_f32_16x16x32_bf16 v[184:187], a[64:67], a[48:51], v[204:207]\n"
      "ds_read_b64_tr_b16 a[126:127], v211 offset:38400\n"
      "v_mul_f32 v221, s61, v221\n"
      "v_mfma_f32_16x16x32_bf16 v[184:187], a[68:71], a[52:55], v[184:187]\n"
      "v_exp_f32 v167, v167\n"
      "v_mfma_f32_16x16x32_bf16 v[184:187], a[72:75], a[56:59], v[184:187]\n"
      "v_cvt_pk_bf16_f32 v163, v166, v167\n"
      "v_mul_f32 v228, s61, v228\n"
      "v_mfma_f32_16x16x32_bf16 v[184:187], a[76:79], a[60:63], v[184:187]\n"
      "v_exp_f32 v168, v168\n"
      "v_mfma_f32_16x16x32_bf16 v[188:191], a[80:83], a[48:51], v[204:207]\n"
      "ds_read_b128 a[64:67], v208 offset:16384\n"
      "ds_read_b128 a[68:71], v208 offset:16896\n"
      "v_mfma_f32_16x16x32_bf16 v[188:191], a[84:87], a[52:55], v[188:191]\n"
      "v_exp_f32 v169, v169\n"
      "v_mfma_f32_16x16x32_bf16 v[188:191], a[88:91], a[56:59], v[188:191]\n"
      "ds_read_b128 a[72:75], v208 offset:17408\n"
      "v_cvt_pk_bf16_f32 v168, v168, v169\n"
      "v_mfma_f32_16x16x32_bf16 v[188:191], a[92:95], a[60:63], v[188:191]\n"
      "v_exp_f32 v170, v170\n"
      "v_mfma_f32_16x16x32_bf16 a[128:131], a[144:147], v[128:131], 0\n"
      "ds_read_b128 a[76:79], v208 offset:17920\n"
      "v_cvt_pk_bf16_f32 v228, v221, v228\n"
      "s_waitcnt lgkmcnt(12)\n"
      "v_mfma_f32_16x16x32_bf16 v[0:3], a[96:99], v[128:131], 0\n"
      "v_exp_f32 v171, v171\n"
      "v_mfma_f32_16x16x32_bf16 v[4:7], a[100:103], v[128:131], 0\n"
      "ds_read_b128 a[80:83], v208 offset:20480\n"
      "v_cvt_pk_bf16_f32 v169, v170, v171\n"
      "v_mfma_f32_16x16x32_bf16 v[8:11], a[104:107], v[128:131], 0\n"
      "v_exp_f32 v172, v172\n"
      "v_mfma_f32_16x16x32_bf16 v[12:15], a[108:111], v[128:131], 0\n"
      "ds_read_b128 a[84:87], v208 offset:20992\n"
      "ds_read_b128 a[88:91], v208 offset:21504\n"
      "s_waitcnt lgkmcnt(7)\n"
      "v_mfma_f32_16x16x32_bf16 v[16:19], a[112:115], v[128:131], 0\n"
      "v_exp_f32 v173, v173\n"
      "v_mfma_f32_16x16x32_bf16 v[20:23], a[116:119], v[128:131], 0\n"
      "ds_read_b128 a[92:95], v208 offset:22016\n"
      "v_cvt_pk_bf16_f32 v170, v172, v173\n"
      "v_mfma_f32_16x16x32_bf16 v[24:27], a[120:123], v[128:131], 0\n"
      "v_exp_f32 v174, v174\n"
      "v_mfma_f32_16x16x32_bf16 v[28:31], a[124:127], v[128:131], 0\n"
      "buffer_store_dwordx4 a[148:151], v214, s[52:55], s76 offen offset:0 sc1\n"
      "v_mfma_f32_16x16x32_bf16 a[132:135], a[144:147], v[136:139], 0\n"
      "v_exp_f32 v175, v175\n"
      "v_mfma_f32_16x16x32_bf16 v[32:35], a[96:99], v[136:139], 0\n"
      "s_add_u32 m0, s58, 0x2000\n"
      "v_cvt_pk_bf16_f32 v171, v174, v175\n"
      "v_accvgpr_write_b32 a164, v228\n"
      "v_mfma_f32_16x16x32_bf16 v[36:39], a[100:103], v[136:139], 0\n"
      "s_add_u32 s68, s57, 0xa000\n"
      "v_exp_f32 v176, v176\n"
      "v_mfma_f32_16x16x32_bf16 v[40:43], a[104:107], v[136:139], 0\n"
      "buffer_load_dwordx4 v212, s[44:47], s68 offen nt lds\n"
      "v_lshlrev_b32 v222, 16, v229\n"
      "v_mfma_f32_16x16x32_bf16 v[44:47], a[108:111], v[136:139], 0\n"
      "v_exp_f32 v177, v177\n"
      "v_mfma_f32_16x16x32_bf16 v[48:51], a[112:115], v[136:139], 0\n"
      "buffer_load_dwordx4 v213, s[44:47], s68 offen offset:1024 nt lds\n"
      "v_cvt_pk_bf16_f32 v176, v176, v177\n"
      "v_mfma_f32_16x16x32_bf16 v[52:55], a[116:119], v[136:139], 0\n"
      "v_exp_f32 v178, v178\n"
      "v_mfma_f32_16x16x32_bf16 v[56:59], a[120:123], v[136:139], 0\n"
      "v_and_b32 v229, s62, v229\n"
      "v_mul_f32 v222, s61, v222\n"
      "v_mfma_f32_16x16x32_bf16 v[60:63], a[124:127], v[136:139], 0\n"
      "v_exp_f32 v179, v179\n"
      "v_mfma_f32_16x16x32_bf16 a[136:139], a[144:147], v[144:147], 0\n"
      "v_cvt_pk_bf16_f32 v177, v178, v179\n"
      "v_mul_f32 v229, s61, v229\n"
      "v_mfma_f32_16x16x32_bf16 v[64:67], a[96:99], v[144:147], 0\n"
      "v_exp_f32 v180, v180\n"
      "v_mfma_f32_16x16x32_bf16 v[68:71], a[100:103], v[144:147], 0\n"
      "v_cvt_pk_bf16_f32 v229, v222, v229\n"
      "v_accvgpr_write_b32 a165, v229\n"
      "v_mfma_f32_16x16x32_bf16 v[72:75], a[104:107], v[144:147], 0\n"
      "v_exp_f32 v181, v181\n"
      "v_mfma_f32_16x16x32_bf16 v[76:79], a[108:111], v[144:147], 0\n"
      "v_cvt_pk_bf16_f32 v178, v180, v181\n"
      "v_lshlrev_b32 v221, 16, v230\n"
      "v_mfma_f32_16x16x32_bf16 v[80:83], a[112:115], v[144:147], 0\n"
      "v_exp_f32 v182, v182\n"
      "v_mfma_f32_16x16x32_bf16 v[84:87], a[116:119], v[144:147], 0\n"
      "s_add_u32 m0, s58, 0xe000\n"
      "ds_read_b128 v[224:227], v209 offset:1024\n"
      "v_and_b32 v230, s62, v230\n"
      "v_mfma_f32_16x16x32_bf16 v[88:91], a[120:123], v[144:147], 0\n"
      "s_add_u32 s67, s57, 0x6000\n"
      "v_exp_f32 v183, v183\n"
      "v_mfma_f32_16x16x32_bf16 v[92:95], a[124:127], v[144:147], 0\n"
      "buffer_load_dwordx4 v212, s[48:51], s67 offen nt lds\n"
      "v_cvt_pk_bf16_f32 v179, v182, v183\n"
      "v_mfma_f32_16x16x32_bf16 a[140:143], a[144:147], v[152:155], 0\n"
      "v_exp_f32 v184, v184\n"
      "v_mfma_f32_16x16x32_bf16 v[96:99], a[96:99], v[152:155], 0\n"
      "buffer_load_dwordx4 v213, s[48:51], s67 offen offset:1024 nt lds\n"
      "v_mul_f32 v221, s61, v221\n"
      "v_mfma_f32_16x16x32_bf16 v[100:103], a[100:103], v[152:155], 0\n"
      "v_exp_f32 v185, v185\n"
      "v_mfma_f32_16x16x32_bf16 v[104:107], a[104:107], v[152:155], 0\n"
      "v_cvt_pk_bf16_f32 v184, v184, v185\n"
      "v_mul_f32 v230, s61, v230\n"
      "v_mfma_f32_16x16x32_bf16 v[108:111], a[108:111], v[152:155], 0\n"
      "v_exp_f32 v186, v186\n"
      "v_mfma_f32_16x16x32_bf16 v[112:115], a[112:115], v[152:155], 0\n"
      "v_cvt_pk_bf16_f32 v230, v221, v230\n"
      "v_accvgpr_write_b32 a166, v230\n"
      "v_mfma_f32_16x16x32_bf16 v[116:119], a[116:119], v[152:155], 0\n"
      "v_lshlrev_b32 v222, 16, v231\n"
      "v_and_b32 v231, s62, v231\n"
      "v_mfma_f32_16x16x32_bf16 v[120:123], a[120:123], v[152:155], 0\n"
      "v_mul_f32 v222, s61, v222\n"
      "v_mul_f32 v231, s61, v231\n"
      "v_mfma_f32_16x16x32_bf16 v[124:127], a[124:127], v[152:155], 0\n"
      "v_cvt_pk_bf16_f32 v231, v222, v231\n"
      "v_accvgpr_write_b32 a167, v231\n"
      "s_waitcnt lgkmcnt(0)\n"
      "v_mfma_f32_16x16x32_bf16 v[128:131], a[64:67], a[0:3], v[192:195]\n"
      "s_waitcnt vmcnt(13)\n"
      "s_barrier\n"
      "v_lshlrev_b32 v221, 16, v224\n"
      "v_and_b32 v224, s62, v224\n"
      "v_mfma_f32_16x16x32_bf16 v[128:131], a[68:71], a[4:7], v[128:131]\n"
      "v_exp_f32 v187, v187\n"
      "v_mfma_f32_16x16x32_bf16 v[128:131], a[72:75], a[8:11], v[128:131]\n"
      "ds_read_b128 a[152:155], v220 offset:9216\n"
      "v_cvt_pk_bf16_f32 v185, v186, v187\n"
      "v_mfma_f32_16x16x32_bf16 v[128:131], a[76:79], a[12:15], v[128:131]\n"
      "v_exp_f32 v188, v188\n"
      "v_mfma_f32_16x16x32_bf16 v[132:135], a[80:83], a[0:3], v[192:195]\n"
      "ds_read_b64_tr_b16 a[96:97], v210 offset:40960\n"
      "ds_read_b64_tr_b16 a[98:99], v210 offset:45056\n"
      "v_mfma_f32_16x16x32_bf16 v[132:135], a[84:87], a[4:7], v[132:135]\n"
      "v_exp_f32 v189, v189\n"
      "v_mfma_f32_16x16x32_bf16 v[132:135], a[88:91], a[8:11], v[132:135]\n"
      "ds_read_b64_tr_b16 a[100:101], v211 offset:40960\n"
      "v_cvt_pk_bf16_f32 v186, v188, v189\n"
      "v_mfma_f32_16x16x32_bf16 v[132:135], a[92:95], a[12:15], v[132:135]\n"
      "v_exp_f32 v190, v190\n"
      "v_mfma_f32_16x16x32_bf16 v[136:139], a[64:67], a[16:19], v[196:199]\n"
      "ds_read_b64_tr_b16 a[102:103], v211 offset:45056\n"
      "ds_read_b64_tr_b16 a[104:105], v210 offset:41472\n"
      "v_mfma_f32_16x16x32_bf16 v[136:139], a[68:71], a[20:23], v[136:139]\n"
      "v_exp_f32 v191, v191\n"
      "v_mfma_f32_16x16x32_bf16 v[136:139], a[72:75], a[24:27], v[136:139]\n"
      "ds_read_b64_tr_b16 a[106:107], v210 offset:45568\n"
      "v_cvt_pk_bf16_f32 v187, v190, v191\n"
      "v_mfma_f32_16x16x32_bf16 v[136:139], a[76:79], a[28:31], v[136:139]\n"
      "v_exp_f32 v128, v128\n"
      "v_mfma_f32_16x16x32_bf16 v[140:143], a[80:83], a[16:19], v[196:199]\n"
      "ds_read_b64_tr_b16 a[108:109], v211 offset:41472\n"
      "ds_read_b64_tr_b16 a[110:111], v211 offset:45568\n"
      "v_mfma_f32_16x16x32_bf16 v[140:143], a[84:87], a[20:23], v[140:143]\n"
      "v_exp_f32 v129, v129\n"
      "v_mfma_f32_16x16x32_bf16 v[140:143], a[88:91], a[24:27], v[140:143]\n"
      "ds_read_b64_tr_b16 a[112:113], v210 offset:41984\n"
      "v_cvt_pk_bf16_f32 v128, v128, v129\n"
      "v_mfma_f32_16x16x32_bf16 v[140:143], a[92:95], a[28:31], v[140:143]\n"
      "v_exp_f32 v130, v130\n"
      "v_mfma_f32_16x16x32_bf16 v[144:147], a[64:67], a[32:35], v[200:203]\n"
      "ds_read_b64_tr_b16 a[114:115], v210 offset:46080\n"
      "ds_read_b64_tr_b16 a[116:117], v211 offset:41984\n"
      "v_mfma_f32_16x16x32_bf16 v[144:147], a[68:71], a[36:39], v[144:147]\n"
      "v_exp_f32 v131, v131\n"
      "v_mfma_f32_16x16x32_bf16 v[144:147], a[72:75], a[40:43], v[144:147]\n"
      "ds_read_b64_tr_b16 a[118:119], v211 offset:46080\n"
      "v_cvt_pk_bf16_f32 v129, v130, v131\n"
      "v_mfma_f32_16x16x32_bf16 v[144:147], a[76:79], a[44:47], v[144:147]\n"
      "v_exp_f32 v132, v132\n"
      "v_mfma_f32_16x16x32_bf16 v[148:151], a[80:83], a[32:35], v[200:203]\n"
      "ds_read_b64_tr_b16 a[120:121], v210 offset:42496\n"
      "ds_read_b64_tr_b16 a[122:123], v210 offset:46592\n"
      "v_mfma_f32_16x16x32_bf16 v[148:151], a[84:87], a[36:39], v[148:151]\n"
      "v_exp_f32 v133, v133\n"
      "v_mfma_f32_16x16x32_bf16 v[148:151], a[88:91], a[40:43], v[148:151]\n"
      "ds_read_b64_tr_b16 a[124:125], v211 offset:42496\n"
      "v_cvt_pk_bf16_f32 v130, v132, v133\n"
      "v_mfma_f32_16x16x32_bf16 v[148:151], a[92:95], a[44:47], v[148:151]\n"
      "v_exp_f32 v134, v134\n"
      "v_mfma_f32_16x16x32_bf16 v[152:155], a[64:67], a[48:51], v[204:207]\n"
      "ds_read_b64_tr_b16 a[126:127], v211 offset:46592\n"
      "v_mul_f32 v221, s61, v221\n"
      "v_mfma_f32_16x16x32_bf16 v[152:155], a[68:71], a[52:55], v[152:155]\n"
      "v_exp_f32 v135, v135\n"
      "v_mfma_f32_16x16x32_bf16 v[152:155], a[72:75], a[56:59], v[152:155]\n"
      "v_cvt_pk_bf16_f32 v131, v134, v135\n"
      "v_mul_f32 v224, s61, v224\n"
      "v_mfma_f32_16x16x32_bf16 v[152:155], a[76:79], a[60:63], v[152:155]\n"
      "v_exp_f32 v136, v136\n"
      "v_mfma_f32_16x16x32_bf16 v[156:159], a[80:83], a[48:51], v[204:207]\n"
      "ds_read_b128 a[64:67], v208 offset:24576\n"
      "ds_read_b128 a[68:71], v208 offset:25088\n"
      "v_mfma_f32_16x16x32_bf16 v[156:159], a[84:87], a[52:55], v[156:159]\n"
      "v_exp_f32 v137, v137\n"
      "v_mfma_f32_16x16x32_bf16 v[156:159], a[88:91], a[56:59], v[156:159]\n"
      "ds_read_b128 a[72:75], v208 offset:25600\n"
      "v_cvt_pk_bf16_f32 v136, v136, v137\n"
      "v_mfma_f32_16x16x32_bf16 v[156:159], a[92:95], a[60:63], v[156:159]\n"
      "v_exp_f32 v138, v138\n"
      "v_mfma_f32_16x16x32_bf16 a[128:131], a[144:147], v[160:163], a[128:131]\n"
      "ds_read_b128 a[76:79], v208 offset:26112\n"
      "v_cvt_pk_bf16_f32 v224, v221, v224\n"
      "s_waitcnt lgkmcnt(12)\n"
      "v_mfma_f32_16x16x32_bf16 v[0:3], a[96:99], v[160:163], v[0:3]\n"
      "v_exp_f32 v139, v139\n"
      "v_mfma_f32_16x16x32_bf16 v[4:7], a[100:103], v[160:163], v[4:7]\n"
      "ds_read_b128 a[80:83], v208 offset:28672\n"
      "v_cvt_pk_bf16_f32 v137, v138, v139\n"
      "v_mfma_f32_16x16x32_bf16 v[8:11], a[104:107], v[160:163], v[8:11]\n"
      "v_exp_f32 v140, v140\n"
      "v_mfma_f32_16x16x32_bf16 v[12:15], a[108:111], v[160:163], v[12:15]\n"
      "ds_read_b128 a[84:87], v208 offset:29184\n"
      "ds_read_b128 a[88:91], v208 offset:29696\n"
      "s_waitcnt lgkmcnt(7)\n"
      "v_mfma_f32_16x16x32_bf16 v[16:19], a[112:115], v[160:163], v[16:19]\n"
      "v_exp_f32 v141, v141\n"
      "v_mfma_f32_16x16x32_bf16 v[20:23], a[116:119], v[160:163], v[20:23]\n"
      "ds_read_b128 a[92:95], v208 offset:30208\n"
      "v_cvt_pk_bf16_f32 v138, v140, v141\n"
      "v_mfma_f32_16x16x32_bf16 v[24:27], a[120:123], v[160:163], v[24:27]\n"
      "v_exp_f32 v142, v142\n"
      "v_mfma_f32_16x16x32_bf16 v[28:31], a[124:127], v[160:163], v[28:31]\n"
      "buffer_store_dwordx4 a[152:155], v214, s[52:55], s76 offen offset:1024 sc1\n"
      "v_mfma_f32_16x16x32_bf16 a[132:135], a[144:147], v[168:171], a[132:135]\n"
      "v_exp_f32 v143, v143\n"
      "v_mfma_f32_16x16x32_bf16 v[32:35], a[96:99], v[168:171], v[32:35]\n"
      "s_add_u32 m0, s58, 0x4000\n"
      "v_cvt_pk_bf16_f32 v139, v142, v143\n"
      "v_accvgpr_write_b32 a168, v224\n"
      "v_mfma_f32_16x16x32_bf16 v[36:39], a[100:103], v[168:171], v[36:39]\n"
      "s_add_u32 s68, s57, 0xc000\n"
      "v_exp_f32 v144, v144\n"
      "v_mfma_f32_16x16x32_bf16 v[40:43], a[104:107], v[168:171], v[40:43]\n"
      "buffer_load_dwordx4 v212, s[44:47], s68 offen nt lds\n"
      "v_lshlrev_b32 v222, 16, v225\n"
      "v_mfma_f32_16x16x32_bf16 v[44:47], a[108:111], v[168:171], v[44:47]\n"
      "v_exp_f32 v145, v145\n"
      "v_mfma_f32_16x16x32_bf16 v[48:51], a[112:115], v[168:171], v[48:51]\n"
      "buffer_load_dwordx4 v213, s[44:47], s68 offen offset:1024 nt lds\n"
      "v_cvt_pk_bf16_f32 v144, v144, v145\n"
      "v_mfma_f32_16x16x32_bf16 v[52:55], a[116:119], v[168:171], v[52:55]\n"
      "v_exp_f32 v146, v146\n"
      "v_mfma_f32_16x16x32_bf16 v[56:59], a[120:123], v[168:171], v[56:59]\n"
      "v_and_b32 v225, s62, v225\n"
      "v_mul_f32 v222, s61, v222\n"
      "v_mfma_f32_16x16x32_bf16 v[60:63], a[124:127], v[168:171], v[60:63]\n"
      "v_exp_f32 v147, v147\n"
      "v_mfma_f32_16x16x32_bf16 a[136:139], a[144:147], v[176:179], a[136:139]\n"
      "v_cvt_pk_bf16_f32 v145, v146, v147\n"
      "v_mul_f32 v225, s61, v225\n"
      "v_mfma_f32_16x16x32_bf16 v[64:67], a[96:99], v[176:179], v[64:67]\n"
      "v_exp_f32 v148, v148\n"
      "v_mfma_f32_16x16x32_bf16 v[68:71], a[100:103], v[176:179], v[68:71]\n"
      "v_cvt_pk_bf16_f32 v225, v222, v225\n"
      "v_accvgpr_write_b32 a169, v225\n"
      "v_mfma_f32_16x16x32_bf16 v[72:75], a[104:107], v[176:179], v[72:75]\n"
      "v_exp_f32 v149, v149\n"
      "v_mfma_f32_16x16x32_bf16 v[76:79], a[108:111], v[176:179], v[76:79]\n"
      "v_cvt_pk_bf16_f32 v146, v148, v149\n"
      "v_lshlrev_b32 v221, 16, v226\n"
      "v_mfma_f32_16x16x32_bf16 v[80:83], a[112:115], v[176:179], v[80:83]\n"
      "v_exp_f32 v150, v150\n"
      "v_mfma_f32_16x16x32_bf16 v[84:87], a[116:119], v[176:179], v[84:87]\n"
      "s_add_u32 m0, s58, 0x8000\n"
      "ds_read_b128 v[228:231], v209 offset:1536\n"
      "v_and_b32 v226, s62, v226\n"
      "v_mfma_f32_16x16x32_bf16 v[88:91], a[120:123], v[176:179], v[88:91]\n"
      "s_add_u32 s67, s57, 0x8000\n"
      "v_exp_f32 v151, v151\n"
      "v_mfma_f32_16x16x32_bf16 v[92:95], a[124:127], v[176:179], v[92:95]\n"
      "buffer_load_dwordx4 v212, s[48:51], s67 offen nt lds\n"
      "v_cvt_pk_bf16_f32 v147, v150, v151\n"
      "v_mfma_f32_16x16x32_bf16 a[140:143], a[144:147], v[184:187], a[140:143]\n"
      "v_exp_f32 v152, v152\n"
      "v_mfma_f32_16x16x32_bf16 v[96:99], a[96:99], v[184:187], v[96:99]\n"
      "buffer_load_dwordx4 v213, s[48:51], s67 offen offset:1024 nt lds\n"
      "v_mul_f32 v221, s61, v221\n"
      "v_mfma_f32_16x16x32_bf16 v[100:103], a[100:103], v[184:187], v[100:103]\n"
      "v_exp_f32 v153, v153\n"
      "v_mfma_f32_16x16x32_bf16 v[104:107], a[104:107], v[184:187], v[104:107]\n"
      "v_cvt_pk_bf16_f32 v152, v152, v153\n"
      "v_mul_f32 v226, s61, v226\n"
      "v_mfma_f32_16x16x32_bf16 v[108:111], a[108:111], v[184:187], v[108:111]\n"
      "v_exp_f32 v154, v154\n"
      "v_mfma_f32_16x16x32_bf16 v[112:115], a[112:115], v[184:187], v[112:115]\n"
      "v_cvt_pk_bf16_f32 v226, v221, v226\n"
      "v_accvgpr_write_b32 a170, v226\n"
      "v_mfma_f32_16x16x32_bf16 v[116:119], a[116:119], v[184:187], v[116:119]\n"
      "v_lshlrev_b32 v222, 16, v227\n"
      "v_and_b32 v227, s62, v227\n"
      "v_mfma_f32_16x16x32_bf16 v[120:123], a[120:123], v[184:187], v[120:123]\n"
      "s_memrealtime s[84:85]\n"
      "v_mul_f32 v222, s61, v222\n"
      "v_mul_f32 v227, s61, v227\n"
      "v_mfma_f32_16x16x32_bf16 v[124:127], a[124:127], v[184:187], v[124:127]\n"
      "v_cvt_pk_bf16_f32 v227, v222, v227\n"
      "v_accvgpr_write_b32 a171, v227\n"
      "s_waitcnt lgkmcnt(0)\n"
      "s_sub_u32 s85, s84, s86\n"
      "s_cmp_ge_u32 s85, s87\n"
      "s_cbranch_scc1 L_PACED0_2\n"
      "L_PACE0_2:\n"
      "s_memrealtime s[84:85]\n"
      "s_waitcnt lgkmcnt(0)\n"
      "s_sub_u32 s85, s84, s86\n"
      "s_cmp_lt_u32 s85, s87\n"
      "s_cbranch_scc1 L_PACE0_2\n"
      "L_PACED0_2:\n"
      "s_mov_b32 s86, s84\n"
      "v_mfma_f32_16x16x32_bf16 v[160:163], a[64:67], a[0:3], v[192:195]\n"
      "s_waitcnt vmcnt(10)\n"
      "s_barrier\n"
      "v_lshlrev_b32 v221, 16, v228\n"
      "v_and_b32 v228, s62, v228\n"
      "v_mul_f32 v221, s61, v221\n"
      "v_mfma_f32_16x16x32_bf16 v[160:163], a[68:71], a[4:7], v[160:163]\n"
      "v_exp_f32 v155, v155\n"
      "v_mfma_f32_16x16x32_bf16 v[160:163], a[72:75], a[8:11], v[160:163]\n"
      "ds_read_b128 a[148:151], v219 offset:10240\n"
      "v_cvt_pk_bf16_f32 v153, v154, v155\n"
      "v_mfma_f32_16x16x32_bf16 v[160:163], a[76:79], a[12:15], v[160:163]\n"
      "v_exp_f32 v156, v156\n"
      "v_mfma_f32_16x16x32_bf16 v[164:167], a[80:83], a[0:3], v[192:195]\n"
      "ds_read_b64_tr_b16 a[96:97], v210 offset:49152\n"
      "ds_read_b64_tr_b16 a[98:99], v210 offset:53248\n"
      "v_mfma_f32_16x16x32_bf16 v[164:167], a[84:87], a[4:7], v[164:167]\n"
      "v_exp_f32 v157, v157\n"
      "v_mfma_f32_16x16x32_bf16 v[164:167], a[88:91], a[8:11], v[164:167]\n"
      "ds_read_b64_tr_b16 a[100:101], v211 offset:49152\n"
      "v_cvt_pk_bf16_f32 v154, v156, v157\n"
      "v_mfma_f32_16x16x32_bf16 v[164:167], a[92:95], a[12:15], v[164:167]\n"
      "v_exp_f32 v158, v158\n"
      "v_mfma_f32_16x16x32_bf16 v[168:171], a[64:67], a[16:19], v[196:199]\n"
      "ds_read_b64_tr_b16 a[102:103], v211 offset:53248\n"
      "ds_read_b64_tr_b16 a[104:105], v210 offset:49664\n"
      "v_mfma_f32_16x16x32_bf16 v[168:171], a[68:71], a[20:23], v[168:171]\n"
      "v_exp_f32 v159, v159\n"
      "v_mfma_f32_16x16x32_bf16 v[168:171], a[72:75], a[24:27], v[168:171]\n"
      "ds_read_b64_tr_b16 a[106:107], v210 offset:53760\n"
      "v_cvt_pk_bf16_f32 v155, v158, v159\n"
      "v_mfma_f32_16x16x32_bf16 v[168:171], a[76:79], a[28:31], v[168:171]\n"
      "v_exp_f32 v160, v160\n"
      "v_mfma_f32_16x16x32_bf16 v[172:175], a[80:83], a[16:19], v[196:199]\n"
      "ds_read_b64_tr_b16 a[108:109], v211 offset:49664\n"
      "ds_read_b64_tr_b16 a[110:111], v211 offset:53760\n"
      "v_mfma_f32_16x16x32_bf16 v[172:175], a[84:87], a[20:23], v[172:175]\n"
      "v_exp_f32 v161, v161\n"
      "v_mfma_f32_16x16x32_bf16 v[172:175], a[88:91], a[24:27], v[172:175]\n"
      "ds_read_b64_tr_b16 a[112:113], v210 offset:50176\n"
      "v_cvt_pk_bf16_f32 v160, v160, v161\n"
      "v_mfma_f32_16x16x32_bf16 v[172:175], a[92:95], a[28:31], v[172:175]\n"
      "v_exp_f32 v162, v162\n"
      "v_mfma_f32_16x16x32_bf16 v[176:179], a[64:67], a[32:35], v[200:203]\n"
      "ds_read_b64_tr_b16 a[114:115], v210 offset:54272\n"
      "ds_read_b64_tr_b16 a[116:117], v211 offset:50176\n"
      "v_mfma_f32_16x16x32_bf16 v[176:179], a[68:71], a[36:39], v[176:179]\n"
      "v_exp_f32 v163, v163\n"
      "v_mfma_f32_16x16x32_bf16 v[176:179], a[72:75], a[40:43], v[176:179]\n"
      "ds_read_b64_tr_b16 a[118:119], v211 offset:54272\n"
      "v_cvt_pk_bf16_f32 v161, v162, v163\n"
      "v_mfma_f32_16x16x32_bf16 v[176:179], a[76:79], a[44:47], v[176:179]\n"
      "v_exp_f32 v164, v164\n"
      "v_mfma_f32_16x16x32_bf16 v[180:183], a[80:83], a[32:35], v[200:203]\n"
      "ds_read_b64_tr_b16 a[120:121], v210 offset:50688\n"
      "ds_read_b64_tr_b16 a[122:123], v210 offset:54784\n"
      "v_mfma_f32_16x16x32_bf16 v[180:183], a[84:87], a[36:39], v[180:183]\n"
      "v_exp_f32 v165, v165\n"
      "v_mfma_f32_16x16x32_bf16 v[180:183], a[88:91], a[40:43], v[180:183]\n"
      "ds_read_b64_tr_b16 a[124:125], v211 offset:50688\n"
      "v_cvt_pk_bf16_f32 v162, v164, v165\n"
      "v_mfma_f32_16x16x32_bf16 v[180:183], a[92:95], a[44:47], v[180:183]\n"
      "v_exp_f32 v166, v166\n"
      "v_mfma_f32_16x16x32_bf16 v[184:187], a[64:67], a[48:51], v[204:207]\n"
      "ds_read_b64_tr_b16 a[126:127], v211 offset:54784\n"
      "v_mul_f32 v228, s61, v228\n"
      "v_mfma_f32_16x16x32_bf16 v[184:187], a[68:71], a[52:55], v[184:187]\n"
      "v_exp_f32 v167, v167\n"
      "v_mfma_f32_16x16x32_bf16 v[184:187], a[72:75], a[56:59], v[184:187]\n"
      "v_cvt_pk_bf16_f32 v163, v166, v167\n"
      "v_cvt_pk_bf16_f32 v228, v221, v228\n"
      "v_mfma_f32_16x16x32_bf16 v[184:187], a[76:79], a[60:63], v[184:187]\n"
      "v_exp_f32 v168, v168\n"
      "v_mfma_f32_16x16x32_bf16 v[188:191], a[80:83], a[48:51], v[204:207]\n"
      "ds_read_b128 a[64:67], v208 offset:0\n"
      "ds_read_b128 a[68:71], v208 offset:512\n"
      "v_mfma_f32_16x16x32_bf16 v[188:191], a[84:87], a[52:55], v[188:191]\n"
      "v_exp_f32 v169, v169\n"
      "v_mfma_f32_16x16x32_bf16 v[188:191], a[88:91], a[56:59], v[188:191]\n"
      "ds_read_b128 a[72:75], v208 offset:1024\n"
      "v_cvt_pk_bf16_f32 v168, v168, v169\n"
      "v_mfma_f32_16x16x32_bf16 v[188:191], a[92:95], a[60:63], v[188:191]\n"
      "v_exp_f32 v170, v170\n"
      "v_mfma_f32_16x16x32_bf16 a[128:131], a[144:147], v[128:131], a[128:131]\n"
      "ds_read_b128 a[76:79], v208 offset:1536\n"
      "v_accvgpr_write_b32 a172, v228\n"
      "s_waitcnt lgkmcnt(12)\n"
      "v_mfma_f32_16x16x32_bf16 v[0:3], a[96:99], v[128:131], v[0:3]\n"
      "v_exp_f32 v171, v171\n"
      "v_mfma_f32_16x16x32_bf16 v[4:7], a[100:103], v[128:131], v[4:7]\n"
      "ds_read_b128 a[80:83], v208 offset:4096\n"
      "v_cvt_pk_bf16_f32 v169, v170, v171\n"
      "v_mfma_f32_16x16x32_bf16 v[8:11], a[104:107], v[128:131], v[8:11]\n"
      "v_exp_f32 v172, v172\n"
      "v_mfma_f32_16x16x32_bf16 v[12:15], a[108:111], v[128:131], v[12:15]\n"
      "ds_read_b128 a[84:87], v208 offset:4608\n"
      "ds_read_b128 a[88:91], v208 offset:5120\n"
      "v_lshlrev_b32 v222, 16, v229\n"
      "s_waitcnt lgkmcnt(7)\n"
      "v_mfma_f32_16x16x32_bf16 v[16:19], a[112:115], v[128:131], v[16:19]\n"
      "v_exp_f32 v173, v173\n"
      "v_mfma_f32_16x16x32_bf16 v[20:23], a[116:119], v[128:131], v[20:23]\n"
      "ds_read_b128 a[92:95], v208 offset:5632\n"
      "v_cvt_pk_bf16_f32 v170, v172, v173\n"
      "v_mfma_f32_16x16x32_bf16 v[24:27], a[120:123], v[128:131], v[24:27]\n"
      "v_exp_f32 v174, v174\n"
      "v_mfma_f32_16x16x32_bf16 v[28:31], a[124:127], v[128:131], v[28:31]\n"
      "s_add_u32 m0, s58, 0x6000\n"
      "buffer_store_dwordx4 a[148:151], v214, s[52:55], s76 offen offset:2048 sc1\n"
      "v_mfma_f32_16x16x32_bf16 a[132:135], a[144:147], v[136:139], a[132:135]\n"
      "s_add_u32 s67, s57, 0xe000\n"
      "v_exp_f32 v175, v175\n"
      "v_mfma_f32_16x16x32_bf16 v[32:35], a[96:99], v[136:139], v[32:35]\n"
      "buffer_load_dwordx4 v212, s[44:47], s67 offen nt lds\n"
      "v_cvt_pk_bf16_f32 v171, v174, v175\n"
      "v_mfma_f32_16x16x32_bf16 v[36:39], a[100:103], v[136:139], v[36:39]\n"
      "v_exp_f32 v176, v176\n"
      "v_mfma_f32_16x16x32_bf16 v[40:43], a[104:107], v[136:139], v[40:43]\n"
      "buffer_load_dwordx4 v213, s[44:47], s67 offen offset:1024 nt lds\n"
      "v_and_b32 v229, s62, v229\n"
      "v_mfma_f32_16x16x32_bf16 v[44:47], a[108:111], v[136:139], v[44:47]\n"
      "v_exp_f32 v177, v177\n"
      "v_mfma_f32_16x16x32_bf16 v[48:51], a[112:115], v[136:139], v[48:51]\n"
      "v_cvt_pk_bf16_f32 v176, v176, v177\n"
      "v_mul_f32 v222, s61, v222\n"
      "v_mfma_f32_16x16x32_bf16 v[52:55], a[116:119], v[136:139], v[52:55]\n"
      "v_exp_f32 v178, v178\n"
      "v_mfma_f32_16x16x32_bf16 v[56:59], a[120:123], v[136:139], v[56:59]\n"
      "v_mul_f32 v229, s61, v229\n"
      "v_cvt_pk_bf16_f32 v229, v222, v229\n"
      "v_mfma_f32_16x16x32_bf16 v[60:63], a[124:127], v[136:139], v[60:63]\n"
      "v_exp_f32 v179, v179\n"
      "v_mfma_f32_16x16x32_bf16 a[136:139], a[144:147], v[144:147], a[136:139]\n"
      "s_add_u32 m0, s58, 0xa000\n"
      "v_cvt_pk_bf16_f32 v177, v178, v179\n"
      "v_accvgpr_write_b32 a173, v229\n"
      "v_mfma_f32_16x16x32_bf16 v[64:67], a[96:99], v[144:147], v[64:67]\n"
      "s_add_u32 s68, s57, 0xa000\n"
      "v_exp_f32 v180, v180\n"
      "v_mfma_f32_16x16x32_bf16 v[68:71], a[100:103], v[144:147], v[68:71]\n"
      "buffer_load_dwordx4 v212, s[48:51], s68 offen nt lds\n"
      "v_lshlrev_b32 v221, 16, v230\n"
      "v_mfma_f32_16x16x32_bf16 v[72:75], a[104:107], v[144:147], v[72:75]\n"
      "v_exp_f32 v181, v181\n"
      "v_mfma_f32_16x16x32_bf16 v[76:79], a[108:111], v[144:147], v[76:79]\n"
      "buffer_load_dwordx4 v213, s[48:51], s68 offen offset:1024 nt lds\n"
      "v_cvt_pk_bf16_f32 v178, v180, v181\n"
      "v_mfma_f32_16x16x32_bf16 v[80:83], a[112:115], v[144:147], v[80:83]\n"
      "v_exp_f32 v182, v182\n"
      "v_mfma_f32_16x16x32_bf16 v[84:87], a[116:119], v[144:147], v[84:87]\n"
      "ds_read_b128 v[224:227], v209 offset:4096\n"
      "v_and_b32 v230, s62, v230\n"
      "v_mfma_f32_16x16x32_bf16 v[88:91], a[120:123], v[144:147], v[88:91]\n"
      "v_exp_f32 v183, v183\n"
      "v_mfma_f32_16x16x32_bf16 v[92:95], a[124:127], v[144:147], v[92:95]\n"
      "s_mov_b32 m0, s59\n"
      "v_cvt_pk_bf16_f32 v179, v182, v183\n"
      "v_mul_f32 v221, s61, v221\n"
      "v_mfma_f32_16x16x32_bf16 a[140:143], a[144:147], v[152:155], a[140:143]\n"
      "s_add_u32 s68, s56, 0x1002000\n"
      "v_exp_f32 v184, v184\n"
      "v_mfma_f32_16x16x32_bf16 v[96:99], a[96:99], v[152:155], v[96:99]\n"
      "buffer_load_dwordx4 v212, s[40:43], s68 offen nt lds\n"
      "v_mul_f32 v230, s61, v230\n"
      "v_mfma_f32_16x16x32_bf16 v[100:103], a[100:103], v[152:155], v[100:103]\n"
      "v_exp_f32 v185, v185\n"
      "v_mfma_f32_16x16x32_bf16 v[104:107], a[104:107], v[152:155], v[104:107]\n"
      "buffer_load_dwordx4 v213, s[40:43], s68 offen offset:1024 nt lds\n"
      "v_cvt_pk_bf16_f32 v184, v184, v185\n"
      "v_mfma_f32_16x16x32_bf16 v[108:111], a[108:111], v[152:155], v[108:111]\n"
      "v_exp_f32 v186, v186\n"
      "v_mfma_f32_16x16x32_bf16 v[112:115], a[112:115], v[152:155], v[112:115]\n"
      "v_cvt_pk_bf16_f32 v230, v221, v230\n"
      "v_accvgpr_write_b32 a174, v230\n"
      "v_mfma_f32_16x16x32_bf16 v[116:119], a[116:119], v[152:155], v[116:119]\n"
      "v_lshlrev_b32 v222, 16, v231\n"
      "v_and_b32 v231, s62, v231\n"
      "v_mfma_f32_16x16x32_bf16 v[120:123], a[120:123], v[152:155], v[120:123]\n"
      "v_mul_f32 v222, s61, v222\n"
      "v_mul_f32 v231, s61, v231\n"
      "v_mfma_f32_16x16x32_bf16 v[124:127], a[124:127], v[152:155], v[124:127]\n"
      "v_cvt_pk_bf16_f32 v231, v222, v231\n"
      "v_accvgpr_write_b32 a175, v231\n"
      "s_waitcnt lgkmcnt(0)\n"
      "v_mfma_f32_16x16x32_bf16 v[128:131], a[64:67], a[0:3], v[192:195]\n"
      "s_waitcnt vmcnt(12)\n"
      "s_barrier\n"
      "v_lshlrev_b32 v221, 16, v224\n"
      "v_and_b32 v224, s62, v224\n"
      "v_mul_f32 v221, s61, v221\n"
      "v_mfma_f32_16x16x32_bf16 v[128:131], a[68:71], a[4:7], v[128:131]\n"
      "v_exp_f32 v187, v187\n"
      "v_mfma_f32_16x16x32_bf16 v[128:131], a[72:75], a[8:11], v[128:131]\n"
      "ds_read_b128 a[152:155], v220 offset:11264\n"
      "v_cvt_pk_bf16_f32 v185, v186, v187\n"
      "v_mfma_f32_16x16x32_bf16 v[128:131], a[76:79], a[12:15], v[128:131]\n"
      "v_exp_f32 v188, v188\n"
      "v_mfma_f32_16x16x32_bf16 v[132:135], a[80:83], a[0:3], v[192:195]\n"
      "ds_read_b64_tr_b16 a[96:97], v210 offset:57344\n"
      "ds_read_b64_tr_b16 a[98:99], v210 offset:61440\n"
      "v_mfma_f32_16x16x32_bf16 v[132:135], a[84:87], a[4:7], v[132:135]\n"
      "v_exp_f32 v189, v189\n"
      "v_mfma_f32_16x16x32_bf16 v[132:135], a[88:91], a[8:11], v[132:135]\n"
      "ds_read_b64_tr_b16 a[100:101], v211 offset:57344\n"
      "v_cvt_pk_bf16_f32 v186, v188, v189\n"
      "v_mfma_f32_16x16x32_bf16 v[132:135], a[92:95], a[12:15], v[132:135]\n"
      "v_exp_f32 v190, v190\n"
      "v_mfma_f32_16x16x32_bf16 v[136:139], a[64:67], a[16:19], v[196:199]\n"
      "ds_read_b64_tr_b16 a[102:103], v211 offset:61440\n"
      "ds_read_b64_tr_b16 a[104:105], v210 offset:57856\n"
      "v_mfma_f32_16x16x32_bf16 v[136:139], a[68:71], a[20:23], v[136:139]\n"
      "v_exp_f32 v191, v191\n"
      "v_mfma_f32_16x16x32_bf16 v[136:139], a[72:75], a[24:27], v[136:139]\n"
      "ds_read_b64_tr_b16 a[106:107], v210 offset:61952\n"
      "v_cvt_pk_bf16_f32 v187, v190, v191\n"
      "v_mfma_f32_16x16x32_bf16 v[136:139], a[76:79], a[28:31], v[136:139]\n"
      "v_exp_f32 v128, v128\n"
      "v_mfma_f32_16x16x32_bf16 v[140:143], a[80:83], a[16:19], v[196:199]\n"
      "ds_read_b64_tr_b16 a[108:109], v211 offset:57856\n"
      "ds_read_b64_tr_b16 a[110:111], v211 offset:61952\n"
      "v_mfma_f32_16x16x32_bf16 v[140:143], a[84:87], a[20:23], v[140:143]\n"
      "v_exp_f32 v129, v129\n"
      "v_mfma_f32_16x16x32_bf16 v[140:143], a[88:91], a[24:27], v[140:143]\n"
      "ds_read_b64_tr_b16 a[112:113], v210 offset:58368\n"
      "v_cvt_pk_bf16_f32 v128, v128, v129\n"
      "v_mfma_f32_16x16x32_bf16 v[140:143], a[92:95], a[28:31], v[140:143]\n"
      "v_exp_f32 v130, v130\n"
      "v_mfma_f32_16x16x32_bf16 v[144:147], a[64:67], a[32:35], v[200:203]\n"
      "ds_read_b64_tr_b16 a[114:115], v210 offset:62464\n"
      "ds_read_b64_tr_b16 a[116:117], v211 offset:58368\n"
      "v_mfma_f32_16x16x32_bf16 v[144:147], a[68:71], a[36:39], v[144:147]\n"
      "v_exp_f32 v131, v131\n"
      "v_mfma_f32_16x16x32_bf16 v[144:147], a[72:75], a[40:43], v[144:147]\n"
      "ds_read_b64_tr_b16 a[118:119], v211 offset:62464\n"
      "v_cvt_pk_bf16_f32 v129, v130, v131\n"
      "v_mfma_f32_16x16x32_bf16 v[144:147], a[76:79], a[44:47], v[144:147]\n"
      "v_exp_f32 v132, v132\n"
      "v_mfma_f32_16x16x32_bf16 v[148:151], a[80:83], a[32:35], v[200:203]\n"
      "ds_read_b64_tr_b16 a[120:121], v210 offset:58880\n"
      "ds_read_b64_tr_b16 a[122:123], v210 offset:62976\n"
      "v_mfma_f32_16x16x32_bf16 v[148:151], a[84:87], a[36:39], v[148:151]\n"
      "v_exp_f32 v133, v133\n"
      "v_mfma_f32_16x16x32_bf16 v[148:151], a[88:91], a[40:43], v[148:151]\n"
      "ds_read_b64_tr_b16 a[124:125], v211 offset:58880\n"
      "v_cvt_pk_bf16_f32 v130, v132, v133\n"
      "v_mfma_f32_16x16x32_bf16 v[148:151], a[92:95], a[44:47], v[148:151]\n"
      "v_exp_f32 v134, v134\n"
      "v_mfma_f32_16x16x32_bf16 v[152:155], a[64:67], a[48:51], v[204:207]\n"
      "ds_read_b64_tr_b16 a[126:127], v211 offset:62976\n"
      "v_mul_f32 v224, s61, v224\n"
      "v_mfma_f32_16x16x32_bf16 v[152:155], a[68:71], a[52:55], v[152:155]\n"
      "v_exp_f32 v135, v135\n"
      "v_mfma_f32_16x16x32_bf16 v[152:155], a[72:75], a[56:59], v[152:155]\n"
      "v_cvt_pk_bf16_f32 v131, v134, v135\n"
      "v_cvt_pk_bf16_f32 v224, v221, v224\n"
      "v_mfma_f32_16x16x32_bf16 v[152:155], a[76:79], a[60:63], v[152:155]\n"
      "v_exp_f32 v136, v136\n"
      "v_mfma_f32_16x16x32_bf16 v[156:159], a[80:83], a[48:51], v[204:207]\n"
      "ds_read_b128 a[64:67], v208 offset:8192\n"
      "ds_read_b128 a[68:71], v208 offset:8704\n"
      "v_mfma_f32_16x16x32_bf16 v[156:159], a[84:87], a[52:55], v[156:159]\n"
      "v_exp_f32 v137, v137\n"
      "v_mfma_f32_16x16x32_bf16 v[156:159], a[88:91], a[56:59], v[156:159]\n"
      "ds_read_b128 a[72:75], v208 offset:9216\n"
      "v_cvt_pk_bf16_f32 v136, v136, v137\n"
      "v_mfma_f32_16x16x32_bf16 v[156:159], a[92:95], a[60:63], v[156:159]\n"
      "v_exp_f32 v138, v138\n"
      "v_mfma_f32_16x16x32_bf16 a[128:131], a[144:147], v[160:163], a[128:131]\n"
      "ds_read_b128 a[76:79], v208 offset:9728\n"
      "v_accvgpr_write_b32 a176, v224\n"
      "s_waitcnt lgkmcnt(12)\n"
      "v_mfma_f32_16x16x32_bf16 v[0:3], a[96:99], v[160:163], v[0:3]\n"
      "v_exp_f32 v139, v139\n"
      "v_mfma_f32_16x16x32_bf16 v[4:7], a[100:103], v[160:163], v[4:7]\n"
      "ds_read_b128 a[80:83], v208 offset:12288\n"
      "v_cvt_pk_bf16_f32 v137, v138, v139\n"
      "v_mfma_f32_16x16x32_bf16 v[8:11], a[104:107], v[160:163], v[8:11]\n"
      "v_exp_f32 v140, v140\n"
      "v_mfma_f32_16x16x32_bf16 v[12:15], a[108:111], v[160:163], v[12:15]\n"
      "ds_read_b128 a[84:87], v208 offset:12800\n"
      "ds_read_b128 a[88:91], v208 offset:13312\n"
      "v_lshlrev_b32 v222, 16, v225\n"
      "s_waitcnt lgkmcnt(7)\n"
      "v_mfma_f32_16x16x32_bf16 v[16:19], a[112:115], v[160:163], v[16:19]\n"
      "v_exp_f32 v141, v141\n"
      "v_mfma_f32_16x16x32_bf16 v[20:23], a[116:119], v[160:163], v[20:23]\n"
      "ds_read_b128 a[92:95], v208 offset:13824\n"
      "v_cvt_pk_bf16_f32 v138, v140, v141\n"
      "v_mfma_f32_16x16x32_bf16 v[24:27], a[120:123], v[160:163], v[24:27]\n"
      "v_exp_f32 v142, v142\n"
      "v_mfma_f32_16x16x32_bf16 v[28:31], a[124:127], v[160:163], v[28:31]\n"
      "s_mov_b32 m0, s58\n"
      "buffer_store_dwordx4 a[152:155], v214, s[52:55], s76 offen offset:3072 sc1\n"
      "v_mfma_f32_16x16x32_bf16 a[132:135], a[144:147], v[168:171], a[132:135]\n"
      "s_add_u32 s68, s57, 0x10000\n"
      "v_exp_f32 v143, v143\n"
      "v_mfma_f32_16x16x32_bf16 v[32:35], a[96:99], v[168:171], v[32:35]\n"
      "buffer_load_dwordx4 v212, s[44:47], s68 offen nt lds\n"
      "v_cvt_pk_bf16_f32 v139, v142, v143\n"
      "v_mfma_f32_16x16x32_bf16 v[36:39], a[100:103], v[168:171], v[36:39]\n"
      "v_exp_f32 v144, v144\n"
      "v_mfma_f32_16x16x32_bf16 v[40:43], a[104:107], v[168:171], v[40:43]\n"
      "buffer_load_dwordx4 v213, s[44:47], s68 offen offset:1024 nt lds\n"
      "v_and_b32 v225, s62, v225\n"
      "v_mfma_f32_16x16x32_bf16 v[44:47], a[108:111], v[168:171], v[44:47]\n"
      "v_exp_f32 v145, v145\n"
      "v_mfma_f32_16x16x32_bf16 v[48:51], a[112:115], v[168:171], v[48:51]\n"
      "v_cvt_pk_bf16_f32 v144, v144, v145\n"
      "v_mul_f32 v222, s61, v222\n"
      "v_mfma_f32_16x16x32_bf16 v[52:55], a[116:119], v[168:171], v[52:55]\n"
      "v_exp_f32 v146, v146\n"
      "v_mfma_f32_16x16x32_bf16 v[56:59], a[120:123], v[168:171], v[56:59]\n"
      "v_mul_f32 v225, s61, v225\n"
      "v_cvt_pk_bf16_f32 v225, v222, v225\n"
      "v_mfma_f32_16x16x32_bf16 v[60:63], a[124:127], v[168:171], v[60:63]\n"
      "v_exp_f32 v147, v147\n"
      "v_mfma_f32_16x16x32_bf16 a[136:139], a[144:147], v[176:179], a[136:139]\n"
      "s_add_u32 m0, s58, 0xc000\n"
      "v_cvt_pk_bf16_f32 v145, v146, v147\n"
      "v_accvgpr_write_b32 a177, v225\n"
      "v_mfma_f32_16x16x32_bf16 v[64:67], a[96:99], v[176:179], v[64:67]\n"
      "s_add_u32 s67, s57, 0xc000\n"
      "v_exp_f32 v148, v148\n"
      "v_mfma_f32_16x16x32_bf16 v[68:71], a[100:103], v[176:179], v[68:71]\n"
      "buffer_load_dwordx4 v212, s[48:51], s67 offen nt lds\n"
      "v_lshlrev_b32 v221, 16, v226\n"
      "v_mfma_f32_16x16x32_bf16 v[72:75], a[104:107], v[176:179], v[72:75]\n"
      "v_exp_f32 v149, v149\n"
      "v_mfma_f32_16x16x32_bf16 v[76:79], a[108:111], v[176:179], v[76:79]\n"
      "buffer_load_dwordx4 v213, s[48:51], s67 offen offset:1024 nt lds\n"
      "v_cvt_pk_bf16_f32 v146, v148, v149\n"
      "v_mfma_f32_16x16x32_bf16 v[80:83], a[112:115], v[176:179], v[80:83]\n"
      "v_exp_f32 v150, v150\n"
      "v_mfma_f32_16x16x32_bf16 v[84:87], a[116:119], v[176:179], v[84:87]\n"
      "ds_read_b128 v[228:231], v209 offset:4608\n"
      "v_and_b32 v226, s62, v226\n"
      "v_mfma_f32_16x16x32_bf16 v[88:91], a[120:123], v[176:179], v[88:91]\n"
      "v_exp_f32 v151, v151\n"
      "v_mfma_f32_16x16x32_bf16 v[92:95], a[124:127], v[176:179], v[92:95]\n"
      "s_add_u32 m0, s59, 0x800\n"
      "v_cvt_pk_bf16_f32 v147, v150, v151\n"
      "v_mul_f32 v221, s61, v221\n"
      "v_mfma_f32_16x16x32_bf16 a[140:143], a[144:147], v[184:187], a[140:143]\n"
      "s_add_u32 s67, s56, 0x1002800\n"
      "v_exp_f32 v152, v152\n"
      "v_mfma_f32_16x16x32_bf16 v[96:99], a[96:99], v[184:187], v[96:99]\n"
      "buffer_load_dwordx4 v212, s[40:43], s67 offen nt lds\n"
      "v_mul_f32 v226, s61, v226\n"
      "v_mfma_f32_16x16x32_bf16 v[100:103], a[100:103], v[184:187], v[100:103]\n"
      "v_exp_f32 v153, v153\n"
      "v_mfma_f32_16x16x32_bf16 v[104:107], a[104:107], v[184:187], v[104:107]\n"
      "buffer_load_dwordx4 v213, s[40:43], s67 offen offset:1024 nt lds\n"
      "v_cvt_pk_bf16_f32 v152, v152, v153\n"
      "v_mfma_f32_16x16x32_bf16 v[108:111], a[108:111], v[184:187], v[108:111]\n"
      "v_exp_f32 v154, v154\n"
      "v_mfma_f32_16x16x32_bf16 v[112:115], a[112:115], v[184:187], v[112:115]\n"
      "v_cvt_pk_bf16_f32 v226, v221, v226\n"
      "v_accvgpr_write_b32 a178, v226\n"
      "v_mfma_f32_16x16x32_bf16 v[116:119], a[116:119], v[184:187], v[116:119]\n"
      "v_lshlrev_b32 v222, 16, v227\n"
      "v_and_b32 v227, s62, v227\n"
      "v_mfma_f32_16x16x32_bf16 v[120:123], a[120:123], v[184:187], v[120:123]\n"
      "s_memrealtime s[84:85]\n"
      "v_mul_f32 v222, s61, v222\n"
      "v_mul_f32 v227, s61, v227\n"
      "v_mfma_f32_16x16x32_bf16 v[124:127], a[124:127], v[184:187], v[124:127]\n"
      "v_cvt_pk_bf16_f32 v227, v222, v227\n"
      "v_accvgpr_write_b32 a179, v227\n"
      "s_waitcnt lgkmcnt(0)\n"
      "s_sub_u32 s85, s84, s86\n"
      "s_cmp_ge_u32 s85, s87\n"
      "s_cbranch_scc1 L_PACED0_4\n"
      "L_PACE0_4:\n"
      "s_memrealtime s[84:85]\n"
      "s_waitcnt lgkmcnt(0)\n"
      "s_sub_u32 s85, s84, s86\n"
      "s_cmp_lt_u32 s85, s87\n"
      "s_cbranch_scc1 L_PACE0_4\n"
      "L_PACED0_4:\n"
      "s_mov_b32 s86, s84\n"
      "v_mfma_f32_16x16x32_bf16 v[160:163], a[64:67], a[0:3], v[192:195]\n"
      "s_waitcnt vmcnt(14)\n"
      "s_barrier\n"
      "v_lshlrev_b32 v221, 16, v228\n"
      "v_and_b32 v228, s62, v228\n"
      "v_mfma_f32_16x16x32_bf16 v[160:163], a[68:71], a[4:7], v[160:163]\n"
      "v_exp_f32 v155, v155\n"
      "v_mfma_f32_16x16x32_bf16 v[160:163], a[72:75], a[8:11], v[160:163]\n"
      "ds_read_b128 a[148:151], v219 offset:12288\n"
      "v_cvt_pk_bf16_f32 v153, v154, v155\n"
      "v_mfma_f32_16x16x32_bf16 v[160:163], a[76:79], a[12:15], v[160:163]\n"
      "v_exp_f32 v156, v156\n"
      "v_mfma_f32_16x16x32_bf16 v[164:167], a[80:83], a[0:3], v[192:195]\n"
      "ds_read_b64_tr_b16 a[96:97], v210 offset:32768\n"
      "ds_read_b64_tr_b16 a[98:99], v210 offset:36864\n"
      "v_mfma_f32_16x16x32_bf16 v[164:167], a[84:87], a[4:7], v[164:167]\n"
      "v_exp_f32 v157, v157\n"
      "v_mfma_f32_16x16x32_bf16 v[164:167], a[88:91], a[8:11], v[164:167]\n"
      "ds_read_b64_tr_b16 a[100:101], v211 offset:32768\n"
      "v_cvt_pk_bf16_f32 v154, v156, v157\n"
      "v_mfma_f32_16x16x32_bf16 v[164:167], a[92:95], a[12:15], v[164:167]\n"
      "v_exp_f32 v158, v158\n"
      "v_mfma_f32_16x16x32_bf16 v[168:171], a[64:67], a[16:19], v[196:199]\n"
      "ds_read_b64_tr_b16 a[102:103], v211 offset:36864\n"
      "ds_read_b64_tr_b16 a[104:105], v210 offset:33280\n"
      "v_mfma_f32_16x16x32_bf16 v[168:171], a[68:71], a[20:23], v[168:171]\n"
      "v_exp_f32 v159, v159\n"
      "v_mfma_f32_16x16x32_bf16 v[168:171], a[72:75], a[24:27], v[168:171]\n"
      "ds_read_b64_tr_b16 a[106:107], v210 offset:37376\n"
      "v_cvt_pk_bf16_f32 v155, v158, v159\n"
      "v_mfma_f32_16x16x32_bf16 v[168:171], a[76:79], a[28:31], v[168:171]\n"
      "v_exp_f32 v160, v160\n"
      "v_mfma_f32_16x16x32_bf16 v[172:175], a[80:83], a[16:19], v[196:199]\n"
      "ds_read_b64_tr_b16 a[108:109], v211 offset:33280\n"
      "ds_read_b64_tr_b16 a[110:111], v211 offset:37376\n"
      "v_mfma_f32_16x16x32_bf16 v[172:175], a[84:87], a[20:23], v[172:175]\n"
      "v_exp_f32 v161, v161\n"
      "v_mfma_f32_16x16x32_bf16 v[172:175], a[88:91], a[24:27], v[172:175]\n"
      "ds_read_b64_tr_b16 a[112:113], v210 offset:33792\n"
      "v_cvt_pk_bf16_f32 v160, v160, v161\n"
      "v_mfma_f32_16x16x32_bf16 v[172:175], a[92:95], a[28:31], v[172:175]\n"
      "v_exp_f32 v162, v162\n"
      "v_mfma_f32_16x16x32_bf16 v[176:179], a[64:67], a[32:35], v[200:203]\n"
      "ds_read_b64_tr_b16 a[114:115], v210 offset:37888\n"
      "ds_read_b64_tr_b16 a[116:117], v211 offset:33792\n"
      "v_mfma_f32_16x16x32_bf16 v[176:179], a[68:71], a[36:39], v[176:179]\n"
      "v_exp_f32 v163, v163\n"
      "v_mfma_f32_16x16x32_bf16 v[176:179], a[72:75], a[40:43], v[176:179]\n"
      "ds_read_b64_tr_b16 a[118:119], v211 offset:37888\n"
      "v_cvt_pk_bf16_f32 v161, v162, v163\n"
      "v_mfma_f32_16x16x32_bf16 v[176:179], a[76:79], a[44:47], v[176:179]\n"
      "v_exp_f32 v164, v164\n"
      "v_mfma_f32_16x16x32_bf16 v[180:183], a[80:83], a[32:35], v[200:203]\n"
      "ds_read_b64_tr_b16 a[120:121], v210 offset:34304\n"
      "ds_read_b64_tr_b16 a[122:123], v210 offset:38400\n"
      "v_mfma_f32_16x16x32_bf16 v[180:183], a[84:87], a[36:39], v[180:183]\n"
      "v_exp_f32 v165, v165\n"
      "v_mfma_f32_16x16x32_bf16 v[180:183], a[88:91], a[40:43], v[180:183]\n"
      "ds_read_b64_tr_b16 a[124:125], v211 offset:34304\n"
      "v_cvt_pk_bf16_f32 v162, v164, v165\n"
      "v_mfma_f32_16x16x32_bf16 v[180:183], a[92:95], a[44:47], v[180:183]\n"
      "v_exp_f32 v166, v166\n"
      "v_mfma_f32_16x16x32_bf16 v[184:187], a[64:67], a[48:51], v[204:207]\n"
      "ds_read_b64_tr_b16 a[126:127], v211 offset:38400\n"
      "v_mul_f32 v221, s61, v221\n"
      "v_mfma_f32_16x16x32_bf16 v[184:187], a[68:71], a[52:55], v[184:187]\n"
      "v_exp_f32 v167, v167\n"
      "v_mfma_f32_16x16x32_bf16 v[184:187], a[72:75], a[56:59], v[184:187]\n"
      "v_cvt_pk_bf16_f32 v163, v166, v167\n"
      "v_mul_f32 v228, s61, v228\n"
      "v_mfma_f32_16x16x32_bf16 v[184:187], a[76:79], a[60:63], v[184:187]\n"
      "v_exp_f32 v168, v168\n"
      "v_mfma_f32_16x16x32_bf16 v[188:191], a[80:83], a[48:51], v[204:207]\n"
      "ds_read_b128 a[64:67], v208 offset:16384\n"
      "ds_read_b128 a[68:71], v208 offset:16896\n"
      "v_mfma_f32_16x16x32_bf16 v[188:191], a[84:87], a[52:55], v[188:191]\n"
      "v_exp_f32 v169, v169\n"
      "v_mfma_f32_16x16x32_bf16 v[188:191], a[88:91], a[56:59], v[188:191]\n"
      "ds_read_b128 a[72:75], v208 offset:17408\n"
      "v_cvt_pk_bf16_f32 v168, v168, v169\n"
      "v_mfma_f32_16x16x32_bf16 v[188:191], a[92:95], a[60:63], v[188:191]\n"
      "v_exp_f32 v170, v170\n"
      "v_mfma_f32_16x16x32_bf16 a[128:131], a[144:147], v[128:131], a[128:131]\n"
      "ds_read_b128 a[76:79], v208 offset:17920\n"
      "v_cvt_pk_bf16_f32 v228, v221, v228\n"
      "s_waitcnt lgkmcnt(12)\n"
      "v_mfma_f32_16x16x32_bf16 v[0:3], a[96:99], v[128:131], v[0:3]\n"
      "v_exp_f32 v171, v171\n"
      "v_mfma_f32_16x16x32_bf16 v[4:7], a[100:103], v[128:131], v[4:7]\n"
      "ds_read_b128 a[80:83], v208 offset:20480\n"
      "v_cvt_pk_bf16_f32 v169, v170, v171\n"
      "v_mfma_f32_16x16x32_bf16 v[8:11], a[104:107], v[128:131], v[8:11]\n"
      "v_exp_f32 v172, v172\n"
      "v_mfma_f32_16x16x32_bf16 v[12:15], a[108:111], v[128:131], v[12:15]\n"
      "ds_read_b128 a[84:87], v208 offset:20992\n"
      "ds_read_b128 a[88:91], v208 offset:21504\n"
      "s_waitcnt lgkmcnt(7)\n"
      "v_mfma_f32_16x16x32_bf16 v[16:19], a[112:115], v[128:131], v[16:19]\n"
      "v_exp_f32 v173, v173\n"
      "v_mfma_f32_16x16x32_bf16 v[20:23], a[116:119], v[128:131], v[20:23]\n"
      "ds_read_b128 a[92:95], v208 offset:22016\n"
      "v_cvt_pk_bf16_f32 v170, v172, v173\n"
      "v_mfma_f32_16x16x32_bf16 v[24:27], a[120:123], v[128:131], v[24:27]\n"
      "v_exp_f32 v174, v174\n"
      "v_mfma_f32_16x16x32_bf16 v[28:31], a[124:127], v[128:131], v[28:31]\n"
      "buffer_store_dwordx4 a[148:151], v214, s[52:55], s77 offen offset:0 sc1\n"
      "v_mfma_f32_16x16x32_bf16 a[132:135], a[144:147], v[136:139], a[132:135]\n"
      "v_exp_f32 v175, v175\n"
      "v_mfma_f32_16x16x32_bf16 v[32:35], a[96:99], v[136:139], v[32:35]\n"
      "s_add_u32 m0, s58, 0x2000\n"
      "v_cvt_pk_bf16_f32 v171, v174, v175\n"
      "v_accvgpr_write_b32 a180, v228\n"
      "v_mfma_f32_16x16x32_bf16 v[36:39], a[100:103], v[136:139], v[36:39]\n"
      "s_add_u32 s68, s57, 0x12000\n"
      "v_exp_f32 v176, v176\n"
      "v_mfma_f32_16x16x32_bf16 v[40:43], a[104:107], v[136:139], v[40:43]\n"
      "buffer_load_dwordx4 v212, s[44:47], s68 offen nt lds\n"
      "v_lshlrev_b32 v222, 16, v229\n"
      "v_mfma_f32_16x16x32_bf16 v[44:47], a[108:111], v[136:139], v[44:47]\n"
      "v_exp_f32 v177, v177\n"
      "v_mfma_f32_16x16x32_bf16 v[48:51], a[112:115], v[136:139], v[48:51]\n"
      "buffer_load_dwordx4 v213, s[44:47], s68 offen offset:1024 nt lds\n"
      "v_cvt_pk_bf16_f32 v176, v176, v177\n"
      "v_mfma_f32_16x16x32_bf16 v[52:55], a[116:119], v[136:139], v[52:55]\n"
      "v_exp_f32 v178, v178\n"
      "v_mfma_f32_16x16x32_bf16 v[56:59], a[120:123], v[136:139], v[56:59]\n"
      "v_and_b32 v229, s62, v229\n"
      "v_mul_f32 v222, s61, v222\n"
      "v_mfma_f32_16x16x32_bf16 v[60:63], a[124:127], v[136:139], v[60:63]\n"
      "v_exp_f32 v179, v179\n"
      "v_mfma_f32_16x16x32_bf16 a[136:139], a[144:147], v[144:147], a[136:139]\n"
      "v_cvt_pk_bf16_f32 v177, v178, v179\n"
      "v_mul_f32 v229, s61, v229\n"
      "v_mfma_f32_16x16x32_bf16 v[64:67], a[96:99], v[144:147], v[64:67]\n"
      "v_exp_f32 v180, v180\n"
      "v_mfma_f32_16x16x32_bf16 v[68:71], a[100:103], v[144:147], v[68:71]\n"
      "v_cvt_pk_bf16_f32 v229, v222, v229\n"
      "v_accvgpr_write_b32 a181, v229\n"
      "v_mfma_f32_16x16x32_bf16 v[72:75], a[104:107], v[144:147], v[72:75]\n"
      "v_exp_f32 v181, v181\n"
      "v_mfma_f32_16x16x32_bf16 v[76:79], a[108:111], v[144:147], v[76:79]\n"
      "v_cvt_pk_bf16_f32 v178, v180, v181\n"
      "v_lshlrev_b32 v221, 16, v230\n"
      "v_mfma_f32_16x16x32_bf16 v[80:83], a[112:115], v[144:147], v[80:83]\n"
      "v_exp_f32 v182, v182\n"
      "v_mfma_f32_16x16x32_bf16 v[84:87], a[116:119], v[144:147], v[84:87]\n"
      "s_add_u32 m0, s58, 0xe000\n"
      "ds_read_b128 v[224:227], v209 offset:5120\n"
      "v_and_b32 v230, s62, v230\n"
      "v_mfma_f32_16x16x32_bf16 v[88:91], a[120:123], v[144:147], v[88:91]\n"
      "s_add_u32 s67, s57, 0xe000\n"
      "v_exp_f32 v183, v183\n"
      "v_mfma_f32_16x16x32_bf16 v[92:95], a[124:127], v[144:147], v[92:95]\n"
      "buffer_load_dwordx4 v212, s[48:51], s67 offen nt lds\n"
      "v_cvt_pk_bf16_f32 v179, v182, v183\n"
      "v_mfma_f32_16x16x32_bf16 a[140:143], a[144:147], v[152:155], a[140:143]\n"
      "v_exp_f32 v184, v184\n"
      "v_mfma_f32_16x16x32_bf16 v[96:99], a[96:99], v[152:155], v[96:99]\n"
      "buffer_load_dwordx4 v213, s[48:51], s67 offen offset:1024 nt lds\n"
      "v_mul_f32 v221, s61, v221\n"
      "v_mfma_f32_16x16x32_bf16 v[100:103], a[100:103], v[152:155], v[100:103]\n"
      "v_exp_f32 v185, v185\n"
      "v_mfma_f32_16x16x32_bf16 v[104:107], a[104:107], v[152:155], v[104:107]\n"
      "v_cvt_pk_bf16_f32 v184, v184, v185\n"
      "v_mul_f32 v230, s61, v230\n"
      "v_mfma_f32_16x16x32_bf16 v[108:111], a[108:111], v[152:155], v[108:111]\n"
      "v_exp_f32 v186, v186\n"
      "v_mfma_f32_16x16x32_bf16 v[112:115], a[112:115], v[152:155], v[112:115]\n"
      "v_cvt_pk_bf16_f32 v230, v221, v230\n"
      "v_accvgpr_write_b32 a182, v230\n"
      "v_mfma_f32_16x16x32_bf16 v[116:119], a[116:119], v[152:155], v[116:119]\n"
      "v_lshlrev_b32 v222, 16, v231\n"
      "v_and_b32 v231, s62, v231\n"
      "v_mfma_f32_16x16x32_bf16 v[120:123], a[120:123], v[152:155], v[120:123]\n"
      "v_mul_f32 v222, s61, v222\n"
      "v_mul_f32 v231, s61, v231\n"
      "v_mfma_f32_16x16x32_bf16 v[124:127], a[124:127], v[152:155], v[124:127]\n"
      "v_cvt_pk_bf16_f32 v231, v222, v231\n"
      "v_accvgpr_write_b32 a183, v231\n"
      "s_waitcnt lgkmcnt(0)\n"
      "v_mfma_f32_16x16x32_bf16 v[128:131], a[64:67], a[0:3], v[192:195]\n"
      "s_waitcnt vmcnt(14)\n"
      "s_barrier\n"
      "v_lshlrev_b32 v221, 16, v224\n"
      "v_and_b32 v224, s62, v224\n"
      "v_mfma_f32_16x16x32_bf16 v[128:131], a[68:71], a[4:7], v[128:131]\n"
      "v_exp_f32 v187, v187\n"
      "v_mfma_f32_16x16x32_bf16 v[128:131], a[72:75], a[8:11], v[128:131]\n"
      "ds_read_b128 a[152:155], v220 offset:13312\n"
      "v_cvt_pk_bf16_f32 v185, v186, v187\n"
      "v_mfma_f32_16x16x32_bf16 v[128:131], a[76:79], a[12:15], v[128:131]\n"
      "v_exp_f32 v188, v188\n"
      "v_mfma_f32_16x16x32_bf16 v[132:135], a[80:83], a[0:3], v[192:195]\n"
      "ds_read_b64_tr_b16 a[96:97], v210 offset:40960\n"
      "ds_read_b64_tr_b16 a[98:99], v210 offset:45056\n"
      "v_mfma_f32_16x16x32_bf16 v[132:135], a[84:87], a[4:7], v[132:135]\n"
      "v_exp_f32 v189, v189\n"
      "v_mfma_f32_16x16x32_bf16 v[132:135], a[88:91], a[8:11], v[132:135]\n"
      "ds_read_b64_tr_b16 a[100:101], v211 offset:40960\n"
      "v_cvt_pk_bf16_f32 v186, v188, v189\n"
      "v_mfma_f32_16x16x32_bf16 v[132:135], a[92:95], a[12:15], v[132:135]\n"
      "v_exp_f32 v190, v190\n"
      "v_mfma_f32_16x16x32_bf16 v[136:139], a[64:67], a[16:19], v[196:199]\n"
      "ds_read_b64_tr_b16 a[102:103], v211 offset:45056\n"
      "ds_read_b64_tr_b16 a[104:105], v210 offset:41472\n"
      "v_mfma_f32_16x16x32_bf16 v[136:139], a[68:71], a[20:23], v[136:139]\n"
      "v_exp_f32 v191, v191\n"
      "v_mfma_f32_16x16x32_bf16 v[136:139], a[72:75], a[24:27], v[136:139]\n"
      "ds_read_b64_tr_b16 a[106:107], v210 offset:45568\n"
      "v_cvt_pk_bf16_f32 v187, v190, v191\n"
      "v_mfma_f32_16x16x32_bf16 v[136:139], a[76:79], a[28:31], v[136:139]\n"
      "v_exp_f32 v128, v128\n"
      "v_mfma_f32_16x16x32_bf16 v[140:143], a[80:83], a[16:19], v[196:199]\n"
      "ds_read_b64_tr_b16 a[108:109], v211 offset:41472\n"
      "ds_read_b64_tr_b16 a[110:111], v211 offset:45568\n"
      "v_mfma_f32_16x16x32_bf16 v[140:143], a[84:87], a[20:23], v[140:143]\n"
      "v_exp_f32 v129, v129\n"
      "v_mfma_f32_16x16x32_bf16 v[140:143], a[88:91], a[24:27], v[140:143]\n"
      "ds_read_b64_tr_b16 a[112:113], v210 offset:41984\n"
      "v_cvt_pk_bf16_f32 v128, v128, v129\n"
      "v_mfma_f32_16x16x32_bf16 v[140:143], a[92:95], a[28:31], v[140:143]\n"
      "v_exp_f32 v130, v130\n"
      "v_mfma_f32_16x16x32_bf16 v[144:147], a[64:67], a[32:35], v[200:203]\n"
      "ds_read_b64_tr_b16 a[114:115], v210 offset:46080\n"
      "ds_read_b64_tr_b16 a[116:117], v211 offset:41984\n"
      "v_mfma_f32_16x16x32_bf16 v[144:147], a[68:71], a[36:39], v[144:147]\n"
      "v_exp_f32 v131, v131\n"
      "v_mfma_f32_16x16x32_bf16 v[144:147], a[72:75], a[40:43], v[144:147]\n"
      "ds_read_b64_tr_b16 a[118:119], v211 offset:46080\n"
      "v_cvt_pk_bf16_f32 v129, v130, v131\n"
      "v_mfma_f32_16x16x32_bf16 v[144:147], a[76:79], a[44:47], v[144:147]\n"
      "v_exp_f32 v132, v132\n"
      "v_mfma_f32_16x16x32_bf16 v[148:151], a[80:83], a[32:35], v[200:203]\n"
      "ds_read_b64_tr_b16 a[120:121], v210 offset:42496\n"
      "ds_read_b64_tr_b16 a[122:123], v210 offset:46592\n"
      "v_mfma_f32_16x16x32_bf16 v[148:151], a[84:87], a[36:39], v[148:151]\n"
      "v_exp_f32 v133, v133\n"
      "v_mfma_f32_16x16x32_bf16 v[148:151], a[88:91], a[40:43], v[148:151]\n"
      "ds_read_b64_tr_b16 a[124:125], v211 offset:42496\n"
      "v_cvt_pk_bf16_f32 v130, v132, v133\n"
      "v_mfma_f32_16x16x32_bf16 v[148:151], a[92:95], a[44:47], v[148:151]\n"
      "v_exp_f32 v134, v134\n"
      "v_mfma_f32_16x16x32_bf16 v[152:155], a[64:67], a[48:51], v[204:207]\n"
      "ds_read_b64_tr_b16 a[126:127], v211 offset:46592\n"
      "v_mul_f32 v221, s61, v221\n"
      "v_mfma_f32_16x16x32_bf16 v[152:155], a[68:71], a[52:55], v[152:155]\n"
      "v_exp_f32 v135, v135\n"
      "v_mfma_f32_16x16x32_bf16 v[152:155], a[72:75], a[56:59], v[152:155]\n"
      "v_cvt_pk_bf16_f32 v131, v134, v135\n"
      "v_mul_f32 v224, s61, v224\n"
      "v_mfma_f32_16x16x32_bf16 v[152:155], a[76:79], a[60:63], v[152:155]\n"
      "v_exp_f32 v136, v136\n"
      "v_mfma_f32_16x16x32_bf16 v[156:159], a[80:83], a[48:51], v[204:207]\n"
      "ds_read_b128 a[64:67], v208 offset:24576\n"
      "ds_read_b128 a[68:71], v208 offset:25088\n"
      "v_mfma_f32_16x16x32_bf16 v[156:159], a[84:87], a[52:55], v[156:159]\n"
      "v_exp_f32 v137, v137\n"
      "v_mfma_f32_16x16x32_bf16 v[156:159], a[88:91], a[56:59], v[156:159]\n"
      "ds_read_b128 a[72:75], v208 offset:25600\n"
      "v_cvt_pk_bf16_f32 v136, v136, v137\n"
      "v_mfma_f32_16x16x32_bf16 v[156:159], a[92:95], a[60:63], v[156:159]\n"
      "v_exp_f32 v138, v138\n"
      "v_mfma_f32_16x16x32_bf16 a[128:131], a[144:147], v[160:163], a[128:131]\n"
      "ds_read_b128 a[76:79], v208 offset:26112\n"
      "v_cvt_pk_bf16_f32 v224, v221, v224\n"
      "s_waitcnt lgkmcnt(12)\n"
      "v_mfma_f32_16x16x32_bf16 v[0:3], a[96:99], v[160:163], v[0:3]\n"
      "v_exp_f32 v139, v139\n"
      "v_mfma_f32_16x16x32_bf16 v[4:7], a[100:103], v[160:163], v[4:7]\n"
      "ds_read_b128 a[80:83], v208 offset:28672\n"
      "v_cvt_pk_bf16_f32 v137, v138, v139\n"
      "v_mfma_f32_16x16x32_bf16 v[8:11], a[104:107], v[160:163], v[8:11]\n"
      "v_exp_f32 v140, v140\n"
      "v_mfma_f32_16x16x32_bf16 v[12:15], a[108:111], v[160:163], v[12:15]\n"
      "ds_read_b128 a[84:87], v208 offset:29184\n"
      "ds_read_b128 a[88:91], v208 offset:29696\n"
      "s_waitcnt lgkmcnt(7)\n"
      "v_mfma_f32_16x16x32_bf16 v[16:19], a[112:115], v[160:163], v[16:19]\n"
      "v_exp_f32 v141, v141\n"
      "v_mfma_f32_16x16x32_bf16 v[20:23], a[116:119], v[160:163], v[20:23]\n"
      "ds_read_b128 a[92:95], v208 offset:30208\n"
      "v_cvt_pk_bf16_f32 v138, v140, v141\n"
      "v_mfma_f32_16x16x32_bf16 v[24:27], a[120:123], v[160:163], v[24:27]\n"
      "v_exp_f32 v142, v142\n"
      "v_mfma_f32_16x16x32_bf16 v[28:31], a[124:127], v[160:163], v[28:31]\n"
      "buffer_store_dwordx4 a[152:155], v214, s[52:55], s77 offen offset:1024 sc1\n"
      "v_mfma_f32_16x16x32_bf16 a[132:135], a[144:147], v[168:171], a[132:135]\n"
      "v_exp_f32 v143, v143\n"
      "v_mfma_f32_16x16x32_bf16 v[32:35], a[96:99], v[168:171], v[32:35]\n"
      "s_add_u32 m0, s58, 0x4000\n"
      "v_cvt_pk_bf16_f32 v139, v142, v143\n"
      "v_accvgpr_write_b32 a184, v224\n"
      "v_mfma_f32_16x16x32_bf16 v[36:39], a[100:103], v[168:171], v[36:39]\n"
      "s_add_u32 s68, s57, 0x14000\n"
      "v_exp_f32 v144, v144\n"
      "v_mfma_f32_16x16x32_bf16 v[40:43], a[104:107], v[168:171], v[40:43]\n"
      "buffer_load_dwordx4 v212, s[44:47], s68 offen nt lds\n"
      "v_lshlrev_b32 v222, 16, v225\n"
      "v_mfma_f32_16x16x32_bf16 v[44:47], a[108:111], v[168:171], v[44:47]\n"
      "v_exp_f32 v145, v145\n"
      "v_mfma_f32_16x16x32_bf16 v[48:51], a[112:115], v[168:171], v[48:51]\n"
      "buffer_load_dwordx4 v213, s[44:47], s68 offen offset:1024 nt lds\n"
      "v_cvt_pk_bf16_f32 v144, v144, v145\n"
      "v_mfma_f32_16x16x32_bf16 v[52:55], a[116:119], v[168:171], v[52:55]\n"
      "v_exp_f32 v146, v146\n"
      "v_mfma_f32_16x16x32_bf16 v[56:59], a[120:123], v[168:171], v[56:59]\n"
      "v_and_b32 v225, s62, v225\n"
      "v_mul_f32 v222, s61, v222\n"
      "v_mfma_f32_16x16x32_bf16 v[60:63], a[124:127], v[168:171], v[60:63]\n"
      "v_exp_f32 v147, v147\n"
      "v_mfma_f32_16x16x32_bf16 a[136:139], a[144:147], v[176:179], a[136:139]\n"
      "v_cvt_pk_bf16_f32 v145, v146, v147\n"
      "v_mul_f32 v225, s61, v225\n"
      "v_mfma_f32_16x16x32_bf16 v[64:67], a[96:99], v[176:179], v[64:67]\n"
      "v_exp_f32 v148, v148\n"
      "v_mfma_f32_16x16x32_bf16 v[68:71], a[100:103], v[176:179], v[68:71]\n"
      "v_cvt_pk_bf16_f32 v225, v222, v225\n"
      "v_accvgpr_write_b32 a185, v225\n"
      "v_mfma_f32_16x16x32_bf16 v[72:75], a[104:107], v[176:179], v[72:75]\n"
      "v_exp_f32 v149, v149\n"
      "v_mfma_f32_16x16x32_bf16 v[76:79], a[108:111], v[176:179], v[76:79]\n"
      "v_cvt_pk_bf16_f32 v146, v148, v149\n"
      "v_lshlrev_b32 v221, 16, v226\n"
      "v_mfma_f32_16x16x32_bf16 v[80:83], a[112:115], v[176:179], v[80:83]\n"
      "v_exp_f32 v150, v150\n"
      "v_mfma_f32_16x16x32_bf16 v[84:87], a[116:119], v[176:179], v[84:87]\n"
      "s_add_u32 m0, s58, 0x8000\n"
      "ds_read_b128 v[228:231], v209 offset:5632\n"
      "v_and_b32 v226, s62, v226\n"
      "v_mfma_f32_16x16x32_bf16 v[88:91], a[120:123], v[176:179], v[88:91]\n"
      "s_add_u32 s67, s57, 0x10000\n"
      "v_exp_f32 v151, v151\n"
      "v_mfma_f32_16x16x32_bf16 v[92:95], a[124:127], v[176:179], v[92:95]\n"
      "buffer_load_dwordx4 v212, s[48:51], s67 offen nt lds\n"
      "v_cvt_pk_bf16_f32 v147, v150, v151\n"
      "v_mfma_f32_16x16x32_bf16 a[140:143], a[144:147], v[184:187], a[140:143]\n"
      "v_exp_f32 v152, v152\n"
      "v_mfma_f32_16x16x32_bf16 v[96:99], a[96:99], v[184:187], v[96:99]\n"
      "buffer_load_dwordx4 v213, s[48:51], s67 offen offset:1024 nt lds\n"
      "v_mul_f32 v221, s61, v221\n"
      "v_mfma_f32_16x16x32_bf16 v[100:103], a[100:103], v[184:187], v[100:103]\n"
      "v_exp_f32 v153, v153\n"
      "v_mfma_f32_16x16x32_bf16 v[104:107], a[104:107], v[184:187], v[104:107]\n"
      "v_cvt_pk_bf16_f32 v152, v152, v153\n"
      "v_mul_f32 v226, s61, v226\n"
      "v_mfma_f32_16x16x32_bf16 v[108:111], a[108:111], v[184:187], v[108:111]\n"
      "v_exp_f32 v154, v154\n"
      "v_mfma_f32_16x16x32_bf16 v[112:115], a[112:115], v[184:187], v[112:115]\n"
      "v_cvt_pk_bf16_f32 v226, v221, v226\n"
      "v_accvgpr_write_b32 a186, v226\n"
      "v_mfma_f32_16x16x32_bf16 v[116:119], a[116:119], v[184:187], v[116:119]\n"
      "v_lshlrev_b32 v222, 16, v227\n"
      "v_and_b32 v227, s62, v227\n"
      "v_mfma_f32_16x16x32_bf16 v[120:123], a[120:123], v[184:187], v[120:123]\n"
      "s_memrealtime s[84:85]\n"
      "v_mul_f32 v222, s61, v222\n"
      "v_mul_f32 v227, s61, v227\n"
      "v_mfma_f32_16x16x32_bf16 v[124:127], a[124:127], v[184:187], v[124:127]\n"
      "v_cvt_pk_bf16_f32 v227, v222, v227\n"
      "v_accvgpr_write_b32 a187, v227\n"
      "s_waitcnt lgkmcnt(0)\n"
      "s_sub_u32 s85, s84, s86\n"
      "s_cmp_ge_u32 s85, s87\n"
      "s_cbranch_scc1 L_PACED0_6\n"
      "L_PACE0_6:\n"
      "s_memrealtime s[84:85]\n"
      "s_waitcnt lgkmcnt(0)\n"
      "s_sub_u32 s85, s84, s86\n"
      "s_cmp_lt_u32 s85, s87\n"
      "s_cbranch_scc1 L_PACE0_6\n"
      "L_PACED0_6:\n"
      "s_mov_b32 s86, s84\n"
      "v_mfma_f32_16x16x32_bf16 v[160:163], a[64:67], a[0:3], v[192:195]\n"
      "s_waitcnt vmcnt(10)\n"
      "s_barrier\n"
      "v_lshlrev_b32 v221, 16, v228\n"
      "v_and_b32 v228, s62, v228\n"
      "v_mul_f32 v221, s61, v221\n"
      "v_mfma_f32_16x16x32_bf16 v[160:163], a[68:71], a[4:7], v[160:163]\n"
      "v_exp_f32 v155, v155\n"
      "v_mfma_f32_16x16x32_bf16 v[160:163], a[72:75], a[8:11], v[160:163]\n"
      "ds_read_b128 a[148:151], v219 offset:14336\n"
      "v_cvt_pk_bf16_f32 v153, v154, v155\n"
      "v_mfma_f32_16x16x32_bf16 v[160:163], a[76:79], a[12:15], v[160:163]\n"
      "v_exp_f32 v156, v156\n"
      "v_mfma_f32_16x16x32_bf16 v[164:167], a[80:83], a[0:3], v[192:195]\n"
      "ds_read_b64_tr_b16 a[96:97], v210 offset:49152\n"
      "ds_read_b64_tr_b16 a[98:99], v210 offset:53248\n"
      "v_mfma_f32_16x16x32_bf16 v[164:167], a[84:87], a[4:7], v[164:167]\n"
      "v_exp_f32 v157, v157\n"
      "v_mfma_f32_16x16x32_bf16 v[164:167], a[88:91], a[8:11], v[164:167]\n"
      "ds_read_b64_tr_b16 a[100:101], v211 offset:49152\n"
      "v_cvt_pk_bf16_f32 v154, v156, v157\n"
      "v_mfma_f32_16x16x32_bf16 v[164:167], a[92:95], a[12:15], v[164:167]\n"
      "v_exp_f32 v158, v158\n"
      "v_mfma_f32_16x16x32_bf16 v[168:171], a[64:67], a[16:19], v[196:199]\n"
      "ds_read_b64_tr_b16 a[102:103], v211 offset:53248\n"
      "ds_read_b64_tr_b16 a[104:105], v210 offset:49664\n"
      "v_mfma_f32_16x16x32_bf16 v[168:171], a[68:71], a[20:23], v[168:171]\n"
      "v_exp_f32 v159, v159\n"
      "v_mfma_f32_16x16x32_bf16 v[168:171], a[72:75], a[24:27], v[168:171]\n"
      "ds_read_b64_tr_b16 a[106:107], v210 offset:53760\n"
      "v_cvt_pk_bf16_f32 v155, v158, v159\n"
      "v_mfma_f32_16x16x32_bf16 v[168:171], a[76:79], a[28:31], v[168:171]\n"
      "v_exp_f32 v160, v160\n"
      "v_mfma_f32_16x16x32_bf16 v[172:175], a[80:83], a[16:19], v[196:199]\n"
      "ds_read_b64_tr_b16 a[108:109], v211 offset:49664\n"
      "ds_read_b64_tr_b16 a[110:111], v211 offset:53760\n"
      "v_mfma_f32_16x16x32_bf16 v[172:175], a[84:87], a[20:23], v[172:175]\n"
      "v_exp_f32 v161, v161\n"
      "v_mfma_f32_16x16x32_bf16 v[172:175], a[88:91], a[24:27], v[172:175]\n"
      "ds_read_b64_tr_b16 a[112:113], v210 offset:50176\n"
      "v_cvt_pk_bf16_f32 v160, v160, v161\n"
      "v_mfma_f32_16x16x32_bf16 v[172:175], a[92:95], a[28:31], v[172:175]\n"
      "v_exp_f32 v162, v162\n"
      "v_mfma_f32_16x16x32_bf16 v[176:179], a[64:67], a[32:35], v[200:203]\n"
      "ds_read_b64_tr_b16 a[114:115], v210 offset:54272\n"
      "ds_read_b64_tr_b16 a[116:117], v211 offset:50176\n"
      "v_mfma_f32_16x16x32_bf16 v[176:179], a[68:71], a[36:39], v[176:179]\n"
      "v_exp_f32 v163, v163\n"
      "v_mfma_f32_16x16x32_bf16 v[176:179], a[72:75], a[40:43], v[176:179]\n"
      "ds_read_b64_tr_b16 a[118:119], v211 offset:54272\n"
      "v_cvt_pk_bf16_f32 v161, v162, v163\n"
      "v_mfma_f32_16x16x32_bf16 v[176:179], a[76:79], a[44:47], v[176:179]\n"
      "v_exp_f32 v164, v164\n"
      "v_mfma_f32_16x16x32_bf16 v[180:183], a[80:83], a[32:35], v[200:203]\n"
      "ds_read_b64_tr_b16 a[120:121], v210 offset:50688\n"
      "ds_read_b64_tr_b16 a[122:123], v210 offset:54784\n"
      "v_mfma_f32_16x16x32_bf16 v[180:183], a[84:87], a[36:39], v[180:183]\n"
      "v_exp_f32 v165, v165\n"
      "v_mfma_f32_16x16x32_bf16 v[180:183], a[88:91], a[40:43], v[180:183]\n"
      "ds_read_b64_tr_b16 a[124:125], v211 offset:50688\n"
      "v_cvt_pk_bf16_f32 v162, v164, v165\n"
      "v_mfma_f32_16x16x32_bf16 v[180:183], a[92:95], a[44:47], v[180:183]\n"
      "v_exp_f32 v166, v166\n"
      "v_mfma_f32_16x16x32_bf16 v[184:187], a[64:67], a[48:51], v[204:207]\n"
      "ds_read_b64_tr_b16 a[126:127], v211 offset:54784\n"
      "v_mul_f32 v228, s61, v228\n"
      "v_mfma_f32_16x16x32_bf16 v[184:187], a[68:71], a[52:55], v[184:187]\n"
      "v_exp_f32 v167, v167\n"
      "v_mfma_f32_16x16x32_bf16 v[184:187], a[72:75], a[56:59], v[184:187]\n"
      "v_cvt_pk_bf16_f32 v163, v166, v167\n"
      "v_cvt_pk_bf16_f32 v228, v221, v228\n"
      "v_mfma_f32_16x16x32_bf16 v[184:187], a[76:79], a[60:63], v[184:187]\n"
      "v_exp_f32 v168, v168\n"
      "v_mfma_f32_16x16x32_bf16 v[188:191], a[80:83], a[48:51], v[204:207]\n"
      "ds_read_b128 a[64:67], v208 offset:0\n"
      "ds_read_b128 a[68:71], v208 offset:512\n"
      "v_mfma_f32_16x16x32_bf16 v[188:191], a[84:87], a[52:55], v[188:191]\n"
      "v_exp_f32 v169, v169\n"
      "v_mfma_f32_16x16x32_bf16 v[188:191], a[88:91], a[56:59], v[188:191]\n"
      "ds_read_b128 a[72:75], v208 offset:1024\n"
      "v_cvt_pk_bf16_f32 v168, v168, v169\n"
      "v_mfma_f32_16x16x32_bf16 v[188:191], a[92:95], a[60:63], v[188:191]\n"
      "v_exp_f32 v170, v170\n"
      "v_mfma_f32_16x16x32_bf16 a[128:131], a[144:147], v[128:131], a[128:131]\n"
      "ds_read_b128 a[76:79], v208 offset:1536\n"
      "v_accvgpr_write_b32 a188, v228\n"
      "s_waitcnt lgkmcnt(12)\n"
      "v_mfma_f32_16x16x32_bf16 v[0:3], a[96:99], v[128:131], v[0:3]\n"
      "v_exp_f32 v171, v171\n"
      "v_mfma_f32_16x16x32_bf16 v[4:7], a[100:103], v[128:131], v[4:7]\n"
      "ds_read_b128 a[80:83], v208 offset:4096\n"
      "v_cvt_pk_bf16_f32 v169, v170, v171\n"
      "v_mfma_f32_16x16x32_bf16 v[8:11], a[104:107], v[128:131], v[8:11]\n"
      "v_exp_f32 v172, v172\n"
      "v_mfma_f32_16x16x32_bf16 v[12:15], a[108:111], v[128:131], v[12:15]\n"
      "ds_read_b128 a[84:87], v208 offset:4608\n"
      "ds_read_b128 a[88:91], v208 offset:5120\n"
      "v_lshlrev_b32 v222, 16, v229\n"
      "s_waitcnt lgkmcnt(7)\n"
      "v_mfma_f32_16x16x32_bf16 v[16:19], a[112:115], v[128:131], v[16:19]\n"
      "v_exp_f32 v173, v173\n"
      "v_mfma_f32_16x16x32_bf16 v[20:23], a[116:119], v[128:131], v[20:23]\n"
      "ds_read_b128 a[92:95], v208 offset:5632\n"
      "v_cvt_pk_bf16_f32 v170, v172, v173\n"
      "v_mfma_f32_16x16x32_bf16 v[24:27], a[120:123], v[128:131], v[24:27]\n"
      "v_exp_f32 v174, v174\n"
      "v_mfma_f32_16x16x32_bf16 v[28:31], a[124:127], v[128:131], v[28:31]\n"
      "s_add_u32 m0, s58, 0x6000\n"
      "buffer_store_dwordx4 a[148:151], v214, s[52:55], s77 offen offset:2048 sc1\n"
      "v_mfma_f32_16x16x32_bf16 a[132:135], a[144:147], v[136:139], a[132:135]\n"
      "s_add_u32 s67, s57, 0x16000\n"
      "v_exp_f32 v175, v175\n"
      "v_mfma_f32_16x16x32_bf16 v[32:35], a[96:99], v[136:139], v[32:35]\n"
      "buffer_load_dwordx4 v212, s[44:47], s67 offen nt lds\n"
      "v_cvt_pk_bf16_f32 v171, v174, v175\n"
      "v_mfma_f32_16x16x32_bf16 v[36:39], a[100:103], v[136:139], v[36:39]\n"
      "v_exp_f32 v176, v176\n"
      "v_mfma_f32_16x16x32_bf16 v[40:43], a[104:107], v[136:139], v[40:43]\n"
      "buffer_load_dwordx4 v213, s[44:47], s67 offen offset:1024 nt lds\n"
      "v_and_b32 v229, s62, v229\n"
      "v_mfma_f32_16x16x32_bf16 v[44:47], a[108:111], v[136:139], v[44:47]\n"
      "v_exp_f32 v177, v177\n"
      "v_mfma_f32_16x16x32_bf16 v[48:51], a[112:115], v[136:139], v[48:51]\n"
      "v_cvt_pk_bf16_f32 v176, v176, v177\n"
      "v_mul_f32 v222, s61, v222\n"
      "v_mfma_f32_16x16x32_bf16 v[52:55], a[116:119], v[136:139], v[52:55]\n"
      "v_exp_f32 v178, v178\n"
      "v_mfma_f32_16x16x32_bf16 v[56:59], a[120:123], v[136:139], v[56:59]\n"
      "v_mul_f32 v229, s61, v229\n"
      "v_cvt_pk_bf16_f32 v229, v222, v229\n"
      "v_mfma_f32_16x16x32_bf16 v[60:63], a[124:127], v[136:139], v[60:63]\n"
      "v_exp_f32 v179, v179\n"
      "v_mfma_f32_16x16x32_bf16 a[136:139], a[144:147], v[144:147], a[136:139]\n"
      "s_add_u32 m0, s58, 0xa000\n"
      "v_cvt_pk_bf16_f32 v177, v178, v179\n"
      "v_accvgpr_write_b32 a189, v229\n"
      "v_mfma_f32_16x16x32_bf16 v[64:67], a[96:99], v[144:147], v[64:67]\n"
      "s_add_u32 s68, s57, 0x12000\n"
      "v_exp_f32 v180, v180\n"
      "v_mfma_f32_16x16x32_bf16 v[68:71], a[100:103], v[144:147], v[68:71]\n"
      "buffer_load_dwordx4 v212, s[48:51], s68 offen nt lds\n"
      "v_lshlrev_b32 v221, 16, v230\n"
      "v_mfma_f32_16x16x32_bf16 v[72:75], a[104:107], v[144:147], v[72:75]\n"
      "v_exp_f32 v181, v181\n"
      "v_mfma_f32_16x16x32_bf16 v[76:79], a[108:111], v[144:147], v[76:79]\n"
      "buffer_load_dwordx4 v213, s[48:51], s68 offen offset:1024 nt lds\n"
      "v_cvt_pk_bf16_f32 v178, v180, v181\n"
      "v_mfma_f32_16x16x32_bf16 v[80:83], a[112:115], v[144:147], v[80:83]\n"
      "v_exp_f32 v182, v182\n"
      "v_mfma_f32_16x16x32_bf16 v[84:87], a[116:119], v[144:147], v[84:87]\n"
      "ds_read_b128 v[224:227], v209 offset:0\n"
      "v_and_b32 v230, s62, v230\n"
      "v_mfma_f32_16x16x32_bf16 v[88:91], a[120:123], v[144:147], v[88:91]\n"
      "v_exp_f32 v183, v183\n"
      "v_mfma_f32_16x16x32_bf16 v[92:95], a[124:127], v[144:147], v[92:95]\n"
      "s_add_u32 m0, s59, 0x1000\n"
      "v_cvt_pk_bf16_f32 v179, v182, v183\n"
      "v_mul_f32 v221, s61, v221\n"
      "v_mfma_f32_16x16x32_bf16 a[140:143], a[144:147], v[152:155], a[140:143]\n"
      "s_add_u32 s68, s56, 0x1003000\n"
      "v_exp_f32 v184, v184\n"
      "v_mfma_f32_16x16x32_bf16 v[96:99], a[96:99], v[152:155], v[96:99]\n"
      "buffer_load_dwordx4 v212, s[40:43], s68 offen nt lds\n"
      "v_mul_f32 v230, s61, v230\n"
      "v_mfma_f32_16x16x32_bf16 v[100:103], a[100:103], v[152:155], v[100:103]\n"
      "v_exp_f32 v185, v185\n"
      "v_mfma_f32_16x16x32_bf16 v[104:107], a[104:107], v[152:155], v[104:107]\n"
      "buffer_load_dwordx4 v213, s[40:43], s68 offen offset:1024 nt lds\n"
      "v_cvt_pk_bf16_f32 v184, v184, v185\n"
      "v_mfma_f32_16x16x32_bf16 v[108:111], a[108:111], v[152:155], v[108:111]\n"
      "v_exp_f32 v186, v186\n"
      "v_mfma_f32_16x16x32_bf16 v[112:115], a[112:115], v[152:155], v[112:115]\n"
      "v_cvt_pk_bf16_f32 v230, v221, v230\n"
      "v_accvgpr_write_b32 a190, v230\n"
      "v_mfma_f32_16x16x32_bf16 v[116:119], a[116:119], v[152:155], v[116:119]\n"
      "v_lshlrev_b32 v222, 16, v231\n"
      "v_and_b32 v231, s62, v231\n"
      "v_mfma_f32_16x16x32_bf16 v[120:123], a[120:123], v[152:155], v[120:123]\n"
      "v_mul_f32 v222, s61, v222\n"
      "v_mul_f32 v231, s61, v231\n"
      "v_mfma_f32_16x16x32_bf16 v[124:127], a[124:127], v[152:155], v[124:127]\n"
      "v_cvt_pk_bf16_f32 v231, v222, v231\n"
      "v_accvgpr_write_b32 a191, v231\n"
      "s_waitcnt lgkmcnt(0)\n"
      "v_mfma_f32_16x16x32_bf16 v[128:131], a[64:67], a[0:3], v[192:195]\n"
      "s_waitcnt vmcnt(12)\n"
      "s_barrier\n"
      "v_lshlrev_b32 v221, 16, v224\n"
      "v_and_b32 v224, s62, v224\n"
      "v_mul_f32 v221, s61, v221\n"
      "v_mfma_f32_16x16x32_bf16 v[128:131], a[68:71], a[4:7], v[128:131]\n"
      "v_exp_f32 v187, v187\n"
      "v_mfma_f32_16x16x32_bf16 v[128:131], a[72:75], a[8:11], v[128:131]\n"
      "ds_read_b128 a[152:155], v220 offset:15360\n"
      "v_cvt_pk_bf16_f32 v185, v186, v187\n"
      "v_mfma_f32_16x16x32_bf16 v[128:131], a[76:79], a[12:15], v[128:131]\n"
      "v_exp_f32 v188, v188\n"
      "v_mfma_f32_16x16x32_bf16 v[132:135], a[80:83], a[0:3], v[192:195]\n"
      "ds_read_b64_tr_b16 a[96:97], v210 offset:57344\n"
      "ds_read_b64_tr_b16 a[98:99], v210 offset:61440\n"
      "v_mfma_f32_16x16x32_bf16 v[132:135], a[84:87], a[4:7], v[132:135]\n"
      "v_exp_f32 v189, v189\n"
      "v_mfma_f32_16x16x32_bf16 v[132:135], a[88:91], a[8:11], v[132:135]\n"
      "ds_read_b64_tr_b16 a[100:101], v211 offset:57344\n"
      "v_cvt_pk_bf16_f32 v186, v188, v189\n"
      "v_mfma_f32_16x16x32_bf16 v[132:135], a[92:95], a[12:15], v[132:135]\n"
      "v_exp_f32 v190, v190\n"
      "v_mfma_f32_16x16x32_bf16 v[136:139], a[64:67], a[16:19], v[196:199]\n"
      "ds_read_b64_tr_b16 a[102:103], v211 offset:61440\n"
      "ds_read_b64_tr_b16 a[104:105], v210 offset:57856\n"
      "v_mfma_f32_16x16x32_bf16 v[136:139], a[68:71], a[20:23], v[136:139]\n"
      "v_exp_f32 v191, v191\n"
      "v_mfma_f32_16x16x32_bf16 v[136:139], a[72:75], a[24:27], v[136:139]\n"
      "ds_read_b64_tr_b16 a[106:107], v210 offset:61952\n"
      "v_cvt_pk_bf16_f32 v187, v190, v191\n"
      "v_mfma_f32_16x16x32_bf16 v[136:139], a[76:79], a[28:31], v[136:139]\n"
      "v_exp_f32 v128, v128\n"
      "v_mfma_f32_16x16x32_bf16 v[140:143], a[80:83], a[16:19], v[196:199]\n"
      "ds_read_b64_tr_b16 a[108:109], v211 offset:57856\n"
      "ds_read_b64_tr_b16 a[110:111], v211 offset:61952\n"
      "v_mfma_f32_16x16x32_bf16 v[140:143], a[84:87], a[20:23], v[140:143]\n"
      "v_exp_f32 v129, v129\n"
      "v_mfma_f32_16x16x32_bf16 v[140:143], a[88:91], a[24:27], v[140:143]\n"
      "ds_read_b64_tr_b16 a[112:113], v210 offset:58368\n"
      "v_cvt_pk_bf16_f32 v128, v128, v129\n"
      "v_mfma_f32_16x16x32_bf16 v[140:143], a[92:95], a[28:31], v[140:143]\n"
      "v_exp_f32 v130, v130\n"
      "v_mfma_f32_16x16x32_bf16 v[144:147], a[64:67], a[32:35], v[200:203]\n"
      "ds_read_b64_tr_b16 a[114:115], v210 offset:62464\n"
      "ds_read_b64_tr_b16 a[116:117], v211 offset:58368\n"
      "v_mfma_f32_16x16x32_bf16 v[144:147], a[68:71], a[36:39], v[144:147]\n"
      "v_exp_f32 v131, v131\n"
      "v_mfma_f32_16x16x32_bf16 v[144:147], a[72:75], a[40:43], v[144:147]\n"
      "ds_read_b64_tr_b16 a[118:119], v211 offset:62464\n"
      "v_cvt_pk_bf16_f32 v129, v130, v131\n"
      "v_mfma_f32_16x16x32_bf16 v[144:147], a[76:79], a[44:47], v[144:147]\n"
      "v_exp_f32 v132, v132\n"
      "v_mfma_f32_16x16x32_bf16 v[148:151], a[80:83], a[32:35], v[200:203]\n"
      "ds_read_b64_tr_b16 a[120:121], v210 offset:58880\n"
      "ds_read_b64_tr_b16 a[122:123], v210 offset:62976\n"
      "v_mfma_f32_16x16x32_bf16 v[148:151], a[84:87], a[36:39], v[148:151]\n"
      "v_exp_f32 v133, v133\n"
      "v_mfma_f32_16x16x32_bf16 v[148:151], a[88:91], a[40:43], v[148:151]\n"
      "ds_read_b64_tr_b16 a[124:125], v211 offset:58880\n"
      "v_cvt_pk_bf16_f32 v130, v132, v133\n"
      "v_mfma_f32_16x16x32_bf16 v[148:151], a[92:95], a[44:47], v[148:151]\n"
      "v_exp_f32 v134, v134\n"
      "v_mfma_f32_16x16x32_bf16 v[152:155], a[64:67], a[48:51], v[204:207]\n"
      "ds_read_b64_tr_b16 a[126:127], v211 offset:62976\n"
      "v_mul_f32 v224, s61, v224\n"
      "v_mfma_f32_16x16x32_bf16 v[152:155], a[68:71], a[52:55], v[152:155]\n"
      "v_exp_f32 v135, v135\n"
      "v_mfma_f32_16x16x32_bf16 v[152:155], a[72:75], a[56:59], v[152:155]\n"
      "v_cvt_pk_bf16_f32 v131, v134, v135\n"
      "v_cvt_pk_bf16_f32 v224, v221, v224\n"
      "v_mfma_f32_16x16x32_bf16 v[152:155], a[76:79], a[60:63], v[152:155]\n"
      "v_exp_f32 v136, v136\n"
      "v_mfma_f32_16x16x32_bf16 v[156:159], a[80:83], a[48:51], v[204:207]\n"
      "ds_read_b128 a[64:67], v208 offset:8192\n"
      "ds_read_b128 a[68:71], v208 offset:8704\n"
      "v_mfma_f32_16x16x32_bf16 v[156:159], a[84:87], a[52:55], v[156:159]\n"
      "v_exp_f32 v137, v137\n"
      "v_mfma_f32_16x16x32_bf16 v[156:159], a[88:91], a[56:59], v[156:159]\n"
      "ds_read_b128 a[72:75], v208 offset:9216\n"
      "v_cvt_pk_bf16_f32 v136, v136, v137\n"
      "v_mfma_f32_16x16x32_bf16 v[156:159], a[92:95], a[60:63], v[156:159]\n"
      "v_exp_f32 v138, v138\n"
      "v_mfma_f32_16x16x32_bf16 a[128:131], a[144:147], v[160:163], a[128:131]\n"
      "ds_read_b128 a[76:79], v208 offset:9728\n"
      "v_accvgpr_write_b32 a192, v224\n"
      "s_waitcnt lgkmcnt(12)\n"
      "v_mfma_f32_16x16x32_bf16 v[0:3], a[96:99], v[160:163], v[0:3]\n"
      "v_exp_f32 v139, v139\n"
      "v_mfma_f32_16x16x32_bf16 v[4:7], a[100:103], v[160:163], v[4:7]\n"
      "ds_read_b128 a[80:83], v208 offset:12288\n"
      "v_cvt_pk_bf16_f32 v137, v138, v139\n"
      "v_mfma_f32_16x16x32_bf16 v[8:11], a[104:107], v[160:163], v[8:11]\n"
      "v_exp_f32 v140, v140\n"
      "v_mfma_f32_16x16x32_bf16 v[12:15], a[108:111], v[160:163], v[12:15]\n"
      "ds_read_b128 a[84:87], v208 offset:12800\n"
      "ds_read_b128 a[88:91], v208 offset:13312\n"
      "v_lshlrev_b32 v222, 16, v225\n"
      "s_waitcnt lgkmcnt(7)\n"
      "v_mfma_f32_16x16x32_bf16 v[16:19], a[112:115], v[160:163], v[16:19]\n"
      "v_exp_f32 v141, v141\n"
      "v_mfma_f32_16x16x32_bf16 v[20:23], a[116:119], v[160:163], v[20:23]\n"
      "ds_read_b128 a[92:95], v208 offset:13824\n"
      "v_cvt_pk_bf16_f32 v138, v140, v141\n"
      "v_mfma_f32_16x16x32_bf16 v[24:27], a[120:123], v[160:163], v[24:27]\n"
      "v_exp_f32 v142, v142\n"
      "v_mfma_f32_16x16x32_bf16 v[28:31], a[124:127], v[160:163], v[28:31]\n"
      "s_mov_b32 m0, s58\n"
      "buffer_store_dwordx4 a[152:155], v214, s[52:55], s77 offen offset:3072 sc1\n"
      "v_mfma_f32_16x16x32_bf16 a[132:135], a[144:147], v[168:171], a[132:135]\n"
      "s_add_u32 s68, s57, 0x18000\n"
      "v_exp_f32 v143, v143\n"
      "v_mfma_f32_16x16x32_bf16 v[32:35], a[96:99], v[168:171], v[32:35]\n"
      "buffer_load_dwordx4 v212, s[44:47], s68 offen nt lds\n"
      "v_cvt_pk_bf16_f32 v139, v142, v143\n"
      "v_mfma_f32_16x16x32_bf16 v[36:39], a[100:103], v[168:171], v[36:39]\n"
      "v_exp_f32 v144, v144\n"
      "v_mfma_f32_16x16x32_bf16 v[40:43], a[104:107], v[168:171], v[40:43]\n"
      "buffer_load_dwordx4 v213, s[44:47], s68 offen offset:1024 nt lds\n"
      "v_and_b32 v225, s62, v225\n"
      "v_mfma_f32_16x16x32_bf16 v[44:47], a[108:111], v[168:171], v[44:47]\n"
      "v_exp_f32 v145, v145\n"
      "v_mfma_f32_16x16x32_bf16 v[48:51], a[112:115], v[168:171], v[48:51]\n"
      "v_cvt_pk_bf16_f32 v144, v144, v145\n"
      "v_mul_f32 v222, s61, v222\n"
      "v_mfma_f32_16x16x32_bf16 v[52:55], a[116:119], v[168:171], v[52:55]\n"
      "v_exp_f32 v146, v146\n"
      "v_mfma_f32_16x16x32_bf16 v[56:59], a[120:123], v[168:171], v[56:59]\n"
      "v_mul_f32 v225, s61, v225\n"
      "v_cvt_pk_bf16_f32 v225, v222, v225\n"
      "v_mfma_f32_16x16x32_bf16 v[60:63], a[124:127], v[168:171], v[60:63]\n"
      "v_exp_f32 v147, v147\n"
      "v_mfma_f32_16x16x32_bf16 a[136:139], a[144:147], v[176:179], a[136:139]\n"
      "s_add_u32 m0, s58, 0xc000\n"
      "v_cvt_pk_bf16_f32 v145, v146, v147\n"
      "v_accvgpr_write_b32 a193, v225\n"
      "v_mfma_f32_16x16x32_bf16 v[64:67], a[96:99], v[176:179], v[64:67]\n"
      "s_add_u32 s67, s57, 0x14000\n"
      "v_exp_f32 v148, v148\n"
      "v_mfma_f32_16x16x32_bf16 v[68:71], a[100:103], v[176:179], v[68:71]\n"
      "buffer_load_dwordx4 v212, s[48:51], s67 offen nt lds\n"
      "v_lshlrev_b32 v221, 16, v226\n"
      "v_mfma_f32_16x16x32_bf16 v[72:75], a[104:107], v[176:179], v[72:75]\n"
      "v_exp_f32 v149, v149\n"
      "v_mfma_f32_16x16x32_bf16 v[76:79], a[108:111], v[176:179], v[76:79]\n"
      "buffer_load_dwordx4 v213, s[48:51], s67 offen offset:1024 nt lds\n"
      "v_cvt_pk_bf16_f32 v146, v148, v149\n"
      "v_mfma_f32_16x16x32_bf16 v[80:83], a[112:115], v[176:179], v[80:83]\n"
      "v_exp_f32 v150, v150\n"
      "v_mfma_f32_16x16x32_bf16 v[84:87], a[116:119], v[176:179], v[84:87]\n"
      "ds_read_b128 v[228:231], v209 offset:512\n"
      "v_and_b32 v226, s62, v226\n"
      "v_mfma_f32_16x16x32_bf16 v[88:91], a[120:123], v[176:179], v[88:91]\n"
      "v_exp_f32 v151, v151\n"
      "v_mfma_f32_16x16x32_bf16 v[92:95], a[124:127], v[176:179], v[92:95]\n"
      "s_add_u32 m0, s59, 0x1800\n"
      "v_cvt_pk_bf16_f32 v147, v150, v151\n"
      "v_mul_f32 v221, s61, v221\n"
      "v_mfma_f32_16x16x32_bf16 a[140:143], a[144:147], v[184:187], a[140:143]\n"
      "s_add_u32 s67, s56, 0x1003800\n"
      "v_exp_f32 v152, v152\n"
      "v_mfma_f32_16x16x32_bf16 v[96:99], a[96:99], v[184:187], v[96:99]\n"
      "buffer_load_dwordx4 v212, s[40:43], s67 offen nt lds\n"
      "v_mul_f32 v226, s61, v226\n"
      "v_mfma_f32_16x16x32_bf16 v[100:103], a[100:103], v[184:187], v[100:103]\n"
      "v_exp_f32 v153, v153\n"
      "v_mfma_f32_16x16x32_bf16 v[104:107], a[104:107], v[184:187], v[104:107]\n"
      "buffer_load_dwordx4 v213, s[40:43], s67 offen offset:1024 nt lds\n"
      "v_cvt_pk_bf16_f32 v152, v152, v153\n"
      "v_mfma_f32_16x16x32_bf16 v[108:111], a[108:111], v[184:187], v[108:111]\n"
      "v_exp_f32 v154, v154\n"
      "v_mfma_f32_16x16x32_bf16 v[112:115], a[112:115], v[184:187], v[112:115]\n"
      "v_cvt_pk_bf16_f32 v226, v221, v226\n"
      "v_accvgpr_write_b32 a194, v226\n"
      "v_mfma_f32_16x16x32_bf16 v[116:119], a[116:119], v[184:187], v[116:119]\n"
      "v_lshlrev_b32 v222, 16, v227\n"
      "v_and_b32 v227, s62, v227\n"
      "v_mfma_f32_16x16x32_bf16 v[120:123], a[120:123], v[184:187], v[120:123]\n"
      "s_memrealtime s[84:85]\n"
      "v_mul_f32 v222, s61, v222\n"
      "v_mul_f32 v227, s61, v227\n"
      "v_mfma_f32_16x16x32_bf16 v[124:127], a[124:127], v[184:187], v[124:127]\n"
      "v_cvt_pk_bf16_f32 v227, v222, v227\n"
      "v_accvgpr_write_b32 a195, v227\n"
      "s_waitcnt lgkmcnt(0)\n"
      "s_sub_u32 s85, s84, s86\n"
      "s_cmp_ge_u32 s85, s87\n"
      "s_cbranch_scc1 L_PACED0_8\n"
      "L_PACE0_8:\n"
      "s_memrealtime s[84:85]\n"
      "s_waitcnt lgkmcnt(0)\n"
      "s_sub_u32 s85, s84, s86\n"
      "s_cmp_lt_u32 s85, s87\n"
      "s_cbranch_scc1 L_PACE0_8\n"
      "L_PACED0_8:\n"
      "s_mov_b32 s86, s84\n"
      "v_mfma_f32_16x16x32_bf16 v[160:163], a[64:67], a[0:3], v[192:195]\n"
      "s_waitcnt vmcnt(14)\n"
      "s_barrier\n"
      "v_lshlrev_b32 v221, 16, v228\n"
      "v_and_b32 v228, s62, v228\n"
      "v_mfma_f32_16x16x32_bf16 v[160:163], a[68:71], a[4:7], v[160:163]\n"
      "v_exp_f32 v155, v155\n"
      "v_mfma_f32_16x16x32_bf16 v[160:163], a[72:75], a[8:11], v[160:163]\n"
      "ds_read_b128 a[148:151], v219 offset:16384\n"
      "v_cvt_pk_bf16_f32 v153, v154, v155\n"
      "v_mfma_f32_16x16x32_bf16 v[160:163], a[76:79], a[12:15], v[160:163]\n"
      "v_exp_f32 v156, v156\n"
      "v_mfma_f32_16x16x32_bf16 v[164:167], a[80:83], a[0:3], v[192:195]\n"
      "ds_read_b64_tr_b16 a[96:97], v210 offset:32768\n"
      "ds_read_b64_tr_b16 a[98:99], v210 offset:36864\n"
      "v_mfma_f32_16x16x32_bf16 v[164:167], a[84:87], a[4:7], v[164:167]\n"
      "v_exp_f32 v157, v157\n"
      "v_mfma_f32_16x16x32_bf16 v[164:167], a[88:91], a[8:11], v[164:167]\n"
      "ds_read_b64_tr_b16 a[100:101], v211 offset:32768\n"
      "v_cvt_pk_bf16_f32 v154, v156, v157\n"
      "v_mfma_f32_16x16x32_bf16 v[164:167], a[92:95], a[12:15], v[164:167]\n"
      "v_exp_f32 v158, v158\n"
      "v_mfma_f32_16x16x32_bf16 v[168:171], a[64:67], a[16:19], v[196:199]\n"
      "ds_read_b64_tr_b16 a[102:103], v211 offset:36864\n"
      "ds_read_b64_tr_b16 a[104:105], v210 offset:33280\n"
      "v_mfma_f32_16x16x32_bf16 v[168:171], a[68:71], a[20:23], v[168:171]\n"
      "v_exp_f32 v159, v159\n"
      "v_mfma_f32_16x16x32_bf16 v[168:171], a[72:75], a[24:27], v[168:171]\n"
      "ds_read_b64_tr_b16 a[106:107], v210 offset:37376\n"
      "v_cvt_pk_bf16_f32 v155, v158, v159\n"
      "v_mfma_f32_16x16x32_bf16 v[168:171], a[76:79], a[28:31], v[168:171]\n"
      "v_exp_f32 v160, v160\n"
      "v_mfma_f32_16x16x32_bf16 v[172:175], a[80:83], a[16:19], v[196:199]\n"
      "ds_read_b64_tr_b16 a[108:109], v211 offset:33280\n"
      "ds_read_b64_tr_b16 a[110:111], v211 offset:37376\n"
      "v_mfma_f32_16x16x32_bf16 v[172:175], a[84:87], a[20:23], v[172:175]\n"
      "v_exp_f32 v161, v161\n"
      "v_mfma_f32_16x16x32_bf16 v[172:175], a[88:91], a[24:27], v[172:175]\n"
      "ds_read_b64_tr_b16 a[112:113], v210 offset:33792\n"
      "v_cvt_pk_bf16_f32 v160, v160, v161\n"
      "v_mfma_f32_16x16x32_bf16 v[172:175], a[92:95], a[28:31], v[172:175]\n"
      "v_exp_f32 v162, v162\n"
      "v_mfma_f32_16x16x32_bf16 v[176:179], a[64:67], a[32:35], v[200:203]\n"
      "ds_read_b64_tr_b16 a[114:115], v210 offset:37888\n"
      "ds_read_b64_tr_b16 a[116:117], v211 offset:33792\n"
      "v_mfma_f32_16x16x32_bf16 v[176:179], a[68:71], a[36:39], v[176:179]\n"
      "v_exp_f32 v163, v163\n"
      "v_mfma_f32_16x16x32_bf16 v[176:179], a[72:75], a[40:43], v[176:179]\n"
      "ds_read_b64_tr_b16 a[118:119], v211 offset:37888\n"
      "v_cvt_pk_bf16_f32 v161, v162, v163\n"
      "v_mfma_f32_16x16x32_bf16 v[176:179], a[76:79], a[44:47], v[176:179]\n"
      "v_exp_f32 v164, v164\n"
      "v_mfma_f32_16x16x32_bf16 v[180:183], a[80:83], a[32:35], v[200:203]\n"
      "ds_read_b64_tr_b16 a[120:121], v210 offset:34304\n"
      "ds_read_b64_tr_b16 a[122:123], v210 offset:38400\n"
      "v_mfma_f32_16x16x32_bf16 v[180:183], a[84:87], a[36:39], v[180:183]\n"
      "v_exp_f32 v165, v165\n"
      "v_mfma_f32_16x16x32_bf16 v[180:183], a[88:91], a[40:43], v[180:183]\n"
      "ds_read_b64_tr_b16 a[124:125], v211 offset:34304\n"
      "v_cvt_pk_bf16_f32 v162, v164, v165\n"
      "v_mfma_f32_16x16x32_bf16 v[180:183], a[92:95], a[44:47], v[180:183]\n"
      "v_exp_f32 v166, v166\n"
      "v_mfma_f32_16x16x32_bf16 v[184:187], a[64:67], a[48:51], v[204:207]\n"
      "ds_read_b64_tr_b16 a[126:127], v211 offset:38400\n"
      "v_mul_f32 v221, s61, v221\n"
      "v_mfma_f32_16x16x32_bf16 v[184:187], a[68:71], a[52:55], v[184:187]\n"
      "v_exp_f32 v167, v167\n"
      "v_mfma_f32_16x16x32_bf16 v[184:187], a[72:75], a[56:59], v[184:187]\n"
      "v_cvt_pk_bf16_f32 v163, v166, v167\n"
      "v_mul_f32 v228, s61, v228\n"
      "v_mfma_f32_16x16x32_bf16 v[184:187], a[76:79], a[60:63], v[184:187]\n"
      "v_exp_f32 v168, v168\n"
      "v_mfma_f32_16x16x32_bf16 v[188:191], a[80:83], a[48:51], v[204:207]\n"
      "ds_read_b128 a[64:67], v208 offset:16384\n"
      "ds_read_b128 a[68:71], v208 offset:16896\n"
      "v_mfma_f32_16x16x32_bf16 v[188:191], a[84:87], a[52:55], v[188:191]\n"
      "v_exp_f32 v169, v169\n"
      "v_mfma_f32_16x16x32_bf16 v[188:191], a[88:91], a[56:59], v[188:191]\n"
      "ds_read_b128 a[72:75], v208 offset:17408\n"
      "v_cvt_pk_bf16_f32 v168, v168, v169\n"
      "v_mfma_f32_16x16x32_bf16 v[188:191], a[92:95], a[60:63], v[188:191]\n"
      "v_exp_f32 v170, v170\n"
      "v_mfma_f32_16x16x32_bf16 a[128:131], a[144:147], v[128:131], a[128:131]\n"
      "ds_read_b128 a[76:79], v208 offset:17920\n"
      "v_cvt_pk_bf16_f32 v228, v221, v228\n"
      "s_waitcnt lgkmcnt(12)\n"
      "v_mfma_f32_16x16x32_bf16 v[0:3], a[96:99], v[128:131], v[0:3]\n"
      "v_exp_f32 v171, v171\n"
      "v_mfma_f32_16x16x32_bf16 v[4:7], a[100:103], v[128:131], v[4:7]\n"
      "ds_read_b128 a[80:83], v208 offset:20480\n"
      "v_cvt_pk_bf16_f32 v169, v170, v171\n"
      "v_mfma_f32_16x16x32_bf16 v[8:11], a[104:107], v[128:131], v[8:11]\n"
      "v_exp_f32 v172, v172\n"
      "v_mfma_f32_16x16x32_bf16 v[12:15], a[108:111], v[128:131], v[12:15]\n"
      "ds_read_b128 a[84:87], v208 offset:20992\n"
      "ds_read_b128 a[88:91], v208 offset:21504\n"
      "s_waitcnt lgkmcnt(7)\n"
      "v_mfma_f32_16x16x32_bf16 v[16:19], a[112:115], v[128:131], v[16:19]\n"
      "v_exp_f32 v173, v173\n"
      "v_mfma_f32_16x16x32_bf16 v[20:23], a[116:119], v[128:131], v[20:23]\n"
      "ds_read_b128 a[92:95], v208 offset:22016\n"
      "v_cvt_pk_bf16_f32 v170, v172, v173\n"
      "v_mfma_f32_16x16x32_bf16 v[24:27], a[120:123], v[128:131], v[24:27]\n"
      "v_exp_f32 v174, v174\n"
      "v_mfma_f32_16x16x32_bf16 v[28:31], a[124:127], v[128:131], v[28:31]\n"
      "buffer_store_dwordx4 a[148:151], v214, s[52:55], s78 offen offset:0 sc1\n"
      "v_mfma_f32_16x16x32_bf16 a[132:135], a[144:147], v[136:139], a[132:135]\n"
      "v_exp_f32 v175, v175\n"
      "v_mfma_f32_16x16x32_bf16 v[32:35], a[96:99], v[136:139], v[32:35]\n"
      "s_add_u32 m0, s58, 0x2000\n"
      "v_cvt_pk_bf16_f32 v171, v174, v175\n"
      "v_accvgpr_write_b32 a196, v228\n"
      "v_mfma_f32_16x16x32_bf16 v[36:39], a[100:103], v[136:139], v[36:39]\n"
      "s_add_u32 s68, s57, 0x1a000\n"
      "v_exp_f32 v176, v176\n"
      "v_mfma_f32_16x16x32_bf16 v[40:43], a[104:107], v[136:139], v[40:43]\n"
      "buffer_load_dwordx4 v212, s[44:47], s68 offen nt lds\n"
      "v_lshlrev_b32 v222, 16, v229\n"
      "v_mfma_f32_16x16x32_bf16 v[44:47], a[108:111], v[136:139], v[44:47]\n"
      "v_exp_f32 v177, v177\n"
      "v_mfma_f32_16x16x32_bf16 v[48:51], a[112:115], v[136:139], v[48:51]\n"
      "buffer_load_dwordx4 v213, s[44:47], s68 offen offset:1024 nt lds\n"
      "v_cvt_pk_bf16_f32 v176, v176, v177\n"
      "v_mfma_f32_16x16x32_bf16 v[52:55], a[116:119], v[136:139], v[52:55]\n"
      "v_exp_f32 v178, v178\n"
      "v_mfma_f32_16x16x32_bf16 v[56:59], a[120:123], v[136:139], v[56:59]\n"
      "v_and_b32 v229, s62, v229\n"
      "v_mul_f32 v222, s61, v222\n"
      "v_mfma_f32_16x16x32_bf16 v[60:63], a[124:127], v[136:139], v[60:63]\n"
      "v_exp_f32 v179, v179\n"
      "v_mfma_f32_16x16x32_bf16 a[136:139], a[144:147], v[144:147], a[136:139]\n"
      "v_cvt_pk_bf16_f32 v177, v178, v179\n"
      "v_mul_f32 v229, s61, v229\n"
      "v_mfma_f32_16x16x32_bf16 v[64:67], a[96:99], v[144:147], v[64:67]\n"
      "v_exp_f32 v180, v180\n"
      "v_mfma_f32_16x16x32_bf16 v[68:71], a[100:103], v[144:147], v[68:71]\n"
      "v_cvt_pk_bf16_f32 v229, v222, v229\n"
      "v_accvgpr_write_b32 a197, v229\n"
      "v_mfma_f32_16x16x32_bf16 v[72:75], a[104:107], v[144:147], v[72:75]\n"
      "v_exp_f32 v181, v181\n"
      "v_mfma_f32_16x16x32_bf16 v[76:79], a[108:111], v[144:147], v[76:79]\n"
      "v_cvt_pk_bf16_f32 v178, v180, v181\n"
      "v_lshlrev_b32 v221, 16, v230\n"
      "v_mfma_f32_16x16x32_bf16 v[80:83], a[112:115], v[144:147], v[80:83]\n"
      "v_exp_f32 v182, v182\n"
      "v_mfma_f32_16x16x32_bf16 v[84:87], a[116:119], v[144:147], v[84:87]\n"
      "s_add_u32 m0, s58, 0xe000\n"
      "ds_read_b128 v[224:227], v209 offset:1024\n"
      "v_and_b32 v230, s62, v230\n"
      "v_mfma_f32_16x16x32_bf16 v[88:91], a[120:123], v[144:147], v[88:91]\n"
      "s_add_u32 s67, s57, 0x16000\n"
      "v_exp_f32 v183, v183\n"
      "v_mfma_f32_16x16x32_bf16 v[92:95], a[124:127], v[144:147], v[92:95]\n"
      "buffer_load_dwordx4 v212, s[48:51], s67 offen nt lds\n"
      "v_cvt_pk_bf16_f32 v179, v182, v183\n"
      "v_mfma_f32_16x16x32_bf16 a[140:143], a[144:147], v[152:155], a[140:143]\n"
      "v_exp_f32 v184, v184\n"
      "v_mfma_f32_16x16x32_bf16 v[96:99], a[96:99], v[152:155], v[96:99]\n"
      "buffer_load_dwordx4 v213, s[48:51], s67 offen offset:1024 nt lds\n"
      "v_mul_f32 v221, s61, v221\n"
      "v_mfma_f32_16x16x32_bf16 v[100:103], a[100:103], v[152:155], v[100:103]\n"
      "v_exp_f32 v185, v185\n"
      "v_mfma_f32_16x16x32_bf16 v[104:107], a[104:107], v[152:155], v[104:107]\n"
      "v_cvt_pk_bf16_f32 v184, v184, v185\n"
      "v_mul_f32 v230, s61, v230\n"
      "v_mfma_f32_16x16x32_bf16 v[108:111], a[108:111], v[152:155], v[108:111]\n"
      "v_exp_f32 v186, v186\n"
      "v_mfma_f32_16x16x32_bf16 v[112:115], a[112:115], v[152:155], v[112:115]\n"
      "v_cvt_pk_bf16_f32 v230, v221, v230\n"
      "v_accvgpr_write_b32 a198, v230\n"
      "v_mfma_f32_16x16x32_bf16 v[116:119], a[116:119], v[152:155], v[116:119]\n"
      "v_lshlrev_b32 v222, 16, v231\n"
      "v_and_b32 v231, s62, v231\n"
      "v_mfma_f32_16x16x32_bf16 v[120:123], a[120:123], v[152:155], v[120:123]\n"
      "v_mul_f32 v222, s61, v222\n"
      "v_mul_f32 v231, s61, v231\n"
      "v_mfma_f32_16x16x32_bf16 v[124:127], a[124:127], v[152:155], v[124:127]\n"
      "v_cvt_pk_bf16_f32 v231, v222, v231\n"
      "v_accvgpr_write_b32 a199, v231\n"
      "s_waitcnt lgkmcnt(0)\n"
      "v_mfma_f32_16x16x32_bf16 v[128:131], a[64:67], a[0:3], v[192:195]\n"
      "s_waitcnt vmcnt(14)\n"
      "s_barrier\n"
      "v_lshlrev_b32 v221, 16, v224\n"
      "v_and_b32 v224, s62, v224\n"
      "v_mfma_f32_16x16x32_bf16 v[128:131], a[68:71], a[4:7], v[128:131]\n"
      "v_exp_f32 v187, v187\n"
      "v_mfma_f32_16x16x32_bf16 v[128:131], a[72:75], a[8:11], v[128:131]\n"
      "ds_read_b128 a[152:155], v220 offset:17408\n"
      "v_cvt_pk_bf16_f32 v185, v186, v187\n"
      "v_mfma_f32_16x16x32_bf16 v[128:131], a[76:79], a[12:15], v[128:131]\n"
      "v_exp_f32 v188, v188\n"
      "v_mfma_f32_16x16x32_bf16 v[132:135], a[80:83], a[0:3], v[192:195]\n"
      "ds_read_b64_tr_b16 a[96:97], v210 offset:40960\n"
      "ds_read_b64_tr_b16 a[98:99], v210 offset:45056\n"
      "v_mfma_f32_16x16x32_bf16 v[132:135], a[84:87], a[4:7], v[132:135]\n"
      "v_exp_f32 v189, v189\n"
      "v_mfma_f32_16x16x32_bf16 v[132:135], a[88:91], a[8:11], v[132:135]\n"
      "ds_read_b64_tr_b16 a[100:101], v211 offset:40960\n"
      "v_cvt_pk_bf16_f32 v186, v188, v189\n"
      "v_mfma_f32_16x16x32_bf16 v[132:135], a[92:95], a[12:15], v[132:135]\n"
      "v_exp_f32 v190, v190\n"
      "v_mfma_f32_16x16x32_bf16 v[136:139], a[64:67], a[16:19], v[196:199]\n"
      "ds_read_b64_tr_b16 a[102:103], v211 offset:45056\n"
      "ds_read_b64_tr_b16 a[104:105], v210 offset:41472\n"
      "v_mfma_f32_16x16x32_bf16 v[136:139], a[68:71], a[20:23], v[136:139]\n"
      "v_exp_f32 v191, v191\n"
      "v_mfma_f32_16x16x32_bf16 v[136:139], a[72:75], a[24:27], v[136:139]\n"
      "ds_read_b64_tr_b16 a[106:107], v210 offset:45568\n"
      "v_cvt_pk_bf16_f32 v187, v190, v191\n"
      "v_mfma_f32_16x16x32_bf16 v[136:139], a[76:79], a[28:31], v[136:139]\n"
      "v_exp_f32 v128, v128\n"
      "v_mfma_f32_16x16x32_bf16 v[140:143], a[80:83], a[16:19], v[196:199]\n"
      "ds_read_b64_tr_b16 a[108:109], v211 offset:41472\n"
      "ds_read_b64_tr_b16 a[110:111], v211 offset:45568\n"
      "v_mfma_f32_16x16x32_bf16 v[140:143], a[84:87], a[20:23], v[140:143]\n"
      "v_exp_f32 v129, v129\n"
      "v_mfma_f32_16x16x32_bf16 v[140:143], a[88:91], a[24:27], v[140:143]\n"
      "ds_read_b64_tr_b16 a[112:113], v210 offset:41984\n"
      "v_cvt_pk_bf16_f32 v128, v128, v129\n"
      "v_mfma_f32_16x16x32_bf16 v[140:143], a[92:95], a[28:31], v[140:143]\n"
      "v_exp_f32 v130, v130\n"
      "v_mfma_f32_16x16x32_bf16 v[144:147], a[64:67], a[32:35], v[200:203]\n"
      "ds_read_b64_tr_b16 a[114:115], v210 offset:46080\n"
      "ds_read_b64_tr_b16 a[116:117], v211 offset:41984\n"
      "v_mfma_f32_16x16x32_bf16 v[144:147], a[68:71], a[36:39], v[144:147]\n"
      "v_exp_f32 v131, v131\n"
      "v_mfma_f32_16x16x32_bf16 v[144:147], a[72:75], a[40:43], v[144:147]\n"
      "ds_read_b64_tr_b16 a[118:119], v211 offset:46080\n"
      "v_cvt_pk_bf16_f32 v129, v130, v131\n"
      "v_mfma_f32_16x16x32_bf16 v[144:147], a[76:79], a[44:47], v[144:147]\n"
      "v_exp_f32 v132, v132\n"
      "v_mfma_f32_16x16x32_bf16 v[148:151], a[80:83], a[32:35], v[200:203]\n"
      "ds_read_b64_tr_b16 a[120:121], v210 offset:42496\n"
      "ds_read_b64_tr_b16 a[122:123], v210 offset:46592\n"
      "v_mfma_f32_16x16x32_bf16 v[148:151], a[84:87], a[36:39], v[148:151]\n"
      "v_exp_f32 v133, v133\n"
      "v_mfma_f32_16x16x32_bf16 v[148:151], a[88:91], a[40:43], v[148:151]\n"
      "ds_read_b64_tr_b16 a[124:125], v211 offset:42496\n"
      "v_cvt_pk_bf16_f32 v130, v132, v133\n"
      "v_mfma_f32_16x16x32_bf16 v[148:151], a[92:95], a[44:47], v[148:151]\n"
      "v_exp_f32 v134, v134\n"
      "v_mfma_f32_16x16x32_bf16 v[152:155], a[64:67], a[48:51], v[204:207]\n"
      "ds_read_b64_tr_b16 a[126:127], v211 offset:46592\n"
      "v_mul_f32 v221, s61, v221\n"
      "v_mfma_f32_16x16x32_bf16 v[152:155], a[68:71], a[52:55], v[152:155]\n"
      "v_exp_f32 v135, v135\n"
      "v_mfma_f32_16x16x32_bf16 v[152:155], a[72:75], a[56:59], v[152:155]\n"
      "v_cvt_pk_bf16_f32 v131, v134, v135\n"
      "v_mul_f32 v224, s61, v224\n"
      "v_mfma_f32_16x16x32_bf16 v[152:155], a[76:79], a[60:63], v[152:155]\n"
      "v_exp_f32 v136, v136\n"
      "v_mfma_f32_16x16x32_bf16 v[156:159], a[80:83], a[48:51], v[204:207]\n"
      "ds_read_b128 a[64:67], v208 offset:24576\n"
      "ds_read_b128 a[68:71], v208 offset:25088\n"
      "v_mfma_f32_16x16x32_bf16 v[156:159], a[84:87], a[52:55], v[156:159]\n"
      "v_exp_f32 v137, v137\n"
      "v_mfma_f32_16x16x32_bf16 v[156:159], a[88:91], a[56:59], v[156:159]\n"
      "ds_read_b128 a[72:75], v208 offset:25600\n"
      "v_cvt_pk_bf16_f32 v136, v136, v137\n"
      "v_mfma_f32_16x16x32_bf16 v[156:159], a[92:95], a[60:63], v[156:159]\n"
      "v_exp_f32 v138, v138\n"
      "v_mfma_f32_16x16x32_bf16 a[128:131], a[144:147], v[160:163], a[128:131]\n"
      "ds_read_b128 a[76:79], v208 offset:26112\n"
      "v_cvt_pk_bf16_f32 v224, v221, v224\n"
      "s_waitcnt lgkmcnt(12)\n"
      "v_mfma_f32_16x16x32_bf16 v[0:3], a[96:99], v[160:163], v[0:3]\n"
      "v_exp_f32 v139, v139\n"
      "v_mfma_f32_16x16x32_bf16 v[4:7], a[100:103], v[160:163], v[4:7]\n"
      "ds_read_b128 a[80:83], v208 offset:28672\n"
      "v_cvt_pk_bf16_f32 v137, v138, v139\n"
      "v_mfma_f32_16x16x32_bf16 v[8:11], a[104:107], v[160:163], v[8:11]\n"
      "v_exp_f32 v140, v140\n"
      "v_mfma_f32_16x16x32_bf16 v[12:15], a[108:111], v[160:163], v[12:15]\n"
      "ds_read_b128 a[84:87], v208 offset:29184\n"
      "ds_read_b128 a[88:91], v208 offset:29696\n"
      "s_waitcnt lgkmcnt(7)\n"
      "v_mfma_f32_16x16x32_bf16 v[16:19], a[112:115], v[160:163], v[16:19]\n"
      "v_exp_f32 v141, v141\n"
      "v_mfma_f32_16x16x32_bf16 v[20:23], a[116:119], v[160:163], v[20:23]\n"
      "ds_read_b128 a[92:95], v208 offset:30208\n"
      "v_cvt_pk_bf16_f32 v138, v140, v141\n"
      "v_mfma_f32_16x16x32_bf16 v[24:27], a[120:123], v[160:163], v[24:27]\n"
      "v_exp_f32 v142, v142\n"
      "v_mfma_f32_16x16x32_bf16 v[28:31], a[124:127], v[160:163], v[28:31]\n"
      "buffer_store_dwordx4 a[152:155], v214, s[52:55], s78 offen offset:1024 sc1\n"
      "v_mfma_f32_16x16x32_bf16 a[132:135], a[144:147], v[168:171], a[132:135]\n"
      "v_exp_f32 v143, v143\n"
      "v_mfma_f32_16x16x32_bf16 v[32:35], a[96:99], v[168:171], v[32:35]\n"
      "s_add_u32 m0, s58, 0x4000\n"
      "v_cvt_pk_bf16_f32 v139, v142, v143\n"
      "v_accvgpr_write_b32 a200, v224\n"
      "v_mfma_f32_16x16x32_bf16 v[36:39], a[100:103], v[168:171], v[36:39]\n"
      "s_add_u32 s68, s57, 0x1c000\n"
      "v_exp_f32 v144, v144\n"
      "v_mfma_f32_16x16x32_bf16 v[40:43], a[104:107], v[168:171], v[40:43]\n"
      "buffer_load_dwordx4 v212, s[44:47], s68 offen nt lds\n"
      "v_lshlrev_b32 v222, 16, v225\n"
      "v_mfma_f32_16x16x32_bf16 v[44:47], a[108:111], v[168:171], v[44:47]\n"
      "v_exp_f32 v145, v145\n"
      "v_mfma_f32_16x16x32_bf16 v[48:51], a[112:115], v[168:171], v[48:51]\n"
      "buffer_load_dwordx4 v213, s[44:47], s68 offen offset:1024 nt lds\n"
      "v_cvt_pk_bf16_f32 v144, v144, v145\n"
      "v_mfma_f32_16x16x32_bf16 v[52:55], a[116:119], v[168:171], v[52:55]\n"
      "v_exp_f32 v146, v146\n"
      "v_mfma_f32_16x16x32_bf16 v[56:59], a[120:123], v[168:171], v[56:59]\n"
      "v_and_b32 v225, s62, v225\n"
      "v_mul_f32 v222, s61, v222\n"
      "v_mfma_f32_16x16x32_bf16 v[60:63], a[124:127], v[168:171], v[60:63]\n"
      "v_exp_f32 v147, v147\n"
      "v_mfma_f32_16x16x32_bf16 a[136:139], a[144:147], v[176:179], a[136:139]\n"
      "v_cvt_pk_bf16_f32 v145, v146, v147\n"
      "v_mul_f32 v225, s61, v225\n"
      "v_mfma_f32_16x16x32_bf16 v[64:67], a[96:99], v[176:179], v[64:67]\n"
      "v_exp_f32 v148, v148\n"
      "v_mfma_f32_16x16x32_bf16 v[68:71], a[100:103], v[176:179], v[68:71]\n"
      "v_cvt_pk_bf16_f32 v225, v222, v225\n"
      "v_accvgpr_write_b32 a201, v225\n"
      "v_mfma_f32_16x16x32_bf16 v[72:75], a[104:107], v[176:179], v[72:75]\n"
      "v_exp_f32 v149, v149\n"
      "v_mfma_f32_16x16x32_bf16 v[76:79], a[108:111], v[176:179], v[76:79]\n"
      "v_cvt_pk_bf16_f32 v146, v148, v149\n"
      "v_lshlrev_b32 v221, 16, v226\n"
      "v_mfma_f32_16x16x32_bf16 v[80:83], a[112:115], v[176:179], v[80:83]\n"
      "v_exp_f32 v150, v150\n"
      "v_mfma_f32_16x16x32_bf16 v[84:87], a[116:119], v[176:179], v[84:87]\n"
      "s_add_u32 m0, s58, 0x8000\n"
      "ds_read_b128 v[228:231], v209 offset:1536\n"
      "v_and_b32 v226, s62, v226\n"
      "v_mfma_f32_16x16x32_bf16 v[88:91], a[120:123], v[176:179], v[88:91]\n"
      "s_add_u32 s67, s57, 0x18000\n"
      "v_exp_f32 v151, v151\n"
      "v_mfma_f32_16x16x32_bf16 v[92:95], a[124:127], v[176:179], v[92:95]\n"
      "buffer_load_dwordx4 v212, s[48:51], s67 offen nt lds\n"
      "v_cvt_pk_bf16_f32 v147, v150, v151\n"
      "v_mfma_f32_16x16x32_bf16 a[140:143], a[144:147], v[184:187], a[140:143]\n"
      "v_exp_f32 v152, v152\n"
      "v_mfma_f32_16x16x32_bf16 v[96:99], a[96:99], v[184:187], v[96:99]\n"
      "buffer_load_dwordx4 v213, s[48:51], s67 offen offset:1024 nt lds\n"
      "v_mul_f32 v221, s61, v221\n"
      "v_mfma_f32_16x16x32_bf16 v[100:103], a[100:103], v[184:187], v[100:103]\n"
      "v_exp_f32 v153, v153\n"
      "v_mfma_f32_16x16x32_bf16 v[104:107], a[104:107], v[184:187], v[104:107]\n"
      "v_cvt_pk_bf16_f32 v152, v152, v153\n"
      "v_mul_f32 v226, s61, v226\n"
      "v_mfma_f32_16x16x32_bf16 v[108:111], a[108:111], v[184:187], v[108:111]\n"
      "v_exp_f32 v154, v154\n"
      "v_mfma_f32_16x16x32_bf16 v[112:115], a[112:115], v[184:187], v[112:115]\n"
      "v_cvt_pk_bf16_f32 v226, v221, v226\n"
      "v_accvgpr_write_b32 a202, v226\n"
      "v_mfma_f32_16x16x32_bf16 v[116:119], a[116:119], v[184:187], v[116:119]\n"
      "v_lshlrev_b32 v222, 16, v227\n"
      "v_and_b32 v227, s62, v227\n"
      "v_mfma_f32_16x16x32_bf16 v[120:123], a[120:123], v[184:187], v[120:123]\n"
      "s_memrealtime s[84:85]\n"
      "v_mul_f32 v222, s61, v222\n"
      "v_mul_f32 v227, s61, v227\n"
      "v_mfma_f32_16x16x32_bf16 v[124:127], a[124:127], v[184:187], v[124:127]\n"
      "v_cvt_pk_bf16_f32 v227, v222, v227\n"
      "v_accvgpr_write_b32 a203, v227\n"
      "s_waitcnt lgkmcnt(0)\n"
      "s_sub_u32 s85, s84, s86\n"
      "s_cmp_ge_u32 s85, s87\n"
      "s_cbranch_scc1 L_PACED0_10\n"
      "L_PACE0_10:\n"
      "s_memrealtime s[84:85]\n"
      "s_waitcnt lgkmcnt(0)\n"
      "s_sub_u32 s85, s84, s86\n"
      "s_cmp_lt_u32 s85, s87\n"
      "s_cbranch_scc1 L_PACE0_10\n"
      "L_PACED0_10:\n"
      "s_mov_b32 s86, s84\n"
      "v_mfma_f32_16x16x32_bf16 v[160:163], a[64:67], a[0:3], v[192:195]\n"
      "s_waitcnt vmcnt(10)\n"
      "s_barrier\n"
      "v_lshlrev_b32 v221, 16, v228\n"
      "v_and_b32 v228, s62, v228\n"
      "v_mul_f32 v221, s61, v221\n"
      "v_mfma_f32_16x16x32_bf16 v[160:163], a[68:71], a[4:7], v[160:163]\n"
      "v_exp_f32 v155, v155\n"
      "v_mfma_f32_16x16x32_bf16 v[160:163], a[72:75], a[8:11], v[160:163]\n"
      "ds_read_b128 a[148:151], v219 offset:18432\n"
      "v_cvt_pk_bf16_f32 v153, v154, v155\n"
      "v_mfma_f32_16x16x32_bf16 v[160:163], a[76:79], a[12:15], v[160:163]\n"
      "v_exp_f32 v156, v156\n"
      "v_mfma_f32_16x16x32_bf16 v[164:167], a[80:83], a[0:3], v[192:195]\n"
      "ds_read_b64_tr_b16 a[96:97], v210 offset:49152\n"
      "ds_read_b64_tr_b16 a[98:99], v210 offset:53248\n"
      "v_mfma_f32_16x16x32_bf16 v[164:167], a[84:87], a[4:7], v[164:167]\n"
      "v_exp_f32 v157, v157\n"
      "v_mfma_f32_16x16x32_bf16 v[164:167], a[88:91], a[8:11], v[164:167]\n"
      "ds_read_b64_tr_b16 a[100:101], v211 offset:49152\n"
      "v_cvt_pk_bf16_f32 v154, v156, v157\n"
      "v_mfma_f32_16x16x32_bf16 v[164:167], a[92:95], a[12:15], v[164:167]\n"
      "v_exp_f32 v158, v158\n"
      "v_mfma_f32_16x16x32_bf16 v[168:171], a[64:67], a[16:19], v[196:199]\n"
      "ds_read_b64_tr_b16 a[102:103], v211 offset:53248\n"
      "ds_read_b64_tr_b16 a[104:105], v210 offset:49664\n"
      "v_mfma_f32_16x16x32_bf16 v[168:171], a[68:71], a[20:23], v[168:171]\n"
      "v_exp_f32 v159, v159\n"
      "v_mfma_f32_16x16x32_bf16 v[168:171], a[72:75], a[24:27], v[168:171]\n"
      "ds_read_b64_tr_b16 a[106:107], v210 offset:53760\n"
      "v_cvt_pk_bf16_f32 v155, v158, v159\n"
      "v_mfma_f32_16x16x32_bf16 v[168:171], a[76:79], a[28:31], v[168:171]\n"
      "v_exp_f32 v160, v160\n"
      "v_mfma_f32_16x16x32_bf16 v[172:175], a[80:83], a[16:19], v[196:199]\n"
      "ds_read_b64_tr_b16 a[108:109], v211 offset:49664\n"
      "ds_read_b64_tr_b16 a[110:111], v211 offset:53760\n"
      "v_mfma_f32_16x16x32_bf16 v[172:175], a[84:87], a[20:23], v[172:175]\n"
      "v_exp_f32 v161, v161\n"
      "v_mfma_f32_16x16x32_bf16 v[172:175], a[88:91], a[24:27], v[172:175]\n"
      "ds_read_b64_tr_b16 a[112:113], v210 offset:50176\n"
      "v_cvt_pk_bf16_f32 v160, v160, v161\n"
      "v_mfma_f32_16x16x32_bf16 v[172:175], a[92:95], a[28:31], v[172:175]\n"
      "v_exp_f32 v162, v162\n"
      "v_mfma_f32_16x16x32_bf16 v[176:179], a[64:67], a[32:35], v[200:203]\n"
      "ds_read_b64_tr_b16 a[114:115], v210 offset:54272\n"
      "ds_read_b64_tr_b16 a[116:117], v211 offset:50176\n"
      "v_mfma_f32_16x16x32_bf16 v[176:179], a[68:71], a[36:39], v[176:179]\n"
      "v_exp_f32 v163, v163\n"
      "v_mfma_f32_16x16x32_bf16 v[176:179], a[72:75], a[40:43], v[176:179]\n"
      "ds_read_b64_tr_b16 a[118:119], v211 offset:54272\n"
      "v_cvt_pk_bf16_f32 v161, v162, v163\n"
      "v_mfma_f32_16x16x32_bf16 v[176:179], a[76:79], a[44:47], v[176:179]\n"
      "v_exp_f32 v164, v164\n"
      "v_mfma_f32_16x16x32_bf16 v[180:183], a[80:83], a[32:35], v[200:203]\n"
      "ds_read_b64_tr_b16 a[120:121], v210 offset:50688\n"
      "ds_read_b64_tr_b16 a[122:123], v210 offset:54784\n"
      "v_mfma_f32_16x16x32_bf16 v[180:183], a[84:87], a[36:39], v[180:183]\n"
      "v_exp_f32 v165, v165\n"
      "v_mfma_f32_16x16x32_bf16 v[180:183], a[88:91], a[40:43], v[180:183]\n"
      "ds_read_b64_tr_b16 a[124:125], v211 offset:50688\n"
      "v_cvt_pk_bf16_f32 v162, v164, v165\n"
      "v_mfma_f32_16x16x32_bf16 v[180:183], a[92:95], a[44:47], v[180:183]\n"
      "v_exp_f32 v166, v166\n"
      "v_mfma_f32_16x16x32_bf16 v[184:187], a[64:67], a[48:51], v[204:207]\n"
      "ds_read_b64_tr_b16 a[126:127], v211 offset:54784\n"
      "v_mul_f32 v228, s61, v228\n"
      "v_mfma_f32_16x16x32_bf16 v[184:187], a[68:71], a[52:55], v[184:187]\n"
      "v_exp_f32 v167, v167\n"
      "v_mfma_f32_16x16x32_bf16 v[184:187], a[72:75], a[56:59], v[184:187]\n"
      "v_cvt_pk_bf16_f32 v163, v166, v167\n"
      "v_cvt_pk_bf16_f32 v228, v221, v228\n"
      "v_mfma_f32_16x16x32_bf16 v[184:187], a[76:79], a[60:63], v[184:187]\n"
      "v_exp_f32 v168, v168\n"
      "v_mfma_f32_16x16x32_bf16 v[188:191], a[80:83], a[48:51], v[204:207]\n"
      "ds_read_b128 a[64:67], v208 offset:0\n"
      "ds_read_b128 a[68:71], v208 offset:512\n"
      "v_mfma_f32_16x16x32_bf16 v[188:191], a[84:87], a[52:55], v[188:191]\n"
      "v_exp_f32 v169, v169\n"
      "v_mfma_f32_16x16x32_bf16 v[188:191], a[88:91], a[56:59], v[188:191]\n"
      "ds_read_b128 a[72:75], v208 offset:1024\n"
      "v_cvt_pk_bf16_f32 v168, v168, v169\n"
      "v_mfma_f32_16x16x32_bf16 v[188:191], a[92:95], a[60:63], v[188:191]\n"
      "v_exp_f32 v170, v170\n"
      "v_mfma_f32_16x16x32_bf16 a[128:131], a[144:147], v[128:131], a[128:131]\n"
      "ds_read_b128 a[76:79], v208 offset:1536\n"
      "v_accvgpr_write_b32 a204, v228\n"
      "s_waitcnt lgkmcnt(12)\n"
      "v_mfma_f32_16x16x32_bf16 v[0:3], a[96:99], v[128:131], v[0:3]\n"
      "v_exp_f32 v171, v171\n"
      "v_mfma_f32_16x16x32_bf16 v[4:7], a[100:103], v[128:131], v[4:7]\n"
      "ds_read_b128 a[80:83], v208 offset:4096\n"
      "v_cvt_pk_bf16_f32 v169, v170, v171\n"
      "v_mfma_f32_16x16x32_bf16 v[8:11], a[104:107], v[128:131], v[8:11]\n"
      "v_exp_f32 v172, v172\n"
      "v_mfma_f32_16x16x32_bf16 v[12:15], a[108:111], v[128:131], v[12:15]\n"
      "ds_read_b128 a[84:87], v208 offset:4608\n"
      "ds_read_b128 a[88:91], v208 offset:5120\n"
      "v_lshlrev_b32 v222, 16, v229\n"
      "s_waitcnt lgkmcnt(7)\n"
      "v_mfma_f32_16x16x32_bf16 v[16:19], a[112:115], v[128:131], v[16:19]\n"
      "v_exp_f32 v173, v173\n"
      "v_mfma_f32_16x16x32_bf16 v[20:23], a[116:119], v[128:131], v[20:23]\n"
      "ds_read_b128 a[92:95], v208 offset:5632\n"
      "v_cvt_pk_bf16_f32 v170, v172, v173\n"
      "v_mfma_f32_16x16x32_bf16 v[24:27], a[120:123], v[128:131], v[24:27]\n"
      "v_exp_f32 v174, v174\n"
      "v_mfma_f32_16x16x32_bf16 v[28:31], a[124:127], v[128:131], v[28:31]\n"
      "s_add_u32 m0, s58, 0x6000\n"
      "buffer_store_dwordx4 a[148:151], v214, s[52:55], s78 offen offset:2048 sc1\n"
      "v_mfma_f32_16x16x32_bf16 a[132:135], a[144:147], v[136:139], a[132:135]\n"
      "s_add_u32 s67, s57, 0x1e000\n"
      "v_exp_f32 v175, v175\n"
      "v_mfma_f32_16x16x32_bf16 v[32:35], a[96:99], v[136:139], v[32:35]\n"
      "buffer_load_dwordx4 v212, s[44:47], s67 offen nt lds\n"
      "v_cvt_pk_bf16_f32 v171, v174, v175\n"
      "v_mfma_f32_16x16x32_bf16 v[36:39], a[100:103], v[136:139], v[36:39]\n"
      "v_exp_f32 v176, v176\n"
      "v_mfma_f32_16x16x32_bf16 v[40:43], a[104:107], v[136:139], v[40:43]\n"
      "buffer_load_dwordx4 v213, s[44:47], s67 offen offset:1024 nt lds\n"
      "v_and_b32 v229, s62, v229\n"
      "v_mfma_f32_16x16x32_bf16 v[44:47], a[108:111], v[136:139], v[44:47]\n"
      "v_exp_f32 v177, v177\n"
      "v_mfma_f32_16x16x32_bf16 v[48:51], a[112:115], v[136:139], v[48:51]\n"
      "v_cvt_pk_bf16_f32 v176, v176, v177\n"
      "v_mul_f32 v222, s61, v222\n"
      "v_mfma_f32_16x16x32_bf16 v[52:55], a[116:119], v[136:139], v[52:55]\n"
      "v_exp_f32 v178, v178\n"
      "v_mfma_f32_16x16x32_bf16 v[56:59], a[120:123], v[136:139], v[56:59]\n"
      "v_mul_f32 v229, s61, v229\n"
      "v_cvt_pk_bf16_f32 v229, v222, v229\n"
      "v_mfma_f32_16x16x32_bf16 v[60:63], a[124:127], v[136:139], v[60:63]\n"
      "v_exp_f32 v179, v179\n"
      "v_mfma_f32_16x16x32_bf16 a[136:139], a[144:147], v[144:147], a[136:139]\n"
      "s_add_u32 m0, s58, 0xa000\n"
      "v_cvt_pk_bf16_f32 v177, v178, v179\n"
      "v_accvgpr_write_b32 a205, v229\n"
      "v_mfma_f32_16x16x32_bf16 v[64:67], a[96:99], v[144:147], v[64:67]\n"
      "s_add_u32 s68, s57, 0x1a000\n"
      "v_exp_f32 v180, v180\n"
      "v_mfma_f32_16x16x32_bf16 v[68:71], a[100:103], v[144:147], v[68:71]\n"
      "buffer_load_dwordx4 v212, s[48:51], s68 offen nt lds\n"
      "v_lshlrev_b32 v221, 16, v230\n"
      "v_mfma_f32_16x16x32_bf16 v[72:75], a[104:107], v[144:147], v[72:75]\n"
      "v_exp_f32 v181, v181\n"
      "v_mfma_f32_16x16x32_bf16 v[76:79], a[108:111], v[144:147], v[76:79]\n"
      "buffer_load_dwordx4 v213, s[48:51], s68 offen offset:1024 nt lds\n"
      "v_cvt_pk_bf16_f32 v178, v180, v181\n"
      "v_mfma_f32_16x16x32_bf16 v[80:83], a[112:115], v[144:147], v[80:83]\n"
      "v_exp_f32 v182, v182\n"
      "v_mfma_f32_16x16x32_bf16 v[84:87], a[116:119], v[144:147], v[84:87]\n"
      "ds_read_b128 v[224:227], v209 offset:4096\n"
      "v_and_b32 v230, s62, v230\n"
      "v_mfma_f32_16x16x32_bf16 v[88:91], a[120:123], v[144:147], v[88:91]\n"
      "v_exp_f32 v183, v183\n"
      "v_mfma_f32_16x16x32_bf16 v[92:95], a[124:127], v[144:147], v[92:95]\n"
      "s_mov_b32 m0, s59\n"
      "v_cvt_pk_bf16_f32 v179, v182, v183\n"
      "v_mul_f32 v221, s61, v221\n"
      "v_mfma_f32_16x16x32_bf16 a[140:143], a[144:147], v[152:155], a[140:143]\n"
      "s_add_u32 s68, s56, 0x2000000\n"
      "v_exp_f32 v184, v184\n"
      "v_mfma_f32_16x16x32_bf16 v[96:99], a[96:99], v[152:155], v[96:99]\n"
      "buffer_load_dwordx4 v212, s[40:43], s68 offen nt lds\n"
      "v_mul_f32 v230, s61, v230\n"
      "v_mfma_f32_16x16x32_bf16 v[100:103], a[100:103], v[152:155], v[100:103]\n"
      "v_exp_f32 v185, v185\n"
      "v_mfma_f32_16x16x32_bf16 v[104:107], a[104:107], v[152:155], v[104:107]\n"
      "buffer_load_dwordx4 v213, s[40:43], s68 offen offset:1024 nt lds\n"
      "v_cvt_pk_bf16_f32 v184, v184, v185\n"
      "v_mfma_f32_16x16x32_bf16 v[108:111], a[108:111], v[152:155], v[108:111]\n"
      "v_exp_f32 v186, v186\n"
      "v_mfma_f32_16x16x32_bf16 v[112:115], a[112:115], v[152:155], v[112:115]\n"
      "v_cvt_pk_bf16_f32 v230, v221, v230\n"
      "v_accvgpr_write_b32 a206, v230\n"
      "v_mfma_f32_16x16x32_bf16 v[116:119], a[116:119], v[152:155], v[116:119]\n"
      "v_lshlrev_b32 v222, 16, v231\n"
      "v_and_b32 v231, s62, v231\n"
      "v_mfma_f32_16x16x32_bf16 v[120:123], a[120:123], v[152:155], v[120:123]\n"
      "v_mul_f32 v222, s61, v222\n"
      "v_mul_f32 v231, s61, v231\n"
      "v_mfma_f32_16x16x32_bf16 v[124:127], a[124:127], v[152:155], v[124:127]\n"
      "v_cvt_pk_bf16_f32 v231, v222, v231\n"
      "v_accvgpr_write_b32 a207, v231\n"
      "s_waitcnt lgkmcnt(0)\n"
      "v_mfma_f32_16x16x32_bf16 v[128:131], a[64:67], a[0:3], v[192:195]\n"
      "s_waitcnt vmcnt(12)\n"
      "s_barrier\n"
      "v_lshlrev_b32 v221, 16, v224\n"
      "v_and_b32 v224, s62, v224\n"
      "v_mul_f32 v221, s61, v221\n"
      "v_mfma_f32_16x16x32_bf16 v[128:131], a[68:71], a[4:7], v[128:131]\n"
      "v_exp_f32 v187, v187\n"
      "v_mfma_f32_16x16x32_bf16 v[128:131], a[72:75], a[8:11], v[128:131]\n"
      "ds_read_b128 a[152:155], v220 offset:19456\n"
      "v_cvt_pk_bf16_f32 v185, v186, v187\n"
      "v_mfma_f32_16x16x32_bf16 v[128:131], a[76:79], a[12:15], v[128:131]\n"
      "v_exp_f32 v188, v188\n"
      "v_mfma_f32_16x16x32_bf16 v[132:135], a[80:83], a[0:3], v[192:195]\n"
      "ds_read_b64_tr_b16 a[96:97], v210 offset:57344\n"
      "ds_read_b64_tr_b16 a[98:99], v210 offset:61440\n"
      "v_mfma_f32_16x16x32_bf16 v[132:135], a[84:87], a[4:7], v[132:135]\n"
      "v_exp_f32 v189, v189\n"
      "v_mfma_f32_16x16x32_bf16 v[132:135], a[88:91], a[8:11], v[132:135]\n"
      "ds_read_b64_tr_b16 a[100:101], v211 offset:57344\n"
      "v_cvt_pk_bf16_f32 v186, v188, v189\n"
      "v_mfma_f32_16x16x32_bf16 v[132:135], a[92:95], a[12:15], v[132:135]\n"
      "v_exp_f32 v190, v190\n"
      "v_mfma_f32_16x16x32_bf16 v[136:139], a[64:67], a[16:19], v[196:199]\n"
      "ds_read_b64_tr_b16 a[102:103], v211 offset:61440\n"
      "ds_read_b64_tr_b16 a[104:105], v210 offset:57856\n"
      "v_mfma_f32_16x16x32_bf16 v[136:139], a[68:71], a[20:23], v[136:139]\n"
      "v_exp_f32 v191, v191\n"
      "v_mfma_f32_16x16x32_bf16 v[136:139], a[72:75], a[24:27], v[136:139]\n"
      "ds_read_b64_tr_b16 a[106:107], v210 offset:61952\n"
      "v_cvt_pk_bf16_f32 v187, v190, v191\n"
      "v_mfma_f32_16x16x32_bf16 v[136:139], a[76:79], a[28:31], v[136:139]\n"
      "v_exp_f32 v128, v128\n"
      "v_mfma_f32_16x16x32_bf16 v[140:143], a[80:83], a[16:19], v[196:199]\n"
      "ds_read_b64_tr_b16 a[108:109], v211 offset:57856\n"
      "ds_read_b64_tr_b16 a[110:111], v211 offset:61952\n"
      "v_mfma_f32_16x16x32_bf16 v[140:143], a[84:87], a[20:23], v[140:143]\n"
      "v_exp_f32 v129, v129\n"
      "v_mfma_f32_16x16x32_bf16 v[140:143], a[88:91], a[24:27], v[140:143]\n"
      "ds_read_b64_tr_b16 a[112:113], v210 offset:58368\n"
      "v_cvt_pk_bf16_f32 v128, v128, v129\n"
      "v_mfma_f32_16x16x32_bf16 v[140:143], a[92:95], a[28:31], v[140:143]\n"
      "v_exp_f32 v130, v130\n"
      "v_mfma_f32_16x16x32_bf16 v[144:147], a[64:67], a[32:35], v[200:203]\n"
      "ds_read_b64_tr_b16 a[114:115], v210 offset:62464\n"
      "ds_read_b64_tr_b16 a[116:117], v211 offset:58368\n"
      "v_mfma_f32_16x16x32_bf16 v[144:147], a[68:71], a[36:39], v[144:147]\n"
      "v_exp_f32 v131, v131\n"
      "v_mfma_f32_16x16x32_bf16 v[144:147], a[72:75], a[40:43], v[144:147]\n"
      "ds_read_b64_tr_b16 a[118:119], v211 offset:62464\n"
      "v_cvt_pk_bf16_f32 v129, v130, v131\n"
      "v_mfma_f32_16x16x32_bf16 v[144:147], a[76:79], a[44:47], v[144:147]\n"
      "v_exp_f32 v132, v132\n"
      "v_mfma_f32_16x16x32_bf16 v[148:151], a[80:83], a[32:35], v[200:203]\n"
      "ds_read_b64_tr_b16 a[120:121], v210 offset:58880\n"
      "ds_read_b64_tr_b16 a[122:123], v210 offset:62976\n"
      "v_mfma_f32_16x16x32_bf16 v[148:151], a[84:87], a[36:39], v[148:151]\n"
      "v_exp_f32 v133, v133\n"
      "v_mfma_f32_16x16x32_bf16 v[148:151], a[88:91], a[40:43], v[148:151]\n"
      "ds_read_b64_tr_b16 a[124:125], v211 offset:58880\n"
      "v_cvt_pk_bf16_f32 v130, v132, v133\n"
      "v_mfma_f32_16x16x32_bf16 v[148:151], a[92:95], a[44:47], v[148:151]\n"
      "v_exp_f32 v134, v134\n"
      "v_mfma_f32_16x16x32_bf16 v[152:155], a[64:67], a[48:51], v[204:207]\n"
      "ds_read_b64_tr_b16 a[126:127], v211 offset:62976\n"
      "v_mul_f32 v224, s61, v224\n"
      "v_mfma_f32_16x16x32_bf16 v[152:155], a[68:71], a[52:55], v[152:155]\n"
      "v_exp_f32 v135, v135\n"
      "v_mfma_f32_16x16x32_bf16 v[152:155], a[72:75], a[56:59], v[152:155]\n"
      "v_cvt_pk_bf16_f32 v131, v134, v135\n"
      "v_cvt_pk_bf16_f32 v224, v221, v224\n"
      "v_mfma_f32_16x16x32_bf16 v[152:155], a[76:79], a[60:63], v[152:155]\n"
      "v_exp_f32 v136, v136\n"
      "v_mfma_f32_16x16x32_bf16 v[156:159], a[80:83], a[48:51], v[204:207]\n"
      "ds_read_b128 a[64:67], v208 offset:8192\n"
      "ds_read_b128 a[68:71], v208 offset:8704\n"
      "v_mfma_f32_16x16x32_bf16 v[156:159], a[84:87], a[52:55], v[156:159]\n"
      "v_exp_f32 v137, v137\n"
      "v_mfma_f32_16x16x32_bf16 v[156:159], a[88:91], a[56:59], v[156:159]\n"
      "ds_read_b128 a[72:75], v208 offset:9216\n"
      "v_cvt_pk_bf16_f32 v136, v136, v137\n"
      "v_mfma_f32_16x16x32_bf16 v[156:159], a[92:95], a[60:63], v[156:159]\n"
      "v_exp_f32 v138, v138\n"
      "v_mfma_f32_16x16x32_bf16 a[128:131], a[144:147], v[160:163], a[128:131]\n"
      "ds_read_b128 a[76:79], v208 offset:9728\n"
      "v_accvgpr_write_b32 a208, v224\n"
      "s_waitcnt lgkmcnt(12)\n"
      "v_mfma_f32_16x16x32_bf16 v[0:3], a[96:99], v[160:163], v[0:3]\n"
      "v_exp_f32 v139, v139\n"
      "v_mfma_f32_16x16x32_bf16 v[4:7], a[100:103], v[160:163], v[4:7]\n"
      "ds_read_b128 a[80:83], v208 offset:12288\n"
      "v_cvt_pk_bf16_f32 v137, v138, v139\n"
      "v_mfma_f32_16x16x32_bf16 v[8:11], a[104:107], v[160:163], v[8:11]\n"
      "v_exp_f32 v140, v140\n"
      "v_mfma_f32_16x16x32_bf16 v[12:15], a[108:111], v[160:163], v[12:15]\n"
      "ds_read_b128 a[84:87], v208 offset:12800\n"
      "ds_read_b128 a[88:91], v208 offset:13312\n"
      "v_lshlrev_b32 v222, 16, v225\n"
      "s_waitcnt lgkmcnt(7)\n"
      "v_mfma_f32_16x16x32_bf16 v[16:19], a[112:115], v[160:163], v[16:19]\n"
      "v_exp_f32 v141, v141\n"
      "v_mfma_f32_16x16x32_bf16 v[20:23], a[116:119], v[160:163], v[20:23]\n"
      "ds_read_b128 a[92:95], v208 offset:13824\n"
      "v_cvt_pk_bf16_f32 v138, v140, v141\n"
      "v_mfma_f32_16x16x32_bf16 v[24:27], a[120:123], v[160:163], v[24:27]\n"
      "v_exp_f32 v142, v142\n"
      "v_mfma_f32_16x16x32_bf16 v[28:31], a[124:127], v[160:163], v[28:31]\n"
      "s_mov_b32 m0, s58\n"
      "buffer_store_dwordx4 a[152:155], v214, s[52:55], s78 offen offset:3072 sc1\n"
      "v_mfma_f32_16x16x32_bf16 a[132:135], a[144:147], v[168:171], a[132:135]\n"
      "s_add_u32 s68, s57, 0x2000000\n"
      "v_exp_f32 v143, v143\n"
      "v_mfma_f32_16x16x32_bf16 v[32:35], a[96:99], v[168:171], v[32:35]\n"
      "buffer_load_dwordx4 v212, s[44:47], s68 offen nt lds\n"
      "v_cvt_pk_bf16_f32 v139, v142, v143\n"
      "v_mfma_f32_16x16x32_bf16 v[36:39], a[100:103], v[168:171], v[36:39]\n"
      "v_exp_f32 v144, v144\n"
      "v_mfma_f32_16x16x32_bf16 v[40:43], a[104:107], v[168:171], v[40:43]\n"
      "buffer_load_dwordx4 v213, s[44:47], s68 offen offset:1024 nt lds\n"
      "v_and_b32 v225, s62, v225\n"
      "v_mfma_f32_16x16x32_bf16 v[44:47], a[108:111], v[168:171], v[44:47]\n"
      "v_exp_f32 v145, v145\n"
      "v_mfma_f32_16x16x32_bf16 v[48:51], a[112:115], v[168:171], v[48:51]\n"
      "v_cvt_pk_bf16_f32 v144, v144, v145\n"
      "v_mul_f32 v222, s61, v222\n"
      "v_mfma_f32_16x16x32_bf16 v[52:55], a[116:119], v[168:171], v[52:55]\n"
      "v_exp_f32 v146, v146\n"
      "v_mfma_f32_16x16x32_bf16 v[56:59], a[120:123], v[168:171], v[56:59]\n"
      "v_mul_f32 v225, s61, v225\n"
      "v_cvt_pk_bf16_f32 v225, v222, v225\n"
      "v_mfma_f32_16x16x32_bf16 v[60:63], a[124:127], v[168:171], v[60:63]\n"
      "v_exp_f32 v147, v147\n"
      "v_mfma_f32_16x16x32_bf16 a[136:139], a[144:147], v[176:179], a[136:139]\n"
      "s_add_u32 m0, s58, 0xc000\n"
      "v_cvt_pk_bf16_f32 v145, v146, v147\n"
      "v_accvgpr_write_b32 a209, v225\n"
      "v_mfma_f32_16x16x32_bf16 v[64:67], a[96:99], v[176:179], v[64:67]\n"
      "s_add_u32 s67, s57, 0x1c000\n"
      "v_exp_f32 v148, v148\n"
      "v_mfma_f32_16x16x32_bf16 v[68:71], a[100:103], v[176:179], v[68:71]\n"
      "buffer_load_dwordx4 v212, s[48:51], s67 offen nt lds\n"
      "v_lshlrev_b32 v221, 16, v226\n"
      "v_mfma_f32_16x16x32_bf16 v[72:75], a[104:107], v[176:179], v[72:75]\n"
      "v_exp_f32 v149, v149\n"
      "v_mfma_f32_16x16x32_bf16 v[76:79], a[108:111], v[176:179], v[76:79]\n"
      "buffer_load_dwordx4 v213, s[48:51], s67 offen offset:1024 nt lds\n"
      "v_cvt_pk_bf16_f32 v146, v148, v149\n"
      "v_mfma_f32_16x16x32_bf16 v[80:83], a[112:115], v[176:179], v[80:83]\n"
      "v_exp_f32 v150, v150\n"
      "v_mfma_f32_16x16x32_bf16 v[84:87], a[116:119], v[176:179], v[84:87]\n"
      "ds_read_b128 v[228:231], v209 offset:4608\n"
      "v_and_b32 v226, s62, v226\n"
      "v_mfma_f32_16x16x32_bf16 v[88:91], a[120:123], v[176:179], v[88:91]\n"
      "v_exp_f32 v151, v151\n"
      "v_mfma_f32_16x16x32_bf16 v[92:95], a[124:127], v[176:179], v[92:95]\n"
      "s_add_u32 m0, s59, 0x800\n"
      "v_cvt_pk_bf16_f32 v147, v150, v151\n"
      "v_mul_f32 v221, s61, v221\n"
      "v_mfma_f32_16x16x32_bf16 a[140:143], a[144:147], v[184:187], a[140:143]\n"
      "s_add_u32 s67, s56, 0x2000800\n"
      "v_exp_f32 v152, v152\n"
      "v_mfma_f32_16x16x32_bf16 v[96:99], a[96:99], v[184:187], v[96:99]\n"
      "buffer_load_dwordx4 v212, s[40:43], s67 offen nt lds\n"
      "v_mul_f32 v226, s61, v226\n"
      "v_mfma_f32_16x16x32_bf16 v[100:103], a[100:103], v[184:187], v[100:103]\n"
      "v_exp_f32 v153, v153\n"
      "v_mfma_f32_16x16x32_bf16 v[104:107], a[104:107], v[184:187], v[104:107]\n"
      "buffer_load_dwordx4 v213, s[40:43], s67 offen offset:1024 nt lds\n"
      "v_cvt_pk_bf16_f32 v152, v152, v153\n"
      "v_mfma_f32_16x16x32_bf16 v[108:111], a[108:111], v[184:187], v[108:111]\n"
      "v_exp_f32 v154, v154\n"
      "v_mfma_f32_16x16x32_bf16 v[112:115], a[112:115], v[184:187], v[112:115]\n"
      "v_cvt_pk_bf16_f32 v226, v221, v226\n"
      "v_accvgpr_write_b32 a210, v226\n"
      "v_mfma_f32_16x16x32_bf16 v[116:119], a[116:119], v[184:187], v[116:119]\n"
      "v_lshlrev_b32 v222, 16, v227\n"
      "v_and_b32 v227, s62, v227\n"
      "v_mfma_f32_16x16x32_bf16 v[120:123], a[120:123], v[184:187], v[120:123]\n"
      "s_memrealtime s[84:85]\n"
      "v_mul_f32 v222, s61, v222\n"
      "v_mul_f32 v227, s61, v227\n"
      "v_mfma_f32_16x16x32_bf16 v[124:127], a[124:127], v[184:187], v[124:127]\n"
      "v_cvt_pk_bf16_f32 v227, v222, v227\n"
      "v_accvgpr_write_b32 a211, v227\n"
      "s_waitcnt lgkmcnt(0)\n"
      "s_sub_u32 s85, s84, s86\n"
      "s_cmp_ge_u32 s85, s87\n"
      "s_cbranch_scc1 L_PACED0_12\n"
      "L_PACE0_12:\n"
      "s_memrealtime s[84:85]\n"
      "s_waitcnt lgkmcnt(0)\n"
      "s_sub_u32 s85, s84, s86\n"
      "s_cmp_lt_u32 s85, s87\n"
      "s_cbranch_scc1 L_PACE0_12\n"
      "L_PACED0_12:\n"
      "s_mov_b32 s86, s84\n"
      "v_mfma_f32_16x16x32_bf16 v[160:163], a[64:67], a[0:3], v[192:195]\n"
      "s_waitcnt vmcnt(14)\n"
      "s_barrier\n"
      "v_lshlrev_b32 v221, 16, v228\n"
      "v_and_b32 v228, s62, v228\n"
      "v_mfma_f32_16x16x32_bf16 v[160:163], a[68:71], a[4:7], v[160:163]\n"
      "v_exp_f32 v155, v155\n"
      "v_mfma_f32_16x16x32_bf16 v[160:163], a[72:75], a[8:11], v[160:163]\n"
      "ds_read_b128 a[148:151], v219 offset:20480\n"
      "v_cvt_pk_bf16_f32 v153, v154, v155\n"
      "v_mfma_f32_16x16x32_bf16 v[160:163], a[76:79], a[12:15], v[160:163]\n"
      "v_exp_f32 v156, v156\n"
      "v_mfma_f32_16x16x32_bf16 v[164:167], a[80:83], a[0:3], v[192:195]\n"
      "ds_read_b64_tr_b16 a[96:97], v210 offset:32768\n"
      "ds_read_b64_tr_b16 a[98:99], v210 offset:36864\n"
      "v_mfma_f32_16x16x32_bf16 v[164:167], a[84:87], a[4:7], v[164:167]\n"
      "v_exp_f32 v157, v157\n"
      "v_mfma_f32_16x16x32_bf16 v[164:167], a[88:91], a[8:11], v[164:167]\n"
      "ds_read_b64_tr_b16 a[100:101], v211 offset:32768\n"
      "v_cvt_pk_bf16_f32 v154, v156, v157\n"
      "v_mfma_f32_16x16x32_bf16 v[164:167], a[92:95], a[12:15], v[164:167]\n"
      "v_exp_f32 v158, v158\n"
      "v_mfma_f32_16x16x32_bf16 v[168:171], a[64:67], a[16:19], v[196:199]\n"
      "ds_read_b64_tr_b16 a[102:103], v211 offset:36864\n"
      "ds_read_b64_tr_b16 a[104:105], v210 offset:33280\n"
      "v_mfma_f32_16x16x32_bf16 v[168:171], a[68:71], a[20:23], v[168:171]\n"
      "v_exp_f32 v159, v159\n"
      "v_mfma_f32_16x16x32_bf16 v[168:171], a[72:75], a[24:27], v[168:171]\n"
      "ds_read_b64_tr_b16 a[106:107], v210 offset:37376\n"
      "v_cvt_pk_bf16_f32 v155, v158, v159\n"
      "v_mfma_f32_16x16x32_bf16 v[168:171], a[76:79], a[28:31], v[168:171]\n"
      "v_exp_f32 v160, v160\n"
      "v_mfma_f32_16x16x32_bf16 v[172:175], a[80:83], a[16:19], v[196:199]\n"
      "ds_read_b64_tr_b16 a[108:109], v211 offset:33280\n"
      "ds_read_b64_tr_b16 a[110:111], v211 offset:37376\n"
      "v_mfma_f32_16x16x32_bf16 v[172:175], a[84:87], a[20:23], v[172:175]\n"
      "v_exp_f32 v161, v161\n"
      "v_mfma_f32_16x16x32_bf16 v[172:175], a[88:91], a[24:27], v[172:175]\n"
      "ds_read_b64_tr_b16 a[112:113], v210 offset:33792\n"
      "v_cvt_pk_bf16_f32 v160, v160, v161\n"
      "v_mfma_f32_16x16x32_bf16 v[172:175], a[92:95], a[28:31], v[172:175]\n"
      "v_exp_f32 v162, v162\n"
      "v_mfma_f32_16x16x32_bf16 v[176:179], a[64:67], a[32:35], v[200:203]\n"
      "ds_read_b64_tr_b16 a[114:115], v210 offset:37888\n"
      "ds_read_b64_tr_b16 a[116:117], v211 offset:33792\n"
      "v_mfma_f32_16x16x32_bf16 v[176:179], a[68:71], a[36:39], v[176:179]\n"
      "v_exp_f32 v163, v163\n"
      "v_mfma_f32_16x16x32_bf16 v[176:179], a[72:75], a[40:43], v[176:179]\n"
      "ds_read_b64_tr_b16 a[118:119], v211 offset:37888\n"
      "v_cvt_pk_bf16_f32 v161, v162, v163\n"
      "v_mfma_f32_16x16x32_bf16 v[176:179], a[76:79], a[44:47], v[176:179]\n"
      "v_exp_f32 v164, v164\n"
      "v_mfma_f32_16x16x32_bf16 v[180:183], a[80:83], a[32:35], v[200:203]\n"
      "ds_read_b64_tr_b16 a[120:121], v210 offset:34304\n"
      "ds_read_b64_tr_b16 a[122:123], v210 offset:38400\n"
      "v_mfma_f32_16x16x32_bf16 v[180:183], a[84:87], a[36:39], v[180:183]\n"
      "v_exp_f32 v165, v165\n"
      "v_mfma_f32_16x16x32_bf16 v[180:183], a[88:91], a[40:43], v[180:183]\n"
      "ds_read_b64_tr_b16 a[124:125], v211 offset:34304\n"
      "v_cvt_pk_bf16_f32 v162, v164, v165\n"
      "v_mfma_f32_16x16x32_bf16 v[180:183], a[92:95], a[44:47], v[180:183]\n"
      "v_exp_f32 v166, v166\n"
      "v_mfma_f32_16x16x32_bf16 v[184:187], a[64:67], a[48:51], v[204:207]\n"
      "ds_read_b64_tr_b16 a[126:127], v211 offset:38400\n"
      "v_mul_f32 v221, s61, v221\n"
      "v_mfma_f32_16x16x32_bf16 v[184:187], a[68:71], a[52:55], v[184:187]\n"
      "v_exp_f32 v167, v167\n"
      "v_mfma_f32_16x16x32_bf16 v[184:187], a[72:75], a[56:59], v[184:187]\n"
      "v_cvt_pk_bf16_f32 v163, v166, v167\n"
      "v_mul_f32 v228, s61, v228\n"
      "v_mfma_f32_16x16x32_bf16 v[184:187], a[76:79], a[60:63], v[184:187]\n"
      "v_exp_f32 v168, v168\n"
      "v_mfma_f32_16x16x32_bf16 v[188:191], a[80:83], a[48:51], v[204:207]\n"
      "ds_read_b128 a[64:67], v208 offset:16384\n"
      "ds_read_b128 a[68:71], v208 offset:16896\n"
      "v_mfma_f32_16x16x32_bf16 v[188:191], a[84:87], a[52:55], v[188:191]\n"
      "v_exp_f32 v169, v169\n"
      "v_mfma_f32_16x16x32_bf16 v[188:191], a[88:91], a[56:59], v[188:191]\n"
      "ds_read_b128 a[72:75], v208 offset:17408\n"
      "v_cvt_pk_bf16_f32 v168, v168, v169\n"
      "v_mfma_f32_16x16x32_bf16 v[188:191], a[92:95], a[60:63], v[188:191]\n"
      "v_exp_f32 v170, v170\n"
      "v_mfma_f32_16x16x32_bf16 a[128:131], a[144:147], v[128:131], a[128:131]\n"
      "ds_read_b128 a[76:79], v208 offset:17920\n"
      "v_cvt_pk_bf16_f32 v228, v221, v228\n"
      "s_waitcnt lgkmcnt(12)\n"
      "v_mfma_f32_16x16x32_bf16 v[0:3], a[96:99], v[128:131], v[0:3]\n"
      "v_exp_f32 v171, v171\n"
      "v_mfma_f32_16x16x32_bf16 v[4:7], a[100:103], v[128:131], v[4:7]\n"
      "ds_read_b128 a[80:83], v208 offset:20480\n"
      "v_cvt_pk_bf16_f32 v169, v170, v171\n"
      "v_mfma_f32_16x16x32_bf16 v[8:11], a[104:107], v[128:131], v[8:11]\n"
      "v_exp_f32 v172, v172\n"
      "v_mfma_f32_16x16x32_bf16 v[12:15], a[108:111], v[128:131], v[12:15]\n"
      "ds_read_b128 a[84:87], v208 offset:20992\n"
      "ds_read_b128 a[88:91], v208 offset:21504\n"
      "s_waitcnt lgkmcnt(7)\n"
      "v_mfma_f32_16x16x32_bf16 v[16:19], a[112:115], v[128:131], v[16:19]\n"
      "v_exp_f32 v173, v173\n"
      "v_mfma_f32_16x16x32_bf16 v[20:23], a[116:119], v[128:131], v[20:23]\n"
      "ds_read_b128 a[92:95], v208 offset:22016\n"
      "v_cvt_pk_bf16_f32 v170, v172, v173\n"
      "v_mfma_f32_16x16x32_bf16 v[24:27], a[120:123], v[128:131], v[24:27]\n"
      "v_exp_f32 v174, v174\n"
      "v_mfma_f32_16x16x32_bf16 v[28:31], a[124:127], v[128:131], v[28:31]\n"
      "buffer_store_dwordx4 a[148:151], v214, s[52:55], s79 offen offset:0 sc1\n"
      "v_mfma_f32_16x16x32_bf16 a[132:135], a[144:147], v[136:139], a[132:135]\n"
      "v_exp_f32 v175, v175\n"
      "v_mfma_f32_16x16x32_bf16 v[32:35], a[96:99], v[136:139], v[32:35]\n"
      "s_add_u32 m0, s58, 0x2000\n"
      "v_cvt_pk_bf16_f32 v171, v174, v175\n"
      "v_accvgpr_write_b32 a212, v228\n"
      "v_mfma_f32_16x16x32_bf16 v[36:39], a[100:103], v[136:139], v[36:39]\n"
      "s_add_u32 s68, s57, 0x2002000\n"
      "v_exp_f32 v176, v176\n"
      "v_mfma_f32_16x16x32_bf16 v[40:43], a[104:107], v[136:139], v[40:43]\n"
      "buffer_load_dwordx4 v212, s[44:47], s68 offen nt lds\n"
      "v_lshlrev_b32 v222, 16, v229\n"
      "v_mfma_f32_16x16x32_bf16 v[44:47], a[108:111], v[136:139], v[44:47]\n"
      "v_exp_f32 v177, v177\n"
      "v_mfma_f32_16x16x32_bf16 v[48:51], a[112:115], v[136:139], v[48:51]\n"
      "buffer_load_dwordx4 v213, s[44:47], s68 offen offset:1024 nt lds\n"
      "v_cvt_pk_bf16_f32 v176, v176, v177\n"
      "v_mfma_f32_16x16x32_bf16 v[52:55], a[116:119], v[136:139], v[52:55]\n"
      "v_exp_f32 v178, v178\n"
      "v_mfma_f32_16x16x32_bf16 v[56:59], a[120:123], v[136:139], v[56:59]\n"
      "v_and_b32 v229, s62, v229\n"
      "v_mul_f32 v222, s61, v222\n"
      "v_mfma_f32_16x16x32_bf16 v[60:63], a[124:127], v[136:139], v[60:63]\n"
      "v_exp_f32 v179, v179\n"
      "v_mfma_f32_16x16x32_bf16 a[136:139], a[144:147], v[144:147], a[136:139]\n"
      "v_cvt_pk_bf16_f32 v177, v178, v179\n"
      "v_mul_f32 v229, s61, v229\n"
      "v_mfma_f32_16x16x32_bf16 v[64:67], a[96:99], v[144:147], v[64:67]\n"
      "v_exp_f32 v180, v180\n"
      "v_mfma_f32_16x16x32_bf16 v[68:71], a[100:103], v[144:147], v[68:71]\n"
      "v_cvt_pk_bf16_f32 v229, v222, v229\n"
      "v_accvgpr_write_b32 a213, v229\n"
      "v_mfma_f32_16x16x32_bf16 v[72:75], a[104:107], v[144:147], v[72:75]\n"
      "v_exp_f32 v181, v181\n"
      "v_mfma_f32_16x16x32_bf16 v[76:79], a[108:111], v[144:147], v[76:79]\n"
      "v_cvt_pk_bf16_f32 v178, v180, v181\n"
      "v_lshlrev_b32 v221, 16, v230\n"
      "v_mfma_f32_16x16x32_bf16 v[80:83], a[112:115], v[144:147], v[80:83]\n"
      "v_exp_f32 v182, v182\n"
      "v_mfma_f32_16x16x32_bf16 v[84:87], a[116:119], v[144:147], v[84:87]\n"
      "s_add_u32 m0, s58, 0xe000\n"
      "ds_read_b128 v[224:227], v209 offset:5120\n"
      "v_and_b32 v230, s62, v230\n"
      "v_mfma_f32_16x16x32_bf16 v[88:91], a[120:123], v[144:147], v[88:91]\n"
      "s_add_u32 s67, s57, 0x1e000\n"
      "v_exp_f32 v183, v183\n"
      "v_mfma_f32_16x16x32_bf16 v[92:95], a[124:127], v[144:147], v[92:95]\n"
      "buffer_load_dwordx4 v212, s[48:51], s67 offen nt lds\n"
      "v_cvt_pk_bf16_f32 v179, v182, v183\n"
      "v_mfma_f32_16x16x32_bf16 a[140:143], a[144:147], v[152:155], a[140:143]\n"
      "v_exp_f32 v184, v184\n"
      "v_mfma_f32_16x16x32_bf16 v[96:99], a[96:99], v[152:155], v[96:99]\n"
      "buffer_load_dwordx4 v213, s[48:51], s67 offen offset:1024 nt lds\n"
      "v_mul_f32 v221, s61, v221\n"
      "v_mfma_f32_16x16x32_bf16 v[100:103], a[100:103], v[152:155], v[100:103]\n"
      "v_exp_f32 v185, v185\n"
      "v_mfma_f32_16x16x32_bf16 v[104:107], a[104:107], v[152:155], v[104:107]\n"
      "v_cvt_pk_bf16_f32 v184, v184, v185\n"
      "v_mul_f32 v230, s61, v230\n"
      "v_mfma_f32_16x16x32_bf16 v[108:111], a[108:111], v[152:155], v[108:111]\n"
      "v_exp_f32 v186, v186\n"
      "v_mfma_f32_16x16x32_bf16 v[112:115], a[112:115], v[152:155], v[112:115]\n"
      "v_cvt_pk_bf16_f32 v230, v221, v230\n"
      "v_accvgpr_write_b32 a214, v230\n"
      "v_mfma_f32_16x16x32_bf16 v[116:119], a[116:119], v[152:155], v[116:119]\n"
      "v_lshlrev_b32 v222, 16, v231\n"
      "v_and_b32 v231, s62, v231\n"
      "v_mfma_f32_16x16x32_bf16 v[120:123], a[120:123], v[152:155], v[120:123]\n"
      "v_mul_f32 v222, s61, v222\n"
      "v_mul_f32 v231, s61, v231\n"
      "v_mfma_f32_16x16x32_bf16 v[124:127], a[124:127], v[152:155], v[124:127]\n"
      "v_cvt_pk_bf16_f32 v231, v222, v231\n"
      "v_accvgpr_write_b32 a215, v231\n"
      "s_waitcnt lgkmcnt(0)\n"
      "v_mfma_f32_16x16x32_bf16 v[128:131], a[64:67], a[0:3], v[192:195]\n"
      "s_waitcnt vmcnt(14)\n"
      "s_barrier\n"
      "v_lshlrev_b32 v221, 16, v224\n"
      "v_and_b32 v224, s62, v224\n"
      "v_mfma_f32_16x16x32_bf16 v[128:131], a[68:71], a[4:7], v[128:131]\n"
      "v_exp_f32 v187, v187\n"
      "v_mfma_f32_16x16x32_bf16 v[128:131], a[72:75], a[8:11], v[128:131]\n"
      "ds_read_b128 a[152:155], v220 offset:21504\n"
      "v_cvt_pk_bf16_f32 v185, v186, v187\n"
      "v_mfma_f32_16x16x32_bf16 v[128:131], a[76:79], a[12:15], v[128:131]\n"
      "v_exp_f32 v188, v188\n"
      "v_mfma_f32_16x16x32_bf16 v[132:135], a[80:83], a[0:3], v[192:195]\n"
      "ds_read_b64_tr_b16 a[96:97], v210 offset:40960\n"
      "ds_read_b64_tr_b16 a[98:99], v210 offset:45056\n"
      "v_mfma_f32_16x16x32_bf16 v[132:135], a[84:87], a[4:7], v[132:135]\n"
      "v_exp_f32 v189, v189\n"
      "v_mfma_f32_16x16x32_bf16 v[132:135], a[88:91], a[8:11], v[132:135]\n"
      "ds_read_b64_tr_b16 a[100:101], v211 offset:40960\n"
      "v_cvt_pk_bf16_f32 v186, v188, v189\n"
      "v_mfma_f32_16x16x32_bf16 v[132:135], a[92:95], a[12:15], v[132:135]\n"
      "v_exp_f32 v190, v190\n"
      "v_mfma_f32_16x16x32_bf16 v[136:139], a[64:67], a[16:19], v[196:199]\n"
      "ds_read_b64_tr_b16 a[102:103], v211 offset:45056\n"
      "ds_read_b64_tr_b16 a[104:105], v210 offset:41472\n"
      "v_mfma_f32_16x16x32_bf16 v[136:139], a[68:71], a[20:23], v[136:139]\n"
      "v_exp_f32 v191, v191\n"
      "v_mfma_f32_16x16x32_bf16 v[136:139], a[72:75], a[24:27], v[136:139]\n"
      "ds_read_b64_tr_b16 a[106:107], v210 offset:45568\n"
      "v_cvt_pk_bf16_f32 v187, v190, v191\n"
      "v_mfma_f32_16x16x32_bf16 v[136:139], a[76:79], a[28:31], v[136:139]\n"
      "v_exp_f32 v128, v128\n"
      "v_mfma_f32_16x16x32_bf16 v[140:143], a[80:83], a[16:19], v[196:199]\n"
      "ds_read_b64_tr_b16 a[108:109], v211 offset:41472\n"
      "ds_read_b64_tr_b16 a[110:111], v211 offset:45568\n"
      "v_mfma_f32_16x16x32_bf16 v[140:143], a[84:87], a[20:23], v[140:143]\n"
      "v_exp_f32 v129, v129\n"
      "v_mfma_f32_16x16x32_bf16 v[140:143], a[88:91], a[24:27], v[140:143]\n"
      "ds_read_b64_tr_b16 a[112:113], v210 offset:41984\n"
      "v_cvt_pk_bf16_f32 v128, v128, v129\n"
      "v_mfma_f32_16x16x32_bf16 v[140:143], a[92:95], a[28:31], v[140:143]\n"
      "v_exp_f32 v130, v130\n"
      "v_mfma_f32_16x16x32_bf16 v[144:147], a[64:67], a[32:35], v[200:203]\n"
      "ds_read_b64_tr_b16 a[114:115], v210 offset:46080\n"
      "ds_read_b64_tr_b16 a[116:117], v211 offset:41984\n"
      "v_mfma_f32_16x16x32_bf16 v[144:147], a[68:71], a[36:39], v[144:147]\n"
      "v_exp_f32 v131, v131\n"
      "v_mfma_f32_16x16x32_bf16 v[144:147], a[72:75], a[40:43], v[144:147]\n"
      "ds_read_b64_tr_b16 a[118:119], v211 offset:46080\n"
      "v_cvt_pk_bf16_f32 v129, v130, v131\n"
      "v_mfma_f32_16x16x32_bf16 v[144:147], a[76:79], a[44:47], v[144:147]\n"
      "v_exp_f32 v132, v132\n"
      "v_mfma_f32_16x16x32_bf16 v[148:151], a[80:83], a[32:35], v[200:203]\n"
      "ds_read_b64_tr_b16 a[120:121], v210 offset:42496\n"
      "ds_read_b64_tr_b16 a[122:123], v210 offset:46592\n"
      "v_mfma_f32_16x16x32_bf16 v[148:151], a[84:87], a[36:39], v[148:151]\n"
      "v_exp_f32 v133, v133\n"
      "v_mfma_f32_16x16x32_bf16 v[148:151], a[88:91], a[40:43], v[148:151]\n"
      "ds_read_b64_tr_b16 a[124:125], v211 offset:42496\n"
      "v_cvt_pk_bf16_f32 v130, v132, v133\n"
      "v_mfma_f32_16x16x32_bf16 v[148:151], a[92:95], a[44:47], v[148:151]\n"
      "v_exp_f32 v134, v134\n"
      "v_mfma_f32_16x16x32_bf16 v[152:155], a[64:67], a[48:51], v[204:207]\n"
      "ds_read_b64_tr_b16 a[126:127], v211 offset:46592\n"
      "v_mul_f32 v221, s61, v221\n"
      "v_mfma_f32_16x16x32_bf16 v[152:155], a[68:71], a[52:55], v[152:155]\n"
      "v_exp_f32 v135, v135\n"
      "v_mfma_f32_16x16x32_bf16 v[152:155], a[72:75], a[56:59], v[152:155]\n"
      "v_cvt_pk_bf16_f32 v131, v134, v135\n"
      "v_mul_f32 v224, s61, v224\n"
      "v_mfma_f32_16x16x32_bf16 v[152:155], a[76:79], a[60:63], v[152:155]\n"
      "v_exp_f32 v136, v136\n"
      "v_mfma_f32_16x16x32_bf16 v[156:159], a[80:83], a[48:51], v[204:207]\n"
      "ds_read_b128 a[64:67], v208 offset:24576\n"
      "ds_read_b128 a[68:71], v208 offset:25088\n"
      "v_mfma_f32_16x16x32_bf16 v[156:159], a[84:87], a[52:55], v[156:159]\n"
      "v_exp_f32 v137, v137\n"
      "v_mfma_f32_16x16x32_bf16 v[156:159], a[88:91], a[56:59], v[156:159]\n"
      "ds_read_b128 a[72:75], v208 offset:25600\n"
      "v_cvt_pk_bf16_f32 v136, v136, v137\n"
      "v_mfma_f32_16x16x32_bf16 v[156:159], a[92:95], a[60:63], v[156:159]\n"
      "v_exp_f32 v138, v138\n"
      "v_mfma_f32_16x16x32_bf16 a[128:131], a[144:147], v[160:163], a[128:131]\n"
      "ds_read_b128 a[76:79], v208 offset:26112\n"
      "v_cvt_pk_bf16_f32 v224, v221, v224\n"
      "s_waitcnt lgkmcnt(12)\n"
      "v_mfma_f32_16x16x32_bf16 v[0:3], a[96:99], v[160:163], v[0:3]\n"
      "v_exp_f32 v139, v139\n"
      "v_mfma_f32_16x16x32_bf16 v[4:7], a[100:103], v[160:163], v[4:7]\n"
      "ds_read_b128 a[80:83], v208 offset:28672\n"
      "v_cvt_pk_bf16_f32 v137, v138, v139\n"
      "v_mfma_f32_16x16x32_bf16 v[8:11], a[104:107], v[160:163], v[8:11]\n"
      "v_exp_f32 v140, v140\n"
      "v_mfma_f32_16x16x32_bf16 v[12:15], a[108:111], v[160:163], v[12:15]\n"
      "ds_read_b128 a[84:87], v208 offset:29184\n"
      "ds_read_b128 a[88:91], v208 offset:29696\n"
      "s_waitcnt lgkmcnt(7)\n"
      "v_mfma_f32_16x16x32_bf16 v[16:19], a[112:115], v[160:163], v[16:19]\n"
      "v_exp_f32 v141, v141\n"
      "v_mfma_f32_16x16x32_bf16 v[20:23], a[116:119], v[160:163], v[20:23]\n"
      "ds_read_b128 a[92:95], v208 offset:30208\n"
      "v_cvt_pk_bf16_f32 v138, v140, v141\n"
      "v_mfma_f32_16x16x32_bf16 v[24:27], a[120:123], v[160:163], v[24:27]\n"
      "v_exp_f32 v142, v142\n"
      "v_mfma_f32_16x16x32_bf16 v[28:31], a[124:127], v[160:163], v[28:31]\n"
      "buffer_store_dwordx4 a[152:155], v214, s[52:55], s79 offen offset:1024 sc1\n"
      "v_mfma_f32_16x16x32_bf16 a[132:135], a[144:147], v[168:171], a[132:135]\n"
      "v_exp_f32 v143, v143\n"
      "v_mfma_f32_16x16x32_bf16 v[32:35], a[96:99], v[168:171], v[32:35]\n"
      "s_add_u32 m0, s58, 0x4000\n"
      "v_cvt_pk_bf16_f32 v139, v142, v143\n"
      "v_accvgpr_write_b32 a216, v224\n"
      "v_mfma_f32_16x16x32_bf16 v[36:39], a[100:103], v[168:171], v[36:39]\n"
      "s_add_u32 s68, s57, 0x2004000\n"
      "v_exp_f32 v144, v144\n"
      "v_mfma_f32_16x16x32_bf16 v[40:43], a[104:107], v[168:171], v[40:43]\n"
      "buffer_load_dwordx4 v212, s[44:47], s68 offen nt lds\n"
      "v_lshlrev_b32 v222, 16, v225\n"
      "v_mfma_f32_16x16x32_bf16 v[44:47], a[108:111], v[168:171], v[44:47]\n"
      "v_exp_f32 v145, v145\n"
      "v_mfma_f32_16x16x32_bf16 v[48:51], a[112:115], v[168:171], v[48:51]\n"
      "buffer_load_dwordx4 v213, s[44:47], s68 offen offset:1024 nt lds\n"
      "v_cvt_pk_bf16_f32 v144, v144, v145\n"
      "v_mfma_f32_16x16x32_bf16 v[52:55], a[116:119], v[168:171], v[52:55]\n"
      "v_exp_f32 v146, v146\n"
      "v_mfma_f32_16x16x32_bf16 v[56:59], a[120:123], v[168:171], v[56:59]\n"
      "v_and_b32 v225, s62, v225\n"
      "v_mul_f32 v222, s61, v222\n"
      "v_mfma_f32_16x16x32_bf16 v[60:63], a[124:127], v[168:171], v[60:63]\n"
      "v_exp_f32 v147, v147\n"
      "v_mfma_f32_16x16x32_bf16 a[136:139], a[144:147], v[176:179], a[136:139]\n"
      "v_cvt_pk_bf16_f32 v145, v146, v147\n"
      "v_mul_f32 v225, s61, v225\n"
      "v_mfma_f32_16x16x32_bf16 v[64:67], a[96:99], v[176:179], v[64:67]\n"
      "v_exp_f32 v148, v148\n"
      "v_mfma_f32_16x16x32_bf16 v[68:71], a[100:103], v[176:179], v[68:71]\n"
      "v_cvt_pk_bf16_f32 v225, v222, v225\n"
      "v_accvgpr_write_b32 a217, v225\n"
      "v_mfma_f32_16x16x32_bf16 v[72:75], a[104:107], v[176:179], v[72:75]\n"
      "v_exp_f32 v149, v149\n"
      "v_mfma_f32_16x16x32_bf16 v[76:79], a[108:111], v[176:179], v[76:79]\n"
      "v_cvt_pk_bf16_f32 v146, v148, v149\n"
      "v_lshlrev_b32 v221, 16, v226\n"
      "v_mfma_f32_16x16x32_bf16 v[80:83], a[112:115], v[176:179], v[80:83]\n"
      "v_exp_f32 v150, v150\n"
      "v_mfma_f32_16x16x32_bf16 v[84:87], a[116:119], v[176:179], v[84:87]\n"
      "s_add_u32 m0, s58, 0x8000\n"
      "ds_read_b128 v[228:231], v209 offset:5632\n"
      "v_and_b32 v226, s62, v226\n"
      "v_mfma_f32_16x16x32_bf16 v[88:91], a[120:123], v[176:179], v[88:91]\n"
      "s_add_u32 s67, s57, 0x2000000\n"
      "v_exp_f32 v151, v151\n"
      "v_mfma_f32_16x16x32_bf16 v[92:95], a[124:127], v[176:179], v[92:95]\n"
      "buffer_load_dwordx4 v212, s[48:51], s67 offen nt lds\n"
      "v_cvt_pk_bf16_f32 v147, v150, v151\n"
      "v_mfma_f32_16x16x32_bf16 a[140:143], a[144:147], v[184:187], a[140:143]\n"
      "v_exp_f32 v152, v152\n"
      "v_mfma_f32_16x16x32_bf16 v[96:99], a[96:99], v[184:187], v[96:99]\n"
      "buffer_load_dwordx4 v213, s[48:51], s67 offen offset:1024 nt lds\n"
      "v_mul_f32 v221, s61, v221\n"
      "v_mfma_f32_16x16x32_bf16 v[100:103], a[100:103], v[184:187], v[100:103]\n"
      "v_exp_f32 v153, v153\n"
      "v_mfma_f32_16x16x32_bf16 v[104:107], a[104:107], v[184:187], v[104:107]\n"
      "v_cvt_pk_bf16_f32 v152, v152, v153\n"
      "v_mul_f32 v226, s61, v226\n"
      "v_mfma_f32_16x16x32_bf16 v[108:111], a[108:111], v[184:187], v[108:111]\n"
      "v_exp_f32 v154, v154\n"
      "v_mfma_f32_16x16x32_bf16 v[112:115], a[112:115], v[184:187], v[112:115]\n"
      "v_cvt_pk_bf16_f32 v226, v221, v226\n"
      "v_accvgpr_write_b32 a218, v226\n"
      "v_mfma_f32_16x16x32_bf16 v[116:119], a[116:119], v[184:187], v[116:119]\n"
      "v_lshlrev_b32 v222, 16, v227\n"
      "v_and_b32 v227, s62, v227\n"
      "v_mfma_f32_16x16x32_bf16 v[120:123], a[120:123], v[184:187], v[120:123]\n"
      "s_memrealtime s[84:85]\n"
      "v_mul_f32 v222, s61, v222\n"
      "v_mul_f32 v227, s61, v227\n"
      "v_mfma_f32_16x16x32_bf16 v[124:127], a[124:127], v[184:187], v[124:127]\n"
      "v_cvt_pk_bf16_f32 v227, v222, v227\n"
      "v_accvgpr_write_b32 a219, v227\n"
      "s_waitcnt lgkmcnt(0)\n"
      "s_sub_u32 s85, s84, s86\n"
      "s_cmp_ge_u32 s85, s87\n"
      "s_cbranch_scc1 L_PACED0_14\n"
      "L_PACE0_14:\n"
      "s_memrealtime s[84:85]\n"
      "s_waitcnt lgkmcnt(0)\n"
      "s_sub_u32 s85, s84, s86\n"
      "s_cmp_lt_u32 s85, s87\n"
      "s_cbranch_scc1 L_PACE0_14\n"
      "L_PACED0_14:\n"
      "s_mov_b32 s86, s84\n"
      "v_mfma_f32_16x16x32_bf16 v[160:163], a[64:67], a[0:3], v[192:195]\n"
      "s_waitcnt vmcnt(12)\n"
      "s_barrier\n"
      "v_lshlrev_b32 v221, 16, v228\n"
      "v_and_b32 v228, s62, v228\n"
      "v_mul_f32 v221, s61, v221\n"
      "v_mfma_f32_16x16x32_bf16 v[160:163], a[68:71], a[4:7], v[160:163]\n"
      "v_exp_f32 v155, v155\n"
      "v_mfma_f32_16x16x32_bf16 v[160:163], a[72:75], a[8:11], v[160:163]\n"
      "ds_read_b128 a[148:151], v219 offset:22528\n"
      "v_cvt_pk_bf16_f32 v153, v154, v155\n"
      "v_mfma_f32_16x16x32_bf16 v[160:163], a[76:79], a[12:15], v[160:163]\n"
      "v_exp_f32 v156, v156\n"
      "v_mfma_f32_16x16x32_bf16 v[164:167], a[80:83], a[0:3], v[192:195]\n"
      "ds_read_b64_tr_b16 a[96:97], v210 offset:49152\n"
      "ds_read_b64_tr_b16 a[98:99], v210 offset:53248\n"
      "v_mfma_f32_16x16x32_bf16 v[164:167], a[84:87], a[4:7], v[164:167]\n"
      "v_exp_f32 v157, v157\n"
      "v_mfma_f32_16x16x32_bf16 v[164:167], a[88:91], a[8:11], v[164:167]\n"
      "ds_read_b64_tr_b16 a[100:101], v211 offset:49152\n"
      "v_cvt_pk_bf16_f32 v154, v156, v157\n"
      "v_mfma_f32_16x16x32_bf16 v[164:167], a[92:95], a[12:15], v[164:167]\n"
      "v_exp_f32 v158, v158\n"
      "v_mfma_f32_16x16x32_bf16 v[168:171], a[64:67], a[16:19], v[196:199]\n"
      "ds_read_b64_tr_b16 a[102:103], v211 offset:53248\n"
      "ds_read_b64_tr_b16 a[104:105], v210 offset:49664\n"
      "v_mfma_f32_16x16x32_bf16 v[168:171], a[68:71], a[20:23], v[168:171]\n"
      "v_exp_f32 v159, v159\n"
      "v_mfma_f32_16x16x32_bf16 v[168:171], a[72:75], a[24:27], v[168:171]\n"
      "ds_read_b64_tr_b16 a[106:107], v210 offset:53760\n"
      "v_cvt_pk_bf16_f32 v155, v158, v159\n"
      "v_mfma_f32_16x16x32_bf16 v[168:171], a[76:79], a[28:31], v[168:171]\n"
      "v_exp_f32 v160, v160\n"
      "v_mfma_f32_16x16x32_bf16 v[172:175], a[80:83], a[16:19], v[196:199]\n"
      "ds_read_b64_tr_b16 a[108:109], v211 offset:49664\n"
      "ds_read_b64_tr_b16 a[110:111], v211 offset:53760\n"
      "v_mfma_f32_16x16x32_bf16 v[172:175], a[84:87], a[20:23], v[172:175]\n"
      "v_exp_f32 v161, v161\n"
      "v_mfma_f32_16x16x32_bf16 v[172:175], a[88:91], a[24:27], v[172:175]\n"
      "ds_read_b64_tr_b16 a[112:113], v210 offset:50176\n"
      "v_cvt_pk_bf16_f32 v160, v160, v161\n"
      "v_mfma_f32_16x16x32_bf16 v[172:175], a[92:95], a[28:31], v[172:175]\n"
      "v_exp_f32 v162, v162\n"
      "v_mfma_f32_16x16x32_bf16 v[176:179], a[64:67], a[32:35], v[200:203]\n"
      "ds_read_b64_tr_b16 a[114:115], v210 offset:54272\n"
      "ds_read_b64_tr_b16 a[116:117], v211 offset:50176\n"
      "v_mfma_f32_16x16x32_bf16 v[176:179], a[68:71], a[36:39], v[176:179]\n"
      "v_exp_f32 v163, v163\n"
      "v_mfma_f32_16x16x32_bf16 v[176:179], a[72:75], a[40:43], v[176:179]\n"
      "ds_read_b64_tr_b16 a[118:119], v211 offset:54272\n"
      "v_cvt_pk_bf16_f32 v161, v162, v163\n"
      "v_mfma_f32_16x16x32_bf16 v[176:179], a[76:79], a[44:47], v[176:179]\n"
      "v_exp_f32 v164, v164\n"
      "v_mfma_f32_16x16x32_bf16 v[180:183], a[80:83], a[32:35], v[200:203]\n"
      "ds_read_b64_tr_b16 a[120:121], v210 offset:50688\n"
      "ds_read_b64_tr_b16 a[122:123], v210 offset:54784\n"
      "v_mfma_f32_16x16x32_bf16 v[180:183], a[84:87], a[36:39], v[180:183]\n"
      "v_exp_f32 v165, v165\n"
      "v_mfma_f32_16x16x32_bf16 v[180:183], a[88:91], a[40:43], v[180:183]\n"
      "ds_read_b64_tr_b16 a[124:125], v211 offset:50688\n"
      "v_cvt_pk_bf16_f32 v162, v164, v165\n"
      "v_mfma_f32_16x16x32_bf16 v[180:183], a[92:95], a[44:47], v[180:183]\n"
      "v_exp_f32 v166, v166\n"
      "v_mfma_f32_16x16x32_bf16 v[184:187], a[64:67], a[48:51], v[204:207]\n"
      "ds_read_b64_tr_b16 a[126:127], v211 offset:54784\n"
      "v_mul_f32 v228, s61, v228\n"
      "v_mfma_f32_16x16x32_bf16 v[184:187], a[68:71], a[52:55], v[184:187]\n"
      "v_exp_f32 v167, v167\n"
      "v_mfma_f32_16x16x32_bf16 v[184:187], a[72:75], a[56:59], v[184:187]\n"
      "v_cvt_pk_bf16_f32 v163, v166, v167\n"
      "v_cvt_pk_bf16_f32 v228, v221, v228\n"
      "v_mfma_f32_16x16x32_bf16 v[184:187], a[76:79], a[60:63], v[184:187]\n"
      "v_exp_f32 v168, v168\n"
      "v_mfma_f32_16x16x32_bf16 v[188:191], a[80:83], a[48:51], v[204:207]\n"
      "ds_read_b128 a[64:67], v208 offset:0\n"
      "ds_read_b128 a[68:71], v208 offset:512\n"
      "v_mfma_f32_16x16x32_bf16 v[188:191], a[84:87], a[52:55], v[188:191]\n"
      "v_exp_f32 v169, v169\n"
      "v_mfma_f32_16x16x32_bf16 v[188:191], a[88:91], a[56:59], v[188:191]\n"
      "ds_read_b128 a[72:75], v208 offset:1024\n"
      "v_cvt_pk_bf16_f32 v168, v168, v169\n"
      "v_mfma_f32_16x16x32_bf16 v[188:191], a[92:95], a[60:63], v[188:191]\n"
      "v_exp_f32 v170, v170\n"
      "v_mfma_f32_16x16x32_bf16 a[128:131], a[144:147], v[128:131], a[128:131]\n"
      "ds_read_b128 a[76:79], v208 offset:1536\n"
      "v_accvgpr_write_b32 a220, v228\n"
      "s_waitcnt lgkmcnt(12)\n"
      "v_mfma_f32_16x16x32_bf16 v[0:3], a[96:99], v[128:131], v[0:3]\n"
      "v_exp_f32 v171, v171\n"
      "v_mfma_f32_16x16x32_bf16 v[4:7], a[100:103], v[128:131], v[4:7]\n"
      "ds_read_b128 a[80:83], v208 offset:4096\n"
      "v_cvt_pk_bf16_f32 v169, v170, v171\n"
      "v_mfma_f32_16x16x32_bf16 v[8:11], a[104:107], v[128:131], v[8:11]\n"
      "v_exp_f32 v172, v172\n"
      "v_mfma_f32_16x16x32_bf16 v[12:15], a[108:111], v[128:131], v[12:15]\n"
      "ds_read_b128 a[84:87], v208 offset:4608\n"
      "ds_read_b128 a[88:91], v208 offset:5120\n"
      "s_waitcnt lgkmcnt(7)\n"
      "v_mfma_f32_16x16x32_bf16 v[16:19], a[112:115], v[128:131], v[16:19]\n"
      "v_exp_f32 v173, v173\n"
      "v_mfma_f32_16x16x32_bf16 v[20:23], a[116:119], v[128:131], v[20:23]\n"
      "ds_read_b128 a[92:95], v208 offset:5632\n"
      "v_cvt_pk_bf16_f32 v170, v172, v173\n"
      "v_mfma_f32_16x16x32_bf16 v[24:27], a[120:123], v[128:131], v[24:27]\n"
      "v_exp_f32 v174, v174\n"
      "v_mfma_f32_16x16x32_bf16 v[28:31], a[124:127], v[128:131], v[28:31]\n"
      "s_add_u32 m0, s58, 0x6000\n"
      "buffer_store_dwordx4 a[148:151], v214, s[52:55], s79 offen offset:2048 sc1\n"
      "v_mfma_f32_16x16x32_bf16 a[132:135], a[144:147], v[136:139], a[132:135]\n"
      "s_add_u32 s67, s57, 0x2006000\n"
      "v_exp_f32 v175, v175\n"
      "v_mfma_f32_16x16x32_bf16 v[32:35], a[96:99], v[136:139], v[32:35]\n"
      "buffer_load_dwordx4 v212, s[44:47], s67 offen nt lds\n"
      "v_cvt_pk_bf16_f32 v171, v174, v175\n"
      "v_mfma_f32_16x16x32_bf16 v[36:39], a[100:103], v[136:139], v[36:39]\n"
      "v_exp_f32 v176, v176\n"
      "v_mfma_f32_16x16x32_bf16 v[40:43], a[104:107], v[136:139], v[40:43]\n"
      "buffer_load_dwordx4 v213, s[44:47], s67 offen offset:1024 nt lds\n"
      "v_lshlrev_b32 v222, 16, v229\n"
      "v_mfma_f32_16x16x32_bf16 v[44:47], a[108:111], v[136:139], v[44:47]\n"
      "v_exp_f32 v177, v177\n"
      "v_mfma_f32_16x16x32_bf16 v[48:51], a[112:115], v[136:139], v[48:51]\n"
      "v_cvt_pk_bf16_f32 v176, v176, v177\n"
      "v_and_b32 v229, s62, v229\n"
      "v_mfma_f32_16x16x32_bf16 v[52:55], a[116:119], v[136:139], v[52:55]\n"
      "v_exp_f32 v178, v178\n"
      "v_mfma_f32_16x16x32_bf16 v[56:59], a[120:123], v[136:139], v[56:59]\n"
      "v_mul_f32 v222, s61, v222\n"
      "v_mul_f32 v229, s61, v229\n"
      "v_mfma_f32_16x16x32_bf16 v[60:63], a[124:127], v[136:139], v[60:63]\n"
      "v_exp_f32 v179, v179\n"
      "v_mfma_f32_16x16x32_bf16 a[136:139], a[144:147], v[144:147], a[136:139]\n"
      "s_add_u32 m0, s58, 0xa000\n"
      "v_cvt_pk_bf16_f32 v177, v178, v179\n"
      "v_cvt_pk_bf16_f32 v229, v222, v229\n"
      "v_mfma_f32_16x16x32_bf16 v[64:67], a[96:99], v[144:147], v[64:67]\n"
      "s_add_u32 s68, s57, 0x2002000\n"
      "v_exp_f32 v180, v180\n"
      "v_mfma_f32_16x16x32_bf16 v[68:71], a[100:103], v[144:147], v[68:71]\n"
      "buffer_load_dwordx4 v212, s[48:51], s68 offen nt lds\n"
      "v_accvgpr_write_b32 a221, v229\n"
      "v_mfma_f32_16x16x32_bf16 v[72:75], a[104:107], v[144:147], v[72:75]\n"
      "v_exp_f32 v181, v181\n"
      "v_mfma_f32_16x16x32_bf16 v[76:79], a[108:111], v[144:147], v[76:79]\n"
      "buffer_load_dwordx4 v213, s[48:51], s68 offen offset:1024 nt lds\n"
      "v_cvt_pk_bf16_f32 v178, v180, v181\n"
      "v_mfma_f32_16x16x32_bf16 v[80:83], a[112:115], v[144:147], v[80:83]\n"
      "v_exp_f32 v182, v182\n"
      "v_mfma_f32_16x16x32_bf16 v[84:87], a[116:119], v[144:147], v[84:87]\n"
      "v_lshlrev_b32 v221, 16, v230\n"
      "v_and_b32 v230, s62, v230\n"
      "v_mfma_f32_16x16x32_bf16 v[88:91], a[120:123], v[144:147], v[88:91]\n"
      "v_exp_f32 v183, v183\n"
      "v_mfma_f32_16x16x32_bf16 v[92:95], a[124:127], v[144:147], v[92:95]\n"
      "s_add_u32 m0, s59, 0x1000\n"
      "v_cvt_pk_bf16_f32 v179, v182, v183\n"
      "v_mul_f32 v221, s61, v221\n"
      "v_mfma_f32_16x16x32_bf16 a[140:143], a[144:147], v[152:155], a[140:143]\n"
      "s_add_u32 s68, s56, 0x2001000\n"
      "v_exp_f32 v184, v184\n"
      "v_mfma_f32_16x16x32_bf16 v[96:99], a[96:99], v[152:155], v[96:99]\n"
      "buffer_load_dwordx4 v212, s[40:43], s68 offen nt lds\n"
      "v_mul_f32 v230, s61, v230\n"
      "v_mfma_f32_16x16x32_bf16 v[100:103], a[100:103], v[152:155], v[100:103]\n"
      "v_exp_f32 v185, v185\n"
      "v_mfma_f32_16x16x32_bf16 v[104:107], a[104:107], v[152:155], v[104:107]\n"
      "buffer_load_dwordx4 v213, s[40:43], s68 offen offset:1024 nt lds\n"
      "v_cvt_pk_bf16_f32 v184, v184, v185\n"
      "v_mfma_f32_16x16x32_bf16 v[108:111], a[108:111], v[152:155], v[108:111]\n"
      "v_exp_f32 v186, v186\n"
      "v_mfma_f32_16x16x32_bf16 v[112:115], a[112:115], v[152:155], v[112:115]\n"
      "v_cvt_pk_bf16_f32 v230, v221, v230\n"
      "v_accvgpr_write_b32 a222, v230\n"
      "v_mfma_f32_16x16x32_bf16 v[116:119], a[116:119], v[152:155], v[116:119]\n"
      "v_lshlrev_b32 v222, 16, v231\n"
      "v_and_b32 v231, s62, v231\n"
      "v_mfma_f32_16x16x32_bf16 v[120:123], a[120:123], v[152:155], v[120:123]\n"
      "v_mul_f32 v222, s61, v222\n"
      "v_mul_f32 v231, s61, v231\n"
      "v_mfma_f32_16x16x32_bf16 v[124:127], a[124:127], v[152:155], v[124:127]\n"
      "v_cvt_pk_bf16_f32 v231, v222, v231\n"
      "v_accvgpr_write_b32 a223, v231\n"
      "s_waitcnt lgkmcnt(0)\n"
      "v_mfma_f32_16x16x32_bf16 v[128:131], a[64:67], a[160:163], 0\n"
      "s_waitcnt vmcnt(12)\n"
      "s_barrier\n"
      "v_mfma_f32_16x16x32_bf16 v[128:131], a[68:71], a[164:167], v[128:131]\n"
      "v_exp_f32 v187, v187\n"
      "v_mfma_f32_16x16x32_bf16 v[128:131], a[72:75], a[168:171], v[128:131]\n"
      "ds_read_b128 a[152:155], v220 offset:23552\n"
      "v_cvt_pk_bf16_f32 v185, v186, v187\n"
      "v_mfma_f32_16x16x32_bf16 v[128:131], a[76:79], a[172:175], v[128:131]\n"
      "v_exp_f32 v188, v188\n"
      "v_mfma_f32_16x16x32_bf16 v[132:135], a[80:83], a[160:163], 0\n"
      "ds_read_b64_tr_b16 a[96:97], v210 offset:57344\n"
      "ds_read_b64_tr_b16 a[98:99], v210 offset:61440\n"
      "v_mfma_f32_16x16x32_bf16 v[132:135], a[84:87], a[164:167], v[132:135]\n"
      "v_exp_f32 v189, v189\n"
      "v_mfma_f32_16x16x32_bf16 v[132:135], a[88:91], a[168:171], v[132:135]\n"
      "ds_read_b64_tr_b16 a[100:101], v211 offset:57344\n"
      "v_cvt_pk_bf16_f32 v186, v188, v189\n"
      "v_mfma_f32_16x16x32_bf16 v[132:135], a[92:95], a[172:175], v[132:135]\n"
      "v_exp_f32 v190, v190\n"
      "v_mfma_f32_16x16x32_bf16 v[136:139], a[64:67], a[176:179], 0\n"
      "ds_read_b64_tr_b16 a[102:103], v211 offset:61440\n"
      "ds_read_b64_tr_b16 a[104:105], v210 offset:57856\n"
      "v_mfma_f32_16x16x32_bf16 v[136:139], a[68:71], a[180:183], v[136:139]\n"
      "v_exp_f32 v191, v191\n"
      "v_mfma_f32_16x16x32_bf16 v[136:139], a[72:75], a[184:187], v[136:139]\n"
      "ds_read_b64_tr_b16 a[106:107], v210 offset:61952\n"
      "v_cvt_pk_bf16_f32 v187, v190, v191\n"
      "v_mfma_f32_16x16x32_bf16 v[136:139], a[76:79], a[188:191], v[136:139]\n"
      "ds_read_b64_tr_b16 a[108:109], v211 offset:57856\n"
      "ds_read_b64_tr_b16 a[110:111], v211 offset:61952\n"
      "v_mfma_f32_16x16x32_bf16 v[140:143], a[80:83], a[176:179], 0\n"
      "ds_read_b64_tr_b16 a[112:113], v210 offset:58368\n"
      "ds_read_b64_tr_b16 a[114:115], v210 offset:62464\n"
      "v_mfma_f32_16x16x32_bf16 v[140:143], a[84:87], a[180:183], v[140:143]\n"
      "ds_read_b64_tr_b16 a[116:117], v211 offset:58368\n"
      "ds_read_b64_tr_b16 a[118:119], v211 offset:62464\n"
      "v_mfma_f32_16x16x32_bf16 v[140:143], a[88:91], a[184:187], v[140:143]\n"
      "ds_read_b64_tr_b16 a[120:121], v210 offset:58880\n"
      "ds_read_b64_tr_b16 a[122:123], v210 offset:62976\n"
      "v_mfma_f32_16x16x32_bf16 v[140:143], a[92:95], a[188:191], v[140:143]\n"
      "ds_read_b64_tr_b16 a[124:125], v211 offset:58880\n"
      "ds_read_b64_tr_b16 a[126:127], v211 offset:62976\n"
      "v_mfma_f32_16x16x32_bf16 v[144:147], a[64:67], a[192:195], 0\n"
      "v_mfma_f32_16x16x32_bf16 v[144:147], a[68:71], a[196:199], v[144:147]\n"
      "v_mfma_f32_16x16x32_bf16 v[144:147], a[72:75], a[200:203], v[144:147]\n"
      "v_mfma_f32_16x16x32_bf16 v[144:147], a[76:79], a[204:207], v[144:147]\n"
      "v_max3_f32 v232, v128, v129, v130\n"
      "v_max3_f32 v233, v131, v132, v133\n"
      "v_mfma_f32_16x16x32_bf16 v[148:151], a[80:83], a[192:195], 0\n"
      "v_max3_f32 v232, v232, v134, v135\n"
      "v_max_f32 v232, v232, v233\n"
      "v_mfma_f32_16x16x32_bf16 v[148:151], a[84:87], a[196:199], v[148:151]\n"
      "v_max3_f32 v234, v136, v137, v138\n"
      "v_max3_f32 v235, v139, v140, v141\n"
      "v_mfma_f32_16x16x32_bf16 v[148:151], a[88:91], a[200:203], v[148:151]\n"
      "v_max3_f32 v234, v234, v142, v143\n"
      "v_max_f32 v234, v234, v235\n"
      "v_mfma_f32_16x16x32_bf16 v[148:151], a[92:95], a[204:207], v[148:151]\n"
      "v_mov_b32 v233, v232\n"
      "v_mov_b32 v235, v234\n"
      "v_mfma_f32_16x16x32_bf16 v[152:155], a[64:67], a[208:211], 0\n"
      "v_permlane32_swap_b32 v232, v233\n"
      "v_permlane32_swap_b32 v234, v235\n"
      "v_mfma_f32_16x16x32_bf16 v[152:155], a[68:71], a[212:215], v[152:155]\n"
      "v_max_f32 v232, v232, v233\n"
      "v_max_f32 v234, v234, v235\n"
      "v_mfma_f32_16x16x32_bf16 v[152:155], a[72:75], a[216:219], v[152:155]\n"
      "v_mov_b32 v233, v232\n"
      "v_mov_b32 v235, v234\n"
      "v_mfma_f32_16x16x32_bf16 v[152:155], a[76:79], a[220:223], v[152:155]\n"
      "v_permlane16_swap_b32 v232, v233\n"
      "v_permlane16_swap_b32 v234, v235\n"
      "v_mfma_f32_16x16x32_bf16 v[156:159], a[80:83], a[208:211], 0\n"
      "ds_read_b128 a[64:67], v208 offset:8192\n"
      "ds_read_b128 a[68:71], v208 offset:8704\n"
      "v_mfma_f32_16x16x32_bf16 v[156:159], a[84:87], a[212:215], v[156:159]\n"
      "ds_read_b128 a[72:75], v208 offset:9216\n"
      "ds_read_b128 a[76:79], v208 offset:9728\n"
      "v_mfma_f32_16x16x32_bf16 v[156:159], a[88:91], a[216:219], v[156:159]\n"
      "v_max_f32 v232, v232, v233\n"
      "v_max_f32 v234, v234, v235\n"
      "v_mfma_f32_16x16x32_bf16 v[156:159], a[92:95], a[220:223], v[156:159]\n"
      "v_sub_f32 v128, v128, v232\n"
      "v_sub_f32 v129, v129, v232\n"
      "v_mfma_f32_16x16x32_bf16 a[128:131], a[144:147], v[160:163], a[128:131]\n"
      "v_sub_f32 v130, v130, v232\n"
      "v_sub_f32 v131, v131, v232\n"
      "s_waitcnt lgkmcnt(12)\n"
      "v_mfma_f32_16x16x32_bf16 v[0:3], a[96:99], v[160:163], v[0:3]\n"
      "v_sub_f32 v132, v132, v232\n"
      "v_sub_f32 v133, v133, v232\n"
      "v_mfma_f32_16x16x32_bf16 v[4:7], a[100:103], v[160:163], v[4:7]\n"
      "ds_read_b128 a[80:83], v208 offset:12288\n"
      "ds_read_b128 a[84:87], v208 offset:12800\n"
      "v_mfma_f32_16x16x32_bf16 v[8:11], a[104:107], v[160:163], v[8:11]\n"
      "ds_read_b128 a[88:91], v208 offset:13312\n"
      "ds_read_b128 a[92:95], v208 offset:13824\n"
      "v_mfma_f32_16x16x32_bf16 v[12:15], a[108:111], v[160:163], v[12:15]\n"
      "v_sub_f32 v134, v134, v232\n"
      "v_sub_f32 v135, v135, v232\n"
      "v_max3_f32 v236, v144, v145, v146\n"
      "s_waitcnt lgkmcnt(8)\n"
      "v_mfma_f32_16x16x32_bf16 v[16:19], a[112:115], v[160:163], v[16:19]\n"
      "v_mul_f32 v192, -1.0, v232\n"
      "v_mul_f32 v193, -1.0, v232\n"
      "v_mfma_f32_16x16x32_bf16 v[20:23], a[116:119], v[160:163], v[20:23]\n"
      "v_mul_f32 v194, -1.0, v232\n"
      "v_mul_f32 v195, -1.0, v232\n"
      "v_mfma_f32_16x16x32_bf16 v[24:27], a[120:123], v[160:163], v[24:27]\n"
      "v_sub_f32 v136, v136, v234\n"
      "v_sub_f32 v137, v137, v234\n"
      "v_mfma_f32_16x16x32_bf16 v[28:31], a[124:127], v[160:163], v[28:31]\n"
      "s_mov_b32 m0, s58\n"
      "buffer_store_dwordx4 a[152:155], v214, s[52:55], s79 offen offset:3072 sc1\n"
      "v_mfma_f32_16x16x32_bf16 a[132:135], a[144:147], v[168:171], a[132:135]\n"
      "s_add_u32 s68, s57, 0x2008000\n"
      "v_sub_f32 v138, v138, v234\n"
      "v_sub_f32 v139, v139, v234\n"
      "v_max3_f32 v237, v147, v148, v149\n"
      "v_mfma_f32_16x16x32_bf16 v[32:35], a[96:99], v[168:171], v[32:35]\n"
      "buffer_load_dwordx4 v212, s[44:47], s68 offen nt lds\n"
      "v_sub_f32 v140, v140, v234\n"
      "v_mfma_f32_16x16x32_bf16 v[36:39], a[100:103], v[168:171], v[36:39]\n"
      "buffer_load_dwordx4 v213, s[44:47], s68 offen offset:1024 nt lds\n"
      "v_sub_f32 v141, v141, v234\n"
      "v_mfma_f32_16x16x32_bf16 v[40:43], a[104:107], v[168:171], v[40:43]\n"
      "v_sub_f32 v142, v142, v234\n"
      "v_sub_f32 v143, v143, v234\n"
      "v_mfma_f32_16x16x32_bf16 v[44:47], a[108:111], v[168:171], v[44:47]\n"
      "v_mul_f32 v196, -1.0, v234\n"
      "v_mul_f32 v197, -1.0, v234\n"
      "v_mfma_f32_16x16x32_bf16 v[48:51], a[112:115], v[168:171], v[48:51]\n"
      "v_mul_f32 v198, -1.0, v234\n"
      "v_mul_f32 v199, -1.0, v234\n"
      "v_max3_f32 v236, v236, v150, v151\n"
      "v_mfma_f32_16x16x32_bf16 v[52:55], a[116:119], v[168:171], v[52:55]\n"
      "v_max_f32 v236, v236, v237\n"
      "v_max3_f32 v238, v152, v153, v154\n"
      "v_mfma_f32_16x16x32_bf16 v[56:59], a[120:123], v[168:171], v[56:59]\n"
      "v_max3_f32 v239, v155, v156, v157\n"
      "v_max3_f32 v238, v238, v158, v159\n"
      "v_mfma_f32_16x16x32_bf16 v[60:63], a[124:127], v[168:171], v[60:63]\n"
      "v_max_f32 v238, v238, v239\n"
      "v_mov_b32 v237, v236\n"
      "v_mfma_f32_16x16x32_bf16 a[136:139], a[144:147], v[176:179], a[136:139]\n"
      "s_add_u32 m0, s58, 0xc000\n"
      "v_mov_b32 v239, v238\n"
      "v_permlane32_swap_b32 v236, v237\n"
      "v_mfma_f32_16x16x32_bf16 v[64:67], a[96:99], v[176:179], v[64:67]\n"
      "s_add_u32 s67, s57, 0x2004000\n"
      "v_permlane32_swap_b32 v238, v239\n"
      "v_max_f32 v236, v236, v237\n"
      "v_mfma_f32_16x16x32_bf16 v[68:71], a[100:103], v[176:179], v[68:71]\n"
      "buffer_load_dwordx4 v212, s[48:51], s67 offen nt lds\n"
      "v_max_f32 v238, v238, v239\n"
      "v_mov_b32 v237, v236\n"
      "v_mfma_f32_16x16x32_bf16 v[72:75], a[104:107], v[176:179], v[72:75]\n"
      "buffer_load_dwordx4 v213, s[48:51], s67 offen offset:1024 nt lds\n"
      "v_mov_b32 v239, v238\n"
      "v_mfma_f32_16x16x32_bf16 v[76:79], a[108:111], v[176:179], v[76:79]\n"
      "v_permlane16_swap_b32 v236, v237\n"
      "v_permlane16_swap_b32 v238, v239\n"
      "v_mfma_f32_16x16x32_bf16 v[80:83], a[112:115], v[176:179], v[80:83]\n"
      "v_max_f32 v236, v236, v237\n"
      "v_max_f32 v238, v238, v239\n"
      "v_mfma_f32_16x16x32_bf16 v[84:87], a[116:119], v[176:179], v[84:87]\n"
      "v_sub_f32 v144, v144, v236\n"
      "v_sub_f32 v145, v145, v236\n"
      "v_mfma_f32_16x16x32_bf16 v[88:91], a[120:123], v[176:179], v[88:91]\n"
      "v_sub_f32 v146, v146, v236\n"
      "v_sub_f32 v147, v147, v236\n"
      "v_sub_f32 v148, v148, v236\n"
      "v_mfma_f32_16x16x32_bf16 v[92:95], a[124:127], v[176:179], v[92:95]\n"
      "s_add_u32 m0, s59, 0x1800\n"
      "v_sub_f32 v149, v149, v236\n"
      "v_sub_f32 v150, v150, v236\n"
      "v_mfma_f32_16x16x32_bf16 a[140:143], a[144:147], v[184:187], a[140:143]\n"
      "s_add_u32 s67, s56, 0x2001800\n"
      "v_sub_f32 v151, v151, v236\n"
      "v_mul_f32 v200, -1.0, v236\n"
      "v_mfma_f32_16x16x32_bf16 v[96:99], a[96:99], v[184:187], v[96:99]\n"
      "buffer_load_dwordx4 v212, s[40:43], s67 offen nt lds\n"
      "v_mul_f32 v201, -1.0, v236\n"
      "v_mfma_f32_16x16x32_bf16 v[100:103], a[100:103], v[184:187], v[100:103]\n"
      "buffer_load_dwordx4 v213, s[40:43], s67 offen offset:1024 nt lds\n"
      "v_mul_f32 v202, -1.0, v236\n"
      "v_mfma_f32_16x16x32_bf16 v[104:107], a[104:107], v[184:187], v[104:107]\n"
      "v_mul_f32 v203, -1.0, v236\n"
      "v_sub_f32 v152, v152, v238\n"
      "v_sub_f32 v153, v153, v238\n"
      "v_mfma_f32_16x16x32_bf16 v[108:111], a[108:111], v[184:187], v[108:111]\n"
      "v_sub_f32 v154, v154, v238\n"
      "v_sub_f32 v155, v155, v238\n"
      "v_mfma_f32_16x16x32_bf16 v[112:115], a[112:115], v[184:187], v[112:115]\n"
      "v_sub_f32 v156, v156, v238\n"
      "v_sub_f32 v157, v157, v238\n"
      "v_mfma_f32_16x16x32_bf16 v[116:119], a[116:119], v[184:187], v[116:119]\n"
      "v_sub_f32 v158, v158, v238\n"
      "v_sub_f32 v159, v159, v238\n"
      "v_mfma_f32_16x16x32_bf16 v[120:123], a[120:123], v[184:187], v[120:123]\n"
      "s_memrealtime s[84:85]\n"
      "v_mul_f32 v204, -1.0, v238\n"
      "v_mul_f32 v205, -1.0, v238\n"
      "v_mfma_f32_16x16x32_bf16 v[124:127], a[124:127], v[184:187], v[124:127]\n"
      "v_mul_f32 v206, -1.0, v238\n"
      "v_mul_f32 v207, -1.0, v238\n"
      "s_waitcnt lgkmcnt(0)\n"
      "s_waitcnt vmcnt(24)\n"
      "ds_read_b128 v[224:227], v209 offset:0\n"
      "ds_read_b128 v[228:231], v209 offset:512\n"
      "v_exp_f32 v128, v128\n"
      "v_exp_f32 v129, v129\n"
      "v_exp_f32 v130, v130\n"
      "v_exp_f32 v131, v131\n"
      "v_exp_f32 v132, v132\n"
      "v_exp_f32 v133, v133\n"
      "v_exp_f32 v134, v134\n"
      "v_exp_f32 v135, v135\n"
      "v_exp_f32 v136, v136\n"
      "v_exp_f32 v137, v137\n"
      "v_exp_f32 v138, v138\n"
      "v_exp_f32 v139, v139\n"
      "s_waitcnt lgkmcnt(0)\n"
      "v_lshlrev_b32 v221, 16, v224\n"
      "v_and_b32 v224, s62, v224\n"
      "v_mul_f32 v221, s61, v221\n"
      "v_mul_f32 v224, s61, v224\n"
      "v_cvt_pk_bf16_f32 v224, v221, v224\n"
      "v_accvgpr_write_b32 a0, v224\n"
      "v_lshlrev_b32 v222, 16, v225\n"
      "v_and_b32 v225, s62, v225\n"
      "v_mul_f32 v222, s61, v222\n"
      "v_mul_f32 v225, s61, v225\n"
      "v_cvt_pk_bf16_f32 v225, v222, v225\n"
      "v_accvgpr_write_b32 a1, v225\n"
      "v_lshlrev_b32 v221, 16, v226\n"
      "v_and_b32 v226, s62, v226\n"
      "v_mul_f32 v221, s61, v221\n"
      "v_mul_f32 v226, s61, v226\n"
      "v_cvt_pk_bf16_f32 v226, v221, v226\n"
      "v_accvgpr_write_b32 a2, v226\n"
      "v_lshlrev_b32 v222, 16, v227\n"
      "v_and_b32 v227, s62, v227\n"
      "v_mul_f32 v222, s61, v222\n"
      "v_mul_f32 v227, s61, v227\n"
      "v_cvt_pk_bf16_f32 v227, v222, v227\n"
      "v_accvgpr_write_b32 a3, v227\n"
      "v_accvgpr_read_b32 v232, a128\n"
      "v_cmp_nge_f32 vcc, s63, v232\n"
      "v_rcp_f32 v234, v232\n"
      "s_nop 0\n"
      "v_mov_b32 v235, v234\n"
      "s_nop 1\n"
      "s_cmp_lg_u64 vcc, 0\n"
      "s_cselect_b32 s69, s65, 0\n"
      "s_or_b32 s64, s64, s69\n"
      "v_pk_mul_f32 v[0:1], v[0:1], v[234:235]\n"
      "v_pk_mul_f32 v[2:3], v[2:3], v[234:235]\n"
      "v_cvt_pk_bf16_f32 v0, v0, v1\n"
      "v_cvt_pk_bf16_f32 v1, v2, v3\n"
      "ds_write_b64 v215, v[0:1] offset:8192\n"
      "v_pk_mul_f32 v[4:5], v[4:5], v[234:235]\n"
      "v_pk_mul_f32 v[6:7], v[6:7], v[234:235]\n"
      "v_cvt_pk_bf16_f32 v4, v4, v5\n"
      "v_cvt_pk_bf16_f32 v5, v6, v7\n"
      "ds_write_b64 v216, v[4:5] offset:8192\n"
      "v_exp_f32 v140, v140\n"
      "v_pk_mul_f32 v[8:9], v[8:9], v[234:235]\n"
      "v_pk_mul_f32 v[10:11], v[10:11], v[234:235]\n"
      "v_cvt_pk_bf16_f32 v8, v8, v9\n"
      "v_cvt_pk_bf16_f32 v9, v10, v11\n"
      "ds_write_b64 v217, v[8:9] offset:8192\n"
      "v_exp_f32 v141, v141\n"
      "v_pk_mul_f32 v[12:13], v[12:13], v[234:235]\n"
      "v_pk_mul_f32 v[14:15], v[14:15], v[234:235]\n"
      "v_cvt_pk_bf16_f32 v12, v12, v13\n"
      "v_cvt_pk_bf16_f32 v13, v14, v15\n"
      "ds_write_b64 v218, v[12:13] offset:8192\n"
      "v_exp_f32 v142, v142\n"
      "v_pk_mul_f32 v[16:17], v[16:17], v[234:235]\n"
      "v_pk_mul_f32 v[18:19], v[18:19], v[234:235]\n"
      "v_cvt_pk_bf16_f32 v16, v16, v17\n"
      "v_cvt_pk_bf16_f32 v17, v18, v19\n"
      "ds_write_b64 v215, v[16:17] offset:8320\n"
      "v_exp_f32 v143, v143\n"
      "v_pk_mul_f32 v[20:21], v[20:21], v[234:235]\n"
      "v_pk_mul_f32 v[22:23], v[22:23], v[234:235]\n"
      "v_cvt_pk_bf16_f32 v20, v20, v21\n"
      "v_cvt_pk_bf16_f32 v21, v22, v23\n"
      "ds_write_b64 v216, v[20:21] offset:8320\n"
      "v_exp_f32 v144, v144\n"
      "v_pk_mul_f32 v[24:25], v[24:25], v[234:235]\n"
      "v_pk_mul_f32 v[26:27], v[26:27], v[234:235]\n"
      "v_cvt_pk_bf16_f32 v24, v24, v25\n"
      "v_cvt_pk_bf16_f32 v25, v26, v27\n"
      "ds_write_b64 v217, v[24:25] offset:8320\n"
      "v_exp_f32 v145, v145\n"
      "v_pk_mul_f32 v[28:29], v[28:29], v[234:235]\n"
      "v_pk_mul_f32 v[30:31], v[30:31], v[234:235]\n"
      "v_cvt_pk_bf16_f32 v28, v28, v29\n"
      "v_cvt_pk_bf16_f32 v29, v30, v31\n"
      "ds_write_b64 v218, v[28:29] offset:8320\n"
      "v_exp_f32 v146, v146\n"
      "v_accvgpr_read_b32 v236, a132\n"
      "v_cmp_nge_f32 vcc, s63, v236\n"
      "v_rcp_f32 v238, v236\n"
      "s_nop 0\n"
      "v_mov_b32 v239, v238\n"
      "s_nop 1\n"
      "s_cmp_lg_u64 vcc, 0\n"
      "s_cselect_b32 s69, s65, 0\n"
      "s_or_b32 s64, s64, s69\n"
      "v_pk_mul_f32 v[32:33], v[32:33], v[238:239]\n"
      "v_pk_mul_f32 v[34:35], v[34:35], v[238:239]\n"
      "v_cvt_pk_bf16_f32 v32, v32, v33\n"
      "v_cvt_pk_bf16_f32 v33, v34, v35\n"
      "ds_write_b64 v215, v[32:33] offset:12288\n"
      "v_pk_mul_f32 v[36:37], v[36:37], v[238:239]\n"
      "v_pk_mul_f32 v[38:39], v[38:39], v[238:239]\n"
      "v_cvt_pk_bf16_f32 v36, v36, v37\n"
      "v_cvt_pk_bf16_f32 v37, v38, v39\n"
      "ds_write_b64 v216, v[36:37] offset:12288\n"
      "v_exp_f32 v147, v147\n"
      "v_pk_mul_f32 v[40:41], v[40:41], v[238:239]\n"
      "v_pk_mul_f32 v[42:43], v[42:43], v[238:239]\n"
      "v_cvt_pk_bf16_f32 v40, v40, v41\n"
      "v_cvt_pk_bf16_f32 v41, v42, v43\n"
      "ds_write_b64 v217, v[40:41] offset:12288\n"
      "v_exp_f32 v148, v148\n"
      "v_pk_mul_f32 v[44:45], v[44:45], v[238:239]\n"
      "v_pk_mul_f32 v[46:47], v[46:47], v[238:239]\n"
      "v_cvt_pk_bf16_f32 v44, v44, v45\n"
      "v_cvt_pk_bf16_f32 v45, v46, v47\n"
      "ds_write_b64 v218, v[44:45] offset:12288\n"
      "v_exp_f32 v149, v149\n"
      "v_pk_mul_f32 v[48:49], v[48:49], v[238:239]\n"
      "v_pk_mul_f32 v[50:51], v[50:51], v[238:239]\n"
      "v_cvt_pk_bf16_f32 v48, v48, v49\n"
      "v_cvt_pk_bf16_f32 v49, v50, v51\n"
      "ds_write_b64 v215, v[48:49] offset:12416\n"
      "v_exp_f32 v150, v150\n"
      "v_pk_mul_f32 v[52:53], v[52:53], v[238:239]\n"
      "v_pk_mul_f32 v[54:55], v[54:55], v[238:239]\n"
      "v_cvt_pk_bf16_f32 v52, v52, v53\n"
      "v_cvt_pk_bf16_f32 v53, v54, v55\n"
      "ds_write_b64 v216, v[52:53] offset:12416\n"
      "v_exp_f32 v151, v151\n"
      "v_pk_mul_f32 v[56:57], v[56:57], v[238:239]\n"
      "v_pk_mul_f32 v[58:59], v[58:59], v[238:239]\n"
      "v_cvt_pk_bf16_f32 v56, v56, v57\n"
      "v_cvt_pk_bf16_f32 v57, v58, v59\n"
      "ds_write_b64 v217, v[56:57] offset:12416\n"
      "v_exp_f32 v152, v152\n"
      "v_pk_mul_f32 v[60:61], v[60:61], v[238:239]\n"
      "v_pk_mul_f32 v[62:63], v[62:63], v[238:239]\n"
      "v_cvt_pk_bf16_f32 v60, v60, v61\n"
      "v_cvt_pk_bf16_f32 v61, v62, v63\n"
      "ds_write_b64 v218, v[60:61] offset:12416\n"
      "v_exp_f32 v153, v153\n"
      "v_accvgpr_read_b32 v232, a136\n"
      "v_cmp_nge_f32 vcc, s63, v232\n"
      "v_rcp_f32 v234, v232\n"
      "s_nop 0\n"
      "v_mov_b32 v235, v234\n"
      "s_nop 1\n"
      "s_cmp_lg_u64 vcc, 0\n"
      "s_cselect_b32 s69, s65, 0\n"
      "s_or_b32 s64, s64, s69\n"
      "v_pk_mul_f32 v[64:65], v[64:65], v[234:235]\n"
      "v_pk_mul_f32 v[66:67], v[66:67], v[234:235]\n"
      "v_cvt_pk_bf16_f32 v64, v64, v65\n"
      "v_cvt_pk_bf16_f32 v65, v66, v67\n"
      "ds_write_b64 v215, v[64:65] offset:16384\n"
      "v_pk_mul_f32 v[68:69], v[68:69], v[234:235]\n"
      "v_pk_mul_f32 v[70:71], v[70:71], v[234:235]\n"
      "v_cvt_pk_bf16_f32 v68, v68, v69\n"
      "v_cvt_pk_bf16_f32 v69, v70, v71\n"
      "ds_write_b64 v216, v[68:69] offset:16384\n"
      "v_exp_f32 v154, v154\n"
      "v_pk_mul_f32 v[72:73], v[72:73], v[234:235]\n"
      "v_pk_mul_f32 v[74:75], v[74:75], v[234:235]\n"
      "v_cvt_pk_bf16_f32 v72, v72, v73\n"
      "v_cvt_pk_bf16_f32 v73, v74, v75\n"
      "ds_write_b64 v217, v[72:73] offset:16384\n"
      "v_cvt_pk_bf16_f32 v128, v128, v129\n"
      "v_pk_mul_f32 v[76:77], v[76:77], v[234:235]\n"
      "v_pk_mul_f32 v[78:79], v[78:79], v[234:235]\n"
      "v_cvt_pk_bf16_f32 v76, v76, v77\n"
      "v_cvt_pk_bf16_f32 v77, v78, v79\n"
      "ds_write_b64 v218, v[76:77] offset:16384\n"
      "v_cvt_pk_bf16_f32 v129, v130, v131\n"
      "v_pk_mul_f32 v[80:81], v[80:81], v[234:235]\n"
      "v_pk_mul_f32 v[82:83], v[82:83], v[234:235]\n"
      "v_cvt_pk_bf16_f32 v80, v80, v81\n"
      "v_cvt_pk_bf16_f32 v81, v82, v83\n"
      "ds_write_b64 v215, v[80:81] offset:16512\n"
      "v_cvt_pk_bf16_f32 v130, v132, v133\n"
      "v_pk_mul_f32 v[84:85], v[84:85], v[234:235]\n"
      "v_pk_mul_f32 v[86:87], v[86:87], v[234:235]\n"
      "v_cvt_pk_bf16_f32 v84, v84, v85\n"
      "v_cvt_pk_bf16_f32 v85, v86, v87\n"
      "ds_write_b64 v216, v[84:85] offset:16512\n"
      "v_cvt_pk_bf16_f32 v131, v134, v135\n"
      "v_pk_mul_f32 v[88:89], v[88:89], v[234:235]\n"
      "v_pk_mul_f32 v[90:91], v[90:91], v[234:235]\n"
      "v_cvt_pk_bf16_f32 v88, v88, v89\n"
      "v_cvt_pk_bf16_f32 v89, v90, v91\n"
      "ds_write_b64 v217, v[88:89] offset:16512\n"
      "v_cvt_pk_bf16_f32 v136, v136, v137\n"
      "v_pk_mul_f32 v[92:93], v[92:93], v[234:235]\n"
      "v_pk_mul_f32 v[94:95], v[94:95], v[234:235]\n"
      "v_cvt_pk_bf16_f32 v92, v92, v93\n"
      "v_cvt_pk_bf16_f32 v93, v94, v95\n"
      "ds_write_b64 v218, v[92:93] offset:16512\n"
      "v_cvt_pk_bf16_f32 v137, v138, v139\n"
      "v_accvgpr_read_b32 v236, a140\n"
      "v_cmp_nge_f32 vcc, s63, v236\n"
      "v_rcp_f32 v238, v236\n"
      "s_nop 0\n"
      "v_mov_b32 v239, v238\n"
      "s_nop 1\n"
      "s_cmp_lg_u64 vcc, 0\n"
      "s_cselect_b32 s69, s65, 0\n"
      "s_or_b32 s64, s64, s69\n"
      "v_pk_mul_f32 v[96:97], v[96:97], v[238:239]\n"
      "v_pk_mul_f32 v[98:99], v[98:99], v[238:239]\n"
      "v_cvt_pk_bf16_f32 v96, v96, v97\n"
      "v_cvt_pk_bf16_f32 v97, v98, v99\n"
      "ds_write_b64 v215, v[96:97] offset:20480\n"
      "v_pk_mul_f32 v[100:101], v[100:101], v[238:239]\n"
      "v_pk_mul_f32 v[102:103], v[102:103], v[238:239]\n"
      "v_cvt_pk_bf16_f32 v100, v100, v101\n"
      "v_cvt_pk_bf16_f32 v101, v102, v103\n"
      "ds_write_b64 v216, v[100:101] offset:20480\n"
      "v_cvt_pk_bf16_f32 v138, v140, v141\n"
      "v_pk_mul_f32 v[104:105], v[104:105], v[238:239]\n"
      "v_pk_mul_f32 v[106:107], v[106:107], v[238:239]\n"
      "v_cvt_pk_bf16_f32 v104, v104, v105\n"
      "v_cvt_pk_bf16_f32 v105, v106, v107\n"
      "ds_write_b64 v217, v[104:105] offset:20480\n"
      "v_cvt_pk_bf16_f32 v139, v142, v143\n"
      "v_pk_mul_f32 v[108:109], v[108:109], v[238:239]\n"
      "v_pk_mul_f32 v[110:111], v[110:111], v[238:239]\n"
      "v_cvt_pk_bf16_f32 v108, v108, v109\n"
      "v_cvt_pk_bf16_f32 v109, v110, v111\n"
      "ds_write_b64 v218, v[108:109] offset:20480\n"
      "v_cvt_pk_bf16_f32 v144, v144, v145\n"
      "v_pk_mul_f32 v[112:113], v[112:113], v[238:239]\n"
      "v_pk_mul_f32 v[114:115], v[114:115], v[238:239]\n"
      "v_cvt_pk_bf16_f32 v112, v112, v113\n"
      "v_cvt_pk_bf16_f32 v113, v114, v115\n"
      "ds_write_b64 v215, v[112:113] offset:20608\n"
      "v_cvt_pk_bf16_f32 v145, v146, v147\n"
      "v_pk_mul_f32 v[116:117], v[116:117], v[238:239]\n"
      "v_pk_mul_f32 v[118:119], v[118:119], v[238:239]\n"
      "v_cvt_pk_bf16_f32 v116, v116, v117\n"
      "v_cvt_pk_bf16_f32 v117, v118, v119\n"
      "ds_write_b64 v216, v[116:117] offset:20608\n"
      "v_cvt_pk_bf16_f32 v146, v148, v149\n"
      "v_pk_mul_f32 v[120:121], v[120:121], v[238:239]\n"
      "v_pk_mul_f32 v[122:123], v[122:123], v[238:239]\n"
      "v_cvt_pk_bf16_f32 v120, v120, v121\n"
      "v_cvt_pk_bf16_f32 v121, v122, v123\n"
      "ds_write_b64 v217, v[120:121] offset:20608\n"
      "v_cvt_pk_bf16_f32 v147, v150, v151\n"
      "v_pk_mul_f32 v[124:125], v[124:125], v[238:239]\n"
      "v_pk_mul_f32 v[126:127], v[126:127], v[238:239]\n"
      "v_cvt_pk_bf16_f32 v124, v124, v125\n"
      "v_cvt_pk_bf16_f32 v125, v126, v127\n"
      "ds_write_b64 v218, v[124:125] offset:20608\n"
      "v_cvt_pk_bf16_f32 v152, v152, v153\n"
      "s_mov_b32 s76, s56\n"
      "s_add_u32 s77, s56, 0x1000\n"
      "s_add_u32 s78, s56, 0x2000\n"
      "s_add_u32 s79, s56, 0x3000\n"
      "s_mov_b32 s54, 0x4000000\n"
      "s_memrealtime s[84:85]\n"
      "s_add_u32 s86, s86, 90\n"
      "s_cmp_eq_u32 s66, 2\n"
      "s_cselect_b32 s87, 0, s87\n"
      "s_add_u32 s57, s57, 0x2000000\n"
      "s_add_u32 s56, s56, 0x1000000\n"
      "s_lshl_b32 s65, s65, 1\n"
      "s_sub_u32 s66, s66, 1\n"
      "s_waitcnt lgkmcnt(0)\n"
      "s_sub_u32 s85, s84, s86\n"
      "s_cmp_ge_u32 s85, s87\n"
      "s_cbranch_scc1 L_PACED1_0\n"
      "L_PACE1_0:\n"
      "s_memrealtime s[84:85]\n"
      "s_waitcnt lgkmcnt(0)\n"
      "s_sub_u32 s85, s84, s86\n"
      "s_cmp_lt_u32 s85, s87\n"
      "s_cbranch_scc1 L_PACE1_0\n"
      "L_PACED1_0:\n"
      "s_mov_b32 s86, s84\n"
      "v_mfma_f32_16x16x32_bf16 v[160:163], a[64:67], a[160:163], v[192:195]\n"
      "s_waitcnt vmcnt(14)\n"
      "s_barrier\n"
      "v_lshlrev_b32 v221, 16, v228\n"
      "v_and_b32 v228, s62, v228\n"
      "v_mfma_f32_16x16x32_bf16 v[160:163], a[68:71], a[164:167], v[160:163]\n"
      "v_exp_f32 v155, v155\n"
      "v_mfma_f32_16x16x32_bf16 v[160:163], a[72:75], a[168:171], v[160:163]\n"
      "ds_read_b128 a[148:151], v219 offset:8192\n"
      "v_cvt_pk_bf16_f32 v153, v154, v155\n"
      "v_mfma_f32_16x16x32_bf16 v[160:163], a[76:79], a[172:175], v[160:163]\n"
      "v_exp_f32 v156, v156\n"
      "v_mfma_f32_16x16x32_bf16 v[164:167], a[80:83], a[160:163], v[192:195]\n"
      "ds_read_b64_tr_b16 a[96:97], v210 offset:32768\n"
      "ds_read_b64_tr_b16 a[98:99], v210 offset:36864\n"
      "v_mfma_f32_16x16x32_bf16 v[164:167], a[84:87], a[164:167], v[164:167]\n"
      "v_exp_f32 v157, v157\n"
      "v_mfma_f32_16x16x32_bf16 v[164:167], a[88:91], a[168:171], v[164:167]\n"
      "ds_read_b64_tr_b16 a[100:101], v211 offset:32768\n"
      "v_cvt_pk_bf16_f32 v154, v156, v157\n"
      "v_mfma_f32_16x16x32_bf16 v[164:167], a[92:95], a[172:175], v[164:167]\n"
      "v_exp_f32 v158, v158\n"
      "v_mfma_f32_16x16x32_bf16 v[168:171], a[64:67], a[176:179], v[196:199]\n"
      "ds_read_b64_tr_b16 a[102:103], v211 offset:36864\n"
      "ds_read_b64_tr_b16 a[104:105], v210 offset:33280\n"
      "v_mfma_f32_16x16x32_bf16 v[168:171], a[68:71], a[180:183], v[168:171]\n"
      "v_exp_f32 v159, v159\n"
      "v_mfma_f32_16x16x32_bf16 v[168:171], a[72:75], a[184:187], v[168:171]\n"
      "ds_read_b64_tr_b16 a[106:107], v210 offset:37376\n"
      "v_cvt_pk_bf16_f32 v155, v158, v159\n"
      "v_mfma_f32_16x16x32_bf16 v[168:171], a[76:79], a[188:191], v[168:171]\n"
      "v_exp_f32 v160, v160\n"
      "v_mfma_f32_16x16x32_bf16 v[172:175], a[80:83], a[176:179], v[196:199]\n"
      "ds_read_b64_tr_b16 a[108:109], v211 offset:33280\n"
      "ds_read_b64_tr_b16 a[110:111], v211 offset:37376\n"
      "v_mfma_f32_16x16x32_bf16 v[172:175], a[84:87], a[180:183], v[172:175]\n"
      "v_exp_f32 v161, v161\n"
      "v_mfma_f32_16x16x32_bf16 v[172:175], a[88:91], a[184:187], v[172:175]\n"
      "ds_read_b64_tr_b16 a[112:113], v210 offset:33792\n"
      "v_cvt_pk_bf16_f32 v160, v160, v161\n"
      "v_mfma_f32_16x16x32_bf16 v[172:175], a[92:95], a[188:191], v[172:175]\n"
      "v_exp_f32 v162, v162\n"
      "v_mfma_f32_16x16x32_bf16 v[176:179], a[64:67], a[192:195], v[200:203]\n"
      "ds_read_b64_tr_b16 a[114:115], v210 offset:37888\n"
      "ds_read_b64_tr_b16 a[116:117], v211 offset:33792\n"
      "v_mfma_f32_16x16x32_bf16 v[176:179], a[68:71], a[196:199], v[176:179]\n"
      "v_exp_f32 v163, v163\n"
      "v_mfma_f32_16x16x32_bf16 v[176:179], a[72:75], a[200:203], v[176:179]\n"
      "ds_read_b64_tr_b16 a[118:119], v211 offset:37888\n"
      "v_cvt_pk_bf16_f32 v161, v162, v163\n"
      "v_mfma_f32_16x16x32_bf16 v[176:179], a[76:79], a[204:207], v[176:179]\n"
      "v_exp_f32 v164, v164\n"
      "v_mfma_f32_16x16x32_bf16 v[180:183], a[80:83], a[192:195], v[200:203]\n"
      "ds_read_b64_tr_b16 a[120:121], v210 offset:34304\n"
      "ds_read_b64_tr_b16 a[122:123], v210 offset:38400\n"
      "v_mfma_f32_16x16x32_bf16 v[180:183], a[84:87], a[196:199], v[180:183]\n"
      "v_exp_f32 v165, v165\n"
      "v_mfma_f32_16x16x32_bf16 v[180:183], a[88:91], a[200:203], v[180:183]\n"
      "ds_read_b64_tr_b16 a[124:125], v211 offset:34304\n"
      "v_cvt_pk_bf16_f32 v162, v164, v165\n"
      "v_mfma_f32_16x16x32_bf16 v[180:183], a[92:95], a[204:207], v[180:183]\n"
      "v_exp_f32 v166, v166\n"
      "v_mfma_f32_16x16x32_bf16 v[184:187], a[64:67], a[208:211], v[204:207]\n"
      "ds_read_b64_tr_b16 a[126:127], v211 offset:38400\n"
      "v_mul_f32 v221, s61, v221\n"
      "v_mfma_f32_16x16x32_bf16 v[184:187], a[68:71], a[212:215], v[184:187]\n"
      "v_exp_f32 v167, v167\n"
      "v_mfma_f32_16x16x32_bf16 v[184:187], a[72:75], a[216:219], v[184:187]\n"
      "v_cvt_pk_bf16_f32 v163, v166, v167\n"
      "v_mul_f32 v228, s61, v228\n"
      "v_mfma_f32_16x16x32_bf16 v[184:187], a[76:79], a[220:223], v[184:187]\n"
      "v_exp_f32 v168, v168\n"
      "v_mfma_f32_16x16x32_bf16 v[188:191], a[80:83], a[208:211], v[204:207]\n"
      "ds_read_b128 a[64:67], v208 offset:16384\n"
      "ds_read_b128 a[68:71], v208 offset:16896\n"
      "v_mfma_f32_16x16x32_bf16 v[188:191], a[84:87], a[212:215], v[188:191]\n"
      "v_exp_f32 v169, v169\n"
      "v_mfma_f32_16x16x32_bf16 v[188:191], a[88:91], a[216:219], v[188:191]\n"
      "ds_read_b128 a[72:75], v208 offset:17408\n"
      "v_cvt_pk_bf16_f32 v168, v168, v169\n"
      "v_mfma_f32_16x16x32_bf16 v[188:191], a[92:95], a[220:223], v[188:191]\n"
      "v_exp_f32 v170, v170\n"
      "v_mfma_f32_16x16x32_bf16 a[128:131], a[144:147], v[128:131], 0\n"
      "ds_read_b128 a[76:79], v208 offset:17920\n"
      "v_cvt_pk_bf16_f32 v228, v221, v228\n"
      "s_waitcnt lgkmcnt(12)\n"
      "v_mfma_f32_16x16x32_bf16 v[0:3], a[96:99], v[128:131], 0\n"
      "v_exp_f32 v171, v171\n"
      "v_mfma_f32_16x16x32_bf16 v[4:7], a[100:103], v[128:131], 0\n"
      "ds_read_b128 a[80:83], v208 offset:20480\n"
      "v_cvt_pk_bf16_f32 v169, v170, v171\n"
      "v_mfma_f32_16x16x32_bf16 v[8:11], a[104:107], v[128:131], 0\n"
      "v_exp_f32 v172, v172\n"
      "v_mfma_f32_16x16x32_bf16 v[12:15], a[108:111], v[128:131], 0\n"
      "ds_read_b128 a[84:87], v208 offset:20992\n"
      "ds_read_b128 a[88:91], v208 offset:21504\n"
      "s_waitcnt lgkmcnt(7)\n"
      "v_mfma_f32_16x16x32_bf16 v[16:19], a[112:115], v[128:131], 0\n"
      "v_exp_f32 v173, v173\n"
      "v_mfma_f32_16x16x32_bf16 v[20:23], a[116:119], v[128:131], 0\n"
      "ds_read_b128 a[92:95], v208 offset:22016\n"
      "v_cvt_pk_bf16_f32 v170, v172, v173\n"
      "v_mfma_f32_16x16x32_bf16 v[24:27], a[120:123], v[128:131], 0\n"
      "v_exp_f32 v174, v174\n"
      "v_mfma_f32_16x16x32_bf16 v[28:31], a[124:127], v[128:131], 0\n"
      "buffer_store_dwordx4 a[148:151], v214, s[52:55], s76 offen offset:0 sc1\n"
      "v_mfma_f32_16x16x32_bf16 a[132:135], a[144:147], v[136:139], 0\n"
      "v_exp_f32 v175, v175\n"
      "v_mfma_f32_16x16x32_bf16 v[32:35], a[96:99], v[136:139], 0\n"
      "s_add_u32 m0, s58, 0x2000\n"
      "v_cvt_pk_bf16_f32 v171, v174, v175\n"
      "v_accvgpr_write_b32 a4, v228\n"
      "v_mfma_f32_16x16x32_bf16 v[36:39], a[100:103], v[136:139], 0\n"
      "s_add_u32 s68, s57, 0xa000\n"
      "v_exp_f32 v176, v176\n"
      "v_mfma_f32_16x16x32_bf16 v[40:43], a[104:107], v[136:139], 0\n"
      "buffer_load_dwordx4 v212, s[44:47], s68 offen nt lds\n"
      "v_lshlrev_b32 v222, 16, v229\n"
      "v_mfma_f32_16x16x32_bf16 v[44:47], a[108:111], v[136:139], 0\n"
      "v_exp_f32 v177, v177\n"
      "v_mfma_f32_16x16x32_bf16 v[48:51], a[112:115], v[136:139], 0\n"
      "buffer_load_dwordx4 v213, s[44:47], s68 offen offset:1024 nt lds\n"
      "v_cvt_pk_bf16_f32 v176, v176, v177\n"
      "v_mfma_f32_16x16x32_bf16 v[52:55], a[116:119], v[136:139], 0\n"
      "v_exp_f32 v178, v178\n"
      "v_mfma_f32_16x16x32_bf16 v[56:59], a[120:123], v[136:139], 0\n"
      "v_and_b32 v229, s62, v229\n"
      "v_mul_f32 v222, s61, v222\n"
      "v_mfma_f32_16x16x32_bf16 v[60:63], a[124:127], v[136:139], 0\n"
      "v_exp_f32 v179, v179\n"
      "v_mfma_f32_16x16x32_bf16 a[136:139], a[144:147], v[144:147], 0\n"
      "v_cvt_pk_bf16_f32 v177, v178, v179\n"
      "v_mul_f32 v229, s61, v229\n"
      "v_mfma_f32_16x16x32_bf16 v[64:67], a[96:99], v[144:147], 0\n"
      "v_exp_f32 v180, v180\n"
      "v_mfma_f32_16x16x32_bf16 v[68:71], a[100:103], v[144:147], 0\n"
      "v_cvt_pk_bf16_f32 v229, v222, v229\n"
      "v_accvgpr_write_b32 a5, v229\n"
      "v_mfma_f32_16x16x32_bf16 v[72:75], a[104:107], v[144:147], 0\n"
      "v_exp_f32 v181, v181\n"
      "v_mfma_f32_16x16x32_bf16 v[76:79], a[108:111], v[144:147], 0\n"
      "v_cvt_pk_bf16_f32 v178, v180, v181\n"
      "v_lshlrev_b32 v221, 16, v230\n"
      "v_mfma_f32_16x16x32_bf16 v[80:83], a[112:115], v[144:147], 0\n"
      "v_exp_f32 v182, v182\n"
      "v_mfma_f32_16x16x32_bf16 v[84:87], a[116:119], v[144:147], 0\n"
      "s_add_u32 m0, s58, 0xe000\n"
      "ds_read_b128 v[224:227], v209 offset:1024\n"
      "v_and_b32 v230, s62, v230\n"
      "v_mfma_f32_16x16x32_bf16 v[88:91], a[120:123], v[144:147], 0\n"
      "s_add_u32 s67, s57, 0x6000\n"
      "v_exp_f32 v183, v183\n"
      "v_mfma_f32_16x16x32_bf16 v[92:95], a[124:127], v[144:147], 0\n"
      "buffer_load_dwordx4 v212, s[48:51], s67 offen nt lds\n"
      "v_cvt_pk_bf16_f32 v179, v182, v183\n"
      "v_mfma_f32_16x16x32_bf16 a[140:143], a[144:147], v[152:155], 0\n"
      "v_exp_f32 v184, v184\n"
      "v_mfma_f32_16x16x32_bf16 v[96:99], a[96:99], v[152:155], 0\n"
      "buffer_load_dwordx4 v213, s[48:51], s67 offen offset:1024 nt lds\n"
      "v_mul_f32 v221, s61, v221\n"
      "v_mfma_f32_16x16x32_bf16 v[100:103], a[100:103], v[152:155], 0\n"
      "v_exp_f32 v185, v185\n"
      "v_mfma_f32_16x16x32_bf16 v[104:107], a[104:107], v[152:155], 0\n"
      "v_cvt_pk_bf16_f32 v184, v184, v185\n"
      "v_mul_f32 v230, s61, v230\n"
      "v_mfma_f32_16x16x32_bf16 v[108:111], a[108:111], v[152:155], 0\n"
      "v_exp_f32 v186, v186\n"
      "v_mfma_f32_16x16x32_bf16 v[112:115], a[112:115], v[152:155], 0\n"
      "v_cvt_pk_bf16_f32 v230, v221, v230\n"
      "v_accvgpr_write_b32 a6, v230\n"
      "v_mfma_f32_16x16x32_bf16 v[116:119], a[116:119], v[152:155], 0\n"
      "v_lshlrev_b32 v222, 16, v231\n"
      "v_and_b32 v231, s62, v231\n"
      "v_mfma_f32_16x16x32_bf16 v[120:123], a[120:123], v[152:155], 0\n"
      "v_mul_f32 v222, s61, v222\n"
      "v_mul_f32 v231, s61, v231\n"
      "v_mfma_f32_16x16x32_bf16 v[124:127], a[124:127], v[152:155], 0\n"
      "v_cvt_pk_bf16_f32 v231, v222, v231\n"
      "v_accvgpr_write_b32 a7, v231\n"
      "s_waitcnt lgkmcnt(0)\n"
      "v_mfma_f32_16x16x32_bf16 v[128:131], a[64:67], a[160:163], v[192:195]\n"
      "s_waitcnt vmcnt(14)\n"
      "s_barrier\n"
      "v_lshlrev_b32 v221, 16, v224\n"
      "v_and_b32 v224, s62, v224\n"
      "v_mfma_f32_16x16x32_bf16 v[128:131], a[68:71], a[164:167], v[128:131]\n"
      "v_exp_f32 v187, v187\n"
      "v_mfma_f32_16x16x32_bf16 v[128:131], a[72:75], a[168:171], v[128:131]\n"
      "ds_read_b128 a[152:155], v220 offset:9216\n"
      "v_cvt_pk_bf16_f32 v185, v186, v187\n"
      "v_mfma_f32_16x16x32_bf16 v[128:131], a[76:79], a[172:175], v[128:131]\n"
      "v_exp_f32 v188, v188\n"
      "v_mfma_f32_16x16x32_bf16 v[132:135], a[80:83], a[160:163], v[192:195]\n"
      "ds_read_b64_tr_b16 a[96:97], v210 offset:40960\n"
      "ds_read_b64_tr_b16 a[98:99], v210 offset:45056\n"
      "v_mfma_f32_16x16x32_bf16 v[132:135], a[84:87], a[164:167], v[132:135]\n"
      "v_exp_f32 v189, v189\n"
      "v_mfma_f32_16x16x32_bf16 v[132:135], a[88:91], a[168:171], v[132:135]\n"
      "ds_read_b64_tr_b16 a[100:101], v211 offset:40960\n"
      "v_cvt_pk_bf16_f32 v186, v188, v189\n"
      "v_mfma_f32_16x16x32_bf16 v[132:135], a[92:95], a[172:175], v[132:135]\n"
      "v_exp_f32 v190, v190\n"
      "v_mfma_f32_16x16x32_bf16 v[136:139], a[64:67], a[176:179], v[196:199]\n"
      "ds_read_b64_tr_b16 a[102:103], v211 offset:45056\n"
      "ds_read_b64_tr_b16 a[104:105], v210 offset:41472\n"
      "v_mfma_f32_16x16x32_bf16 v[136:139], a[68:71], a[180:183], v[136:139]\n"
      "v_exp_f32 v191, v191\n"
      "v_mfma_f32_16x16x32_bf16 v[136:139], a[72:75], a[184:187], v[136:139]\n"
      "ds_read_b64_tr_b16 a[106:107], v210 offset:45568\n"
      "v_cvt_pk_bf16_f32 v187, v190, v191\n"
      "v_mfma_f32_16x16x32_bf16 v[136:139], a[76:79], a[188:191], v[136:139]\n"
      "v_exp_f32 v128, v128\n"
      "v_mfma_f32_16x16x32_bf16 v[140:143], a[80:83], a[176:179], v[196:199]\n"
      "ds_read_b64_tr_b16 a[108:109], v211 offset:41472\n"
      "ds_read_b64_tr_b16 a[110:111], v211 offset:45568\n"
      "v_mfma_f32_16x16x32_bf16 v[140:143], a[84:87], a[180:183], v[140:143]\n"
      "v_exp_f32 v129, v129\n"
      "v_mfma_f32_16x16x32_bf16 v[140:143], a[88:91], a[184:187], v[140:143]\n"
      "ds_read_b64_tr_b16 a[112:113], v210 offset:41984\n"
      "v_cvt_pk_bf16_f32 v128, v128, v129\n"
      "v_mfma_f32_16x16x32_bf16 v[140:143], a[92:95], a[188:191], v[140:143]\n"
      "v_exp_f32 v130, v130\n"
      "v_mfma_f32_16x16x32_bf16 v[144:147], a[64:67], a[192:195], v[200:203]\n"
      "ds_read_b64_tr_b16 a[114:115], v210 offset:46080\n"
      "ds_read_b64_tr_b16 a[116:117], v211 offset:41984\n"
      "v_mfma_f32_16x16x32_bf16 v[144:147], a[68:71], a[196:199], v[144:147]\n"
      "v_exp_f32 v131, v131\n"
      "v_mfma_f32_16x16x32_bf16 v[144:147], a[72:75], a[200:203], v[144:147]\n"
      "ds_read_b64_tr_b16 a[118:119], v211 offset:46080\n"
      "v_cvt_pk_bf16_f32 v129, v130, v131\n"
      "v_mfma_f32_16x16x32_bf16 v[144:147], a[76:79], a[204:207], v[144:147]\n"
      "v_exp_f32 v132, v132\n"
      "v_mfma_f32_16x16x32_bf16 v[148:151], a[80:83], a[192:195], v[200:203]\n"
      "ds_read_b64_tr_b16 a[120:121], v210 offset:42496\n"
      "ds_read_b64_tr_b16 a[122:123], v210 offset:46592\n"
      "v_mfma_f32_16x16x32_bf16 v[148:151], a[84:87], a[196:199], v[148:151]\n"
      "v_exp_f32 v133, v133\n"
      "v_mfma_f32_16x16x32_bf16 v[148:151], a[88:91], a[200:203], v[148:151]\n"
      "ds_read_b64_tr_b16 a[124:125], v211 offset:42496\n"
      "v_cvt_pk_bf16_f32 v130, v132, v133\n"
      "v_mfma_f32_16x16x32_bf16 v[148:151], a[92:95], a[204:207], v[148:151]\n"
      "v_exp_f32 v134, v134\n"
      "v_mfma_f32_16x16x32_bf16 v[152:155], a[64:67], a[208:211], v[204:207]\n"
      "ds_read_b64_tr_b16 a[126:127], v211 offset:46592\n"
      "v_mul_f32 v221, s61, v221\n"
      "v_mfma_f32_16x16x32_bf16 v[152:155], a[68:71], a[212:215], v[152:155]\n"
      "v_exp_f32 v135, v135\n"
      "v_mfma_f32_16x16x32_bf16 v[152:155], a[72:75], a[216:219], v[152:155]\n"
      "v_cvt_pk_bf16_f32 v131, v134, v135\n"
      "v_mul_f32 v224, s61, v224\n"
      "v_mfma_f32_16x16x32_bf16 v[152:155], a[76:79], a[220:223], v[152:155]\n"
      "v_exp_f32 v136, v136\n"
      "v_mfma_f32_16x16x32_bf16 v[156:159], a[80:83], a[208:211], v[204:207]\n"
      "ds_read_b128 a[64:67], v208 offset:24576\n"
      "ds_read_b128 a[68:71], v208 offset:25088\n"
      "v_mfma_f32_16x16x32_bf16 v[156:159], a[84:87], a[212:215], v[156:159]\n"
      "v_exp_f32 v137, v137\n"
      "v_mfma_f32_16x16x32_bf16 v[156:159], a[88:91], a[216:219], v[156:159]\n"
      "ds_read_b128 a[72:75], v208 offset:25600\n"
      "v_cvt_pk_bf16_f32 v136, v136, v137\n"
      "v_mfma_f32_16x16x32_bf16 v[156:159], a[92:95], a[220:223], v[156:159]\n"
      "v_exp_f32 v138, v138\n"
      "v_mfma_f32_16x16x32_bf16 a[128:131], a[144:147], v[160:163], a[128:131]\n"
      "ds_read_b128 a[76:79], v208 offset:26112\n"
      "v_cvt_pk_bf16_f32 v224, v221, v224\n"
      "s_waitcnt lgkmcnt(12)\n"
      "v_mfma_f32_16x16x32_bf16 v[0:3], a[96:99], v[160:163], v[0:3]\n"
      "v_exp_f32 v139, v139\n"
      "v_mfma_f32_16x16x32_bf16 v[4:7], a[100:103], v[160:163], v[4:7]\n"
      "ds_read_b128 a[80:83], v208 offset:28672\n"
      "v_cvt_pk_bf16_f32 v137, v138, v139\n"
      "v_mfma_f32_16x16x32_bf16 v[8:11], a[104:107], v[160:163], v[8:11]\n"
      "v_exp_f32 v140, v140\n"
      "v_mfma_f32_16x16x32_bf16 v[12:15], a[108:111], v[160:163], v[12:15]\n"
      "ds_read_b128 a[84:87], v208 offset:29184\n"
      "ds_read_b128 a[88:91], v208 offset:29696\n"
      "s_waitcnt lgkmcnt(7)\n"
      "v_mfma_f32_16x16x32_bf16 v[16:19], a[112:115], v[160:163], v[16:19]\n"
      "v_exp_f32 v141, v141\n"
      "v_mfma_f32_16x16x32_bf16 v[20:23], a[116:119], v[160:163], v[20:23]\n"
      "ds_read_b128 a[92:95], v208 offset:30208\n"
      "v_cvt_pk_bf16_f32 v138, v140, v141\n"
      "v_mfma_f32_16x16x32_bf16 v[24:27], a[120:123], v[160:163], v[24:27]\n"
      "v_exp_f32 v142, v142\n"
      "v_mfma_f32_16x16x32_bf16 v[28:31], a[124:127], v[160:163], v[28:31]\n"
      "buffer_store_dwordx4 a[152:155], v214, s[52:55], s76 offen offset:1024 sc1\n"
      "v_mfma_f32_16x16x32_bf16 a[132:135], a[144:147], v[168:171], a[132:135]\n"
      "v_exp_f32 v143, v143\n"
      "v_mfma_f32_16x16x32_bf16 v[32:35], a[96:99], v[168:171], v[32:35]\n"
      "s_add_u32 m0, s58, 0x4000\n"
      "v_cvt_pk_bf16_f32 v139, v142, v143\n"
      "v_accvgpr_write_b32 a8, v224\n"
      "v_mfma_f32_16x16x32_bf16 v[36:39], a[100:103], v[168:171], v[36:39]\n"
      "s_add_u32 s68, s57, 0xc000\n"
      "v_exp_f32 v144, v144\n"
      "v_mfma_f32_16x16x32_bf16 v[40:43], a[104:107], v[168:171], v[40:43]\n"
      "buffer_load_dwordx4 v212, s[44:47], s68 offen nt lds\n"
      "v_lshlrev_b32 v222, 16, v225\n"
      "v_mfma_f32_16x16x32_bf16 v[44:47], a[108:111], v[168:171], v[44:47]\n"
      "v_exp_f32 v145, v145\n"
      "v_mfma_f32_16x16x32_bf16 v[48:51], a[112:115], v[168:171], v[48:51]\n"
      "buffer_load_dwordx4 v213, s[44:47], s68 offen offset:1024 nt lds\n"
      "v_cvt_pk_bf16_f32 v144, v144, v145\n"
      "v_mfma_f32_16x16x32_bf16 v[52:55], a[116:119], v[168:171], v[52:55]\n"
      "v_exp_f32 v146, v146\n"
      "v_mfma_f32_16x16x32_bf16 v[56:59], a[120:123], v[168:171], v[56:59]\n"
      "v_and_b32 v225, s62, v225\n"
      "v_mul_f32 v222, s61, v222\n"
      "v_mfma_f32_16x16x32_bf16 v[60:63], a[124:127], v[168:171], v[60:63]\n"
      "v_exp_f32 v147, v147\n"
      "v_mfma_f32_16x16x32_bf16 a[136:139], a[144:147], v[176:179], a[136:139]\n"
      "v_cvt_pk_bf16_f32 v145, v146, v147\n"
      "v_mul_f32 v225, s61, v225\n"
      "v_mfma_f32_16x16x32_bf16 v[64:67], a[96:99], v[176:179], v[64:67]\n"
      "v_exp_f32 v148, v148\n"
      "v_mfma_f32_16x16x32_bf16 v[68:71], a[100:103], v[176:179], v[68:71]\n"
      "v_cvt_pk_bf16_f32 v225, v222, v225\n"
      "v_accvgpr_write_b32 a9, v225\n"
      "v_mfma_f32_16x16x32_bf16 v[72:75], a[104:107], v[176:179], v[72:75]\n"
      "v_exp_f32 v149, v149\n"
      "v_mfma_f32_16x16x32_bf16 v[76:79], a[108:111], v[176:179], v[76:79]\n"
      "v_cvt_pk_bf16_f32 v146, v148, v149\n"
      "v_lshlrev_b32 v221, 16, v226\n"
      "v_mfma_f32_16x16x32_bf16 v[80:83], a[112:115], v[176:179], v[80:83]\n"
      "v_exp_f32 v150, v150\n"
      "v_mfma_f32_16x16x32_bf16 v[84:87], a[116:119], v[176:179], v[84:87]\n"
      "s_add_u32 m0, s58, 0x8000\n"
      "ds_read_b128 v[228:231], v209 offset:1536\n"
      "v_and_b32 v226, s62, v226\n"
      "v_mfma_f32_16x16x32_bf16 v[88:91], a[120:123], v[176:179], v[88:91]\n"
      "s_add_u32 s67, s57, 0x8000\n"
      "v_exp_f32 v151, v151\n"
      "v_mfma_f32_16x16x32_bf16 v[92:95], a[124:127], v[176:179], v[92:95]\n"
      "buffer_load_dwordx4 v212, s[48:51], s67 offen nt lds\n"
      "v_cvt_pk_bf16_f32 v147, v150, v151\n"
      "v_mfma_f32_16x16x32_bf16 a[140:143], a[144:147], v[184:187], a[140:143]\n"
      "v_exp_f32 v152, v152\n"
      "v_mfma_f32_16x16x32_bf16 v[96:99], a[96:99], v[184:187], v[96:99]\n"
      "buffer_load_dwordx4 v213, s[48:51], s67 offen offset:1024 nt lds\n"
      "v_mul_f32 v221, s61, v221\n"
      "v_mfma_f32_16x16x32_bf16 v[100:103], a[100:103], v[184:187], v[100:103]\n"
      "v_exp_f32 v153, v153\n"
      "v_mfma_f32_16x16x32_bf16 v[104:107], a[104:107], v[184:187], v[104:107]\n"
      "v_cvt_pk_bf16_f32 v152, v152, v153\n"
      "v_mul_f32 v226, s61, v226\n"
      "v_mfma_f32_16x16x32_bf16 v[108:111], a[108:111], v[184:187], v[108:111]\n"
      "v_exp_f32 v154, v154\n"
      "v_mfma_f32_16x16x32_bf16 v[112:115], a[112:115], v[184:187], v[112:115]\n"
      "v_cvt_pk_bf16_f32 v226, v221, v226\n"
      "v_accvgpr_write_b32 a10, v226\n"
      "v_mfma_f32_16x16x32_bf16 v[116:119], a[116:119], v[184:187], v[116:119]\n"
      "v_lshlrev_b32 v222, 16, v227\n"
      "v_and_b32 v227, s62, v227\n"
      "v_mfma_f32_16x16x32_bf16 v[120:123], a[120:123], v[184:187], v[120:123]\n"
      "s_memrealtime s[84:85]\n"
      "v_mul_f32 v222, s61, v222\n"
      "v_mul_f32 v227, s61, v227\n"
      "v_mfma_f32_16x16x32_bf16 v[124:127], a[124:127], v[184:187], v[124:127]\n"
      "v_cvt_pk_bf16_f32 v227, v222, v227\n"
      "v_accvgpr_write_b32 a11, v227\n"
      "s_waitcnt lgkmcnt(0)\n"
      "s_sub_u32 s85, s84, s86\n"
      "s_cmp_ge_u32 s85, s87\n"
      "s_cbranch_scc1 L_PACED1_2\n"
      "L_PACE1_2:\n"
      "s_memrealtime s[84:85]\n"
      "s_waitcnt lgkmcnt(0)\n"
      "s_sub_u32 s85, s84, s86\n"
      "s_cmp_lt_u32 s85, s87\n"
      "s_cbranch_scc1 L_PACE1_2\n"
      "L_PACED1_2:\n"
      "s_mov_b32 s86, s84\n"
      "v_mfma_f32_16x16x32_bf16 v[160:163], a[64:67], a[160:163], v[192:195]\n"
      "s_waitcnt vmcnt(10)\n"
      "s_barrier\n"
      "v_lshlrev_b32 v221, 16, v228\n"
      "v_and_b32 v228, s62, v228\n"
      "v_mul_f32 v221, s61, v221\n"
      "v_mfma_f32_16x16x32_bf16 v[160:163], a[68:71], a[164:167], v[160:163]\n"
      "v_exp_f32 v155, v155\n"
      "v_mfma_f32_16x16x32_bf16 v[160:163], a[72:75], a[168:171], v[160:163]\n"
      "ds_read_b128 a[148:151], v219 offset:10240\n"
      "v_cvt_pk_bf16_f32 v153, v154, v155\n"
      "v_mfma_f32_16x16x32_bf16 v[160:163], a[76:79], a[172:175], v[160:163]\n"
      "v_exp_f32 v156, v156\n"
      "v_mfma_f32_16x16x32_bf16 v[164:167], a[80:83], a[160:163], v[192:195]\n"
      "ds_read_b64_tr_b16 a[96:97], v210 offset:49152\n"
      "ds_read_b64_tr_b16 a[98:99], v210 offset:53248\n"
      "v_mfma_f32_16x16x32_bf16 v[164:167], a[84:87], a[164:167], v[164:167]\n"
      "v_exp_f32 v157, v157\n"
      "v_mfma_f32_16x16x32_bf16 v[164:167], a[88:91], a[168:171], v[164:167]\n"
      "ds_read_b64_tr_b16 a[100:101], v211 offset:49152\n"
      "v_cvt_pk_bf16_f32 v154, v156, v157\n"
      "v_mfma_f32_16x16x32_bf16 v[164:167], a[92:95], a[172:175], v[164:167]\n"
      "v_exp_f32 v158, v158\n"
      "v_mfma_f32_16x16x32_bf16 v[168:171], a[64:67], a[176:179], v[196:199]\n"
      "ds_read_b64_tr_b16 a[102:103], v211 offset:53248\n"
      "ds_read_b64_tr_b16 a[104:105], v210 offset:49664\n"
      "v_mfma_f32_16x16x32_bf16 v[168:171], a[68:71], a[180:183], v[168:171]\n"
      "v_exp_f32 v159, v159\n"
      "v_mfma_f32_16x16x32_bf16 v[168:171], a[72:75], a[184:187], v[168:171]\n"
      "ds_read_b64_tr_b16 a[106:107], v210 offset:53760\n"
      "v_cvt_pk_bf16_f32 v155, v158, v159\n"
      "v_mfma_f32_16x16x32_bf16 v[168:171], a[76:79], a[188:191], v[168:171]\n"
      "v_exp_f32 v160, v160\n"
      "v_mfma_f32_16x16x32_bf16 v[172:175], a[80:83], a[176:179], v[196:199]\n"
      "ds_read_b64_tr_b16 a[108:109], v211 offset:49664\n"
      "ds_read_b64_tr_b16 a[110:111], v211 offset:53760\n"
      "v_mfma_f32_16x16x32_bf16 v[172:175], a[84:87], a[180:183], v[172:175]\n"
      "v_exp_f32 v161, v161\n"
      "v_mfma_f32_16x16x32_bf16 v[172:175], a[88:91], a[184:187], v[172:175]\n"
      "ds_read_b64_tr_b16 a[112:113], v210 offset:50176\n"
      "v_cvt_pk_bf16_f32 v160, v160, v161\n"
      "v_mfma_f32_16x16x32_bf16 v[172:175], a[92:95], a[188:191], v[172:175]\n"
      "v_exp_f32 v162, v162\n"
      "v_mfma_f32_16x16x32_bf16 v[176:179], a[64:67], a[192:195], v[200:203]\n"
      "ds_read_b64_tr_b16 a[114:115], v210 offset:54272\n"
      "ds_read_b64_tr_b16 a[116:117], v211 offset:50176\n"
      "v_mfma_f32_16x16x32_bf16 v[176:179], a[68:71], a[196:199], v[176:179]\n"
      "v_exp_f32 v163, v163\n"
      "v_mfma_f32_16x16x32_bf16 v[176:179], a[72:75], a[200:203], v[176:179]\n"
      "ds_read_b64_tr_b16 a[118:119], v211 offset:54272\n"
      "v_cvt_pk_bf16_f32 v161, v162, v163\n"
      "v_mfma_f32_16x16x32_bf16 v[176:179], a[76:79], a[204:207], v[176:179]\n"
      "v_exp_f32 v164, v164\n"
      "v_mfma_f32_16x16x32_bf16 v[180:183], a[80:83], a[192:195], v[200:203]\n"
      "ds_read_b64_tr_b16 a[120:121], v210 offset:50688\n"
      "ds_read_b64_tr_b16 a[122:123], v210 offset:54784\n"
      "v_mfma_f32_16x16x32_bf16 v[180:183], a[84:87], a[196:199], v[180:183]\n"
      "v_exp_f32 v165, v165\n"
      "v_mfma_f32_16x16x32_bf16 v[180:183], a[88:91], a[200:203], v[180:183]\n"
      "ds_read_b64_tr_b16 a[124:125], v211 offset:50688\n"
      "v_cvt_pk_bf16_f32 v162, v164, v165\n"
      "v_mfma_f32_16x16x32_bf16 v[180:183], a[92:95], a[204:207], v[180:183]\n"
      "v_exp_f32 v166, v166\n"
      "v_mfma_f32_16x16x32_bf16 v[184:187], a[64:67], a[208:211], v[204:207]\n"
      "ds_read_b64_tr_b16 a[126:127], v211 offset:54784\n"
      "v_mul_f32 v228, s61, v228\n"
      "v_mfma_f32_16x16x32_bf16 v[184:187], a[68:71], a[212:215], v[184:187]\n"
      "v_exp_f32 v167, v167\n"
      "v_mfma_f32_16x16x32_bf16 v[184:187], a[72:75], a[216:219], v[184:187]\n"
      "v_cvt_pk_bf16_f32 v163, v166, v167\n"
      "v_cvt_pk_bf16_f32 v228, v221, v228\n"
      "v_mfma_f32_16x16x32_bf16 v[184:187], a[76:79], a[220:223], v[184:187]\n"
      "v_exp_f32 v168, v168\n"
      "v_mfma_f32_16x16x32_bf16 v[188:191], a[80:83], a[208:211], v[204:207]\n"
      "ds_read_b128 a[64:67], v208 offset:0\n"
      "ds_read_b128 a[68:71], v208 offset:512\n"
      "v_mfma_f32_16x16x32_bf16 v[188:191], a[84:87], a[212:215], v[188:191]\n"
      "v_exp_f32 v169, v169\n"
      "v_mfma_f32_16x16x32_bf16 v[188:191], a[88:91], a[216:219], v[188:191]\n"
      "ds_read_b128 a[72:75], v208 offset:1024\n"
      "v_cvt_pk_bf16_f32 v168, v168, v169\n"
      "v_mfma_f32_16x16x32_bf16 v[188:191], a[92:95], a[220:223], v[188:191]\n"
      "v_exp_f32 v170, v170\n"
      "v_mfma_f32_16x16x32_bf16 a[128:131], a[144:147], v[128:131], a[128:131]\n"
      "ds_read_b128 a[76:79], v208 offset:1536\n"
      "v_accvgpr_write_b32 a12, v228\n"
      "s_waitcnt lgkmcnt(12)\n"
      "v_mfma_f32_16x16x32_bf16 v[0:3], a[96:99], v[128:131], v[0:3]\n"
      "v_exp_f32 v171, v171\n"
      "v_mfma_f32_16x16x32_bf16 v[4:7], a[100:103], v[128:131], v[4:7]\n"
      "ds_read_b128 a[80:83], v208 offset:4096\n"
      "v_cvt_pk_bf16_f32 v169, v170, v171\n"
      "v_mfma_f32_16x16x32_bf16 v[8:11], a[104:107], v[128:131], v[8:11]\n"
      "v_exp_f32 v172, v172\n"
      "v_mfma_f32_16x16x32_bf16 v[12:15], a[108:111], v[128:131], v[12:15]\n"
      "ds_read_b128 a[84:87], v208 offset:4608\n"
      "ds_read_b128 a[88:91], v208 offset:5120\n"
      "v_lshlrev_b32 v222, 16, v229\n"
      "s_waitcnt lgkmcnt(7)\n"
      "v_mfma_f32_16x16x32_bf16 v[16:19], a[112:115], v[128:131], v[16:19]\n"
      "v_exp_f32 v173, v173\n"
      "v_mfma_f32_16x16x32_bf16 v[20:23], a[116:119], v[128:131], v[20:23]\n"
      "ds_read_b128 a[92:95], v208 offset:5632\n"
      "v_cvt_pk_bf16_f32 v170, v172, v173\n"
      "v_mfma_f32_16x16x32_bf16 v[24:27], a[120:123], v[128:131], v[24:27]\n"
      "v_exp_f32 v174, v174\n"
      "v_mfma_f32_16x16x32_bf16 v[28:31], a[124:127], v[128:131], v[28:31]\n"
      "s_add_u32 m0, s58, 0x6000\n"
      "buffer_store_dwordx4 a[148:151], v214, s[52:55], s76 offen offset:2048 sc1\n"
      "v_mfma_f32_16x16x32_bf16 a[132:135], a[144:147], v[136:139], a[132:135]\n"
      "s_add_u32 s67, s57, 0xe000\n"
      "v_exp_f32 v175, v175\n"
      "v_mfma_f32_16x16x32_bf16 v[32:35], a[96:99], v[136:139], v[32:35]\n"
      "buffer_load_dwordx4 v212, s[44:47], s67 offen nt lds\n"
      "v_cvt_pk_bf16_f32 v171, v174, v175\n"
      "v_mfma_f32_16x16x32_bf16 v[36:39], a[100:103], v[136:139], v[36:39]\n"
      "v_exp_f32 v176, v176\n"
      "v_mfma_f32_16x16x32_bf16 v[40:43], a[104:107], v[136:139], v[40:43]\n"
      "buffer_load_dwordx4 v213, s[44:47], s67 offen offset:1024 nt lds\n"
      "v_and_b32 v229, s62, v229\n"
      "v_mfma_f32_16x16x32_bf16 v[44:47], a[108:111], v[136:139], v[44:47]\n"
      "v_exp_f32 v177, v177\n"
      "v_mfma_f32_16x16x32_bf16 v[48:51], a[112:115], v[136:139], v[48:51]\n"
      "v_cvt_pk_bf16_f32 v176, v176, v177\n"
      "v_mul_f32 v222, s61, v222\n"
      "v_mfma_f32_16x16x32_bf16 v[52:55], a[116:119], v[136:139], v[52:55]\n"
      "v_exp_f32 v178, v178\n"
      "v_mfma_f32_16x16x32_bf16 v[56:59], a[120:123], v[136:139], v[56:59]\n"
      "v_mul_f32 v229, s61, v229\n"
      "v_cvt_pk_bf16_f32 v229, v222, v229\n"
      "v_mfma_f32_16x16x32_bf16 v[60:63], a[124:127], v[136:139], v[60:63]\n"
      "v_exp_f32 v179, v179\n"
      "v_mfma_f32_16x16x32_bf16 a[136:139], a[144:147], v[144:147], a[136:139]\n"
      "s_add_u32 m0, s58, 0xa000\n"
      "v_cvt_pk_bf16_f32 v177, v178, v179\n"
      "v_accvgpr_write_b32 a13, v229\n"
      "v_mfma_f32_16x16x32_bf16 v[64:67], a[96:99], v[144:147], v[64:67]\n"
      "s_add_u32 s68, s57, 0xa000\n"
      "v_exp_f32 v180, v180\n"
      "v_mfma_f32_16x16x32_bf16 v[68:71], a[100:103], v[144:147], v[68:71]\n"
      "buffer_load_dwordx4 v212, s[48:51], s68 offen nt lds\n"
      "v_lshlrev_b32 v221, 16, v230\n"
      "v_mfma_f32_16x16x32_bf16 v[72:75], a[104:107], v[144:147], v[72:75]\n"
      "v_exp_f32 v181, v181\n"
      "v_mfma_f32_16x16x32_bf16 v[76:79], a[108:111], v[144:147], v[76:79]\n"
      "buffer_load_dwordx4 v213, s[48:51], s68 offen offset:1024 nt lds\n"
      "v_cvt_pk_bf16_f32 v178, v180, v181\n"
      "v_mfma_f32_16x16x32_bf16 v[80:83], a[112:115], v[144:147], v[80:83]\n"
      "v_exp_f32 v182, v182\n"
      "v_mfma_f32_16x16x32_bf16 v[84:87], a[116:119], v[144:147], v[84:87]\n"
      "ds_read_b128 v[224:227], v209 offset:4096\n"
      "v_and_b32 v230, s62, v230\n"
      "v_mfma_f32_16x16x32_bf16 v[88:91], a[120:123], v[144:147], v[88:91]\n"
      "v_exp_f32 v183, v183\n"
      "v_mfma_f32_16x16x32_bf16 v[92:95], a[124:127], v[144:147], v[92:95]\n"
      "s_mov_b32 m0, s59\n"
      "v_cvt_pk_bf16_f32 v179, v182, v183\n"
      "v_mul_f32 v221, s61, v221\n"
      "v_mfma_f32_16x16x32_bf16 a[140:143], a[144:147], v[152:155], a[140:143]\n"
      "s_add_u32 s68, s56, 0x1002000\n"
      "v_exp_f32 v184, v184\n"
      "v_mfma_f32_16x16x32_bf16 v[96:99], a[96:99], v[152:155], v[96:99]\n"
      "buffer_load_dwordx4 v212, s[40:43], s68 offen nt lds\n"
      "v_mul_f32 v230, s61, v230\n"
      "v_mfma_f32_16x16x32_bf16 v[100:103], a[100:103], v[152:155], v[100:103]\n"
      "v_exp_f32 v185, v185\n"
      "v_mfma_f32_16x16x32_bf16 v[104:107], a[104:107], v[152:155], v[104:107]\n"
      "buffer_load_dwordx4 v213, s[40:43], s68 offen offset:1024 nt lds\n"
      "v_cvt_pk_bf16_f32 v184, v184, v185\n"
      "v_mfma_f32_16x16x32_bf16 v[108:111], a[108:111], v[152:155], v[108:111]\n"
      "v_exp_f32 v186, v186\n"
      "v_mfma_f32_16x16x32_bf16 v[112:115], a[112:115], v[152:155], v[112:115]\n"
      "v_cvt_pk_bf16_f32 v230, v221, v230\n"
      "v_accvgpr_write_b32 a14, v230\n"
      "v_mfma_f32_16x16x32_bf16 v[116:119], a[116:119], v[152:155], v[116:119]\n"
      "v_lshlrev_b32 v222, 16, v231\n"
      "v_and_b32 v231, s62, v231\n"
      "v_mfma_f32_16x16x32_bf16 v[120:123], a[120:123], v[152:155], v[120:123]\n"
      "v_mul_f32 v222, s61, v222\n"
      "v_mul_f32 v231, s61, v231\n"
      "v_mfma_f32_16x16x32_bf16 v[124:127], a[124:127], v[152:155], v[124:127]\n"
      "v_cvt_pk_bf16_f32 v231, v222, v231\n"
      "v_accvgpr_write_b32 a15, v231\n"
      "s_waitcnt lgkmcnt(0)\n"
      "v_mfma_f32_16x16x32_bf16 v[128:131], a[64:67], a[160:163], v[192:195]\n"
      "s_waitcnt vmcnt(12)\n"
      "s_barrier\n"
      "v_lshlrev_b32 v221, 16, v224\n"
      "v_and_b32 v224, s62, v224\n"
      "v_mul_f32 v221, s61, v221\n"
      "v_mfma_f32_16x16x32_bf16 v[128:131], a[68:71], a[164:167], v[128:131]\n"
      "v_exp_f32 v187, v187\n"
      "v_mfma_f32_16x16x32_bf16 v[128:131], a[72:75], a[168:171], v[128:131]\n"
      "ds_read_b128 a[152:155], v220 offset:11264\n"
      "v_cvt_pk_bf16_f32 v185, v186, v187\n"
      "v_mfma_f32_16x16x32_bf16 v[128:131], a[76:79], a[172:175], v[128:131]\n"
      "v_exp_f32 v188, v188\n"
      "v_mfma_f32_16x16x32_bf16 v[132:135], a[80:83], a[160:163], v[192:195]\n"
      "ds_read_b64_tr_b16 a[96:97], v210 offset:57344\n"
      "ds_read_b64_tr_b16 a[98:99], v210 offset:61440\n"
      "v_mfma_f32_16x16x32_bf16 v[132:135], a[84:87], a[164:167], v[132:135]\n"
      "v_exp_f32 v189, v189\n"
      "v_mfma_f32_16x16x32_bf16 v[132:135], a[88:91], a[168:171], v[132:135]\n"
      "ds_read_b64_tr_b16 a[100:101], v211 offset:57344\n"
      "v_cvt_pk_bf16_f32 v186, v188, v189\n"
      "v_mfma_f32_16x16x32_bf16 v[132:135], a[92:95], a[172:175], v[132:135]\n"
      "v_exp_f32 v190, v190\n"
      "v_mfma_f32_16x16x32_bf16 v[136:139], a[64:67], a[176:179], v[196:199]\n"
      "ds_read_b64_tr_b16 a[102:103], v211 offset:61440\n"
      "ds_read_b64_tr_b16 a[104:105], v210 offset:57856\n"
      "v_mfma_f32_16x16x32_bf16 v[136:139], a[68:71], a[180:183], v[136:139]\n"
      "v_exp_f32 v191, v191\n"
      "v_mfma_f32_16x16x32_bf16 v[136:139], a[72:75], a[184:187], v[136:139]\n"
      "ds_read_b64_tr_b16 a[106:107], v210 offset:61952\n"
      "v_cvt_pk_bf16_f32 v187, v190, v191\n"
      "v_mfma_f32_16x16x32_bf16 v[136:139], a[76:79], a[188:191], v[136:139]\n"
      "v_exp_f32 v128, v128\n"
      "v_mfma_f32_16x16x32_bf16 v[140:143], a[80:83], a[176:179], v[196:199]\n"
      "ds_read_b64_tr_b16 a[108:109], v211 offset:57856\n"
      "ds_read_b64_tr_b16 a[110:111], v211 offset:61952\n"
      "v_mfma_f32_16x16x32_bf16 v[140:143], a[84:87], a[180:183], v[140:143]\n"
      "v_exp_f32 v129, v129\n"
      "v_mfma_f32_16x16x32_bf16 v[140:143], a[88:91], a[184:187], v[140:143]\n"
      "ds_read_b64_tr_b16 a[112:113], v210 offset:58368\n"
      "v_cvt_pk_bf16_f32 v128, v128, v129\n"
      "v_mfma_f32_16x16x32_bf16 v[140:143], a[92:95], a[188:191], v[140:143]\n"
      "v_exp_f32 v130, v130\n"
      "v_mfma_f32_16x16x32_bf16 v[144:147], a[64:67], a[192:195], v[200:203]\n"
      "ds_read_b64_tr_b16 a[114:115], v210 offset:62464\n"
      "ds_read_b64_tr_b16 a[116:117], v211 offset:58368\n"
      "v_mfma_f32_16x16x32_bf16 v[144:147], a[68:71], a[196:199], v[144:147]\n"
      "v_exp_f32 v131, v131\n"
      "v_mfma_f32_16x16x32_bf16 v[144:147], a[72:75], a[200:203], v[144:147]\n"
      "ds_read_b64_tr_b16 a[118:119], v211 offset:62464\n"
      "v_cvt_pk_bf16_f32 v129, v130, v131\n"
      "v_mfma_f32_16x16x32_bf16 v[144:147], a[76:79], a[204:207], v[144:147]\n"
      "v_exp_f32 v132, v132\n"
      "v_mfma_f32_16x16x32_bf16 v[148:151], a[80:83], a[192:195], v[200:203]\n"
      "ds_read_b64_tr_b16 a[120:121], v210 offset:58880\n"
      "ds_read_b64_tr_b16 a[122:123], v210 offset:62976\n"
      "v_mfma_f32_16x16x32_bf16 v[148:151], a[84:87], a[196:199], v[148:151]\n"
      "v_exp_f32 v133, v133\n"
      "v_mfma_f32_16x16x32_bf16 v[148:151], a[88:91], a[200:203], v[148:151]\n"
      "ds_read_b64_tr_b16 a[124:125], v211 offset:58880\n"
      "v_cvt_pk_bf16_f32 v130, v132, v133\n"
      "v_mfma_f32_16x16x32_bf16 v[148:151], a[92:95], a[204:207], v[148:151]\n"
      "v_exp_f32 v134, v134\n"
      "v_mfma_f32_16x16x32_bf16 v[152:155], a[64:67], a[208:211], v[204:207]\n"
      "ds_read_b64_tr_b16 a[126:127], v211 offset:62976\n"
      "v_mul_f32 v224, s61, v224\n"
      "v_mfma_f32_16x16x32_bf16 v[152:155], a[68:71], a[212:215], v[152:155]\n"
      "v_exp_f32 v135, v135\n"
      "v_mfma_f32_16x16x32_bf16 v[152:155], a[72:75], a[216:219], v[152:155]\n"
      "v_cvt_pk_bf16_f32 v131, v134, v135\n"
      "v_cvt_pk_bf16_f32 v224, v221, v224\n"
      "v_mfma_f32_16x16x32_bf16 v[152:155], a[76:79], a[220:223], v[152:155]\n"
      "v_exp_f32 v136, v136\n"
      "v_mfma_f32_16x16x32_bf16 v[156:159], a[80:83], a[208:211], v[204:207]\n"
      "ds_read_b128 a[64:67], v208 offset:8192\n"
      "ds_read_b128 a[68:71], v208 offset:8704\n"
      "v_mfma_f32_16x16x32_bf16 v[156:159], a[84:87], a[212:215], v[156:159]\n"
      "v_exp_f32 v137, v137\n"
      "v_mfma_f32_16x16x32_bf16 v[156:159], a[88:91], a[216:219], v[156:159]\n"
      "ds_read_b128 a[72:75], v208 offset:9216\n"
      "v_cvt_pk_bf16_f32 v136, v136, v137\n"
      "v_mfma_f32_16x16x32_bf16 v[156:159], a[92:95], a[220:223], v[156:159]\n"
      "v_exp_f32 v138, v138\n"
      "v_mfma_f32_16x16x32_bf16 a[128:131], a[144:147], v[160:163], a[128:131]\n"
      "ds_read_b128 a[76:79], v208 offset:9728\n"
      "v_accvgpr_write_b32 a16, v224\n"
      "s_waitcnt lgkmcnt(12)\n"
      "v_mfma_f32_16x16x32_bf16 v[0:3], a[96:99], v[160:163], v[0:3]\n"
      "v_exp_f32 v139, v139\n"
      "v_mfma_f32_16x16x32_bf16 v[4:7], a[100:103], v[160:163], v[4:7]\n"
      "ds_read_b128 a[80:83], v208 offset:12288\n"
      "v_cvt_pk_bf16_f32 v137, v138, v139\n"
      "v_mfma_f32_16x16x32_bf16 v[8:11], a[104:107], v[160:163], v[8:11]\n"
      "v_exp_f32 v140, v140\n"
      "v_mfma_f32_16x16x32_bf16 v[12:15], a[108:111], v[160:163], v[12:15]\n"
      "ds_read_b128 a[84:87], v208 offset:12800\n"
      "ds_read_b128 a[88:91], v208 offset:13312\n"
      "v_lshlrev_b32 v222, 16, v225\n"
      "s_waitcnt lgkmcnt(7)\n"
      "v_mfma_f32_16x16x32_bf16 v[16:19], a[112:115], v[160:163], v[16:19]\n"
      "v_exp_f32 v141, v141\n"
      "v_mfma_f32_16x16x32_bf16 v[20:23], a[116:119], v[160:163], v[20:23]\n"
      "ds_read_b128 a[92:95], v208 offset:13824\n"
      "v_cvt_pk_bf16_f32 v138, v140, v141\n"
      "v_mfma_f32_16x16x32_bf16 v[24:27], a[120:123], v[160:163], v[24:27]\n"
      "v_exp_f32 v142, v142\n"
      "v_mfma_f32_16x16x32_bf16 v[28:31], a[124:127], v[160:163], v[28:31]\n"
      "s_mov_b32 m0, s58\n"
      "buffer_store_dwordx4 a[152:155], v214, s[52:55], s76 offen offset:3072 sc1\n"
      "v_mfma_f32_16x16x32_bf16 a[132:135], a[144:147], v[168:171], a[132:135]\n"
      "s_add_u32 s68, s57, 0x10000\n"
      "v_exp_f32 v143, v143\n"
      "v_mfma_f32_16x16x32_bf16 v[32:35], a[96:99], v[168:171], v[32:35]\n"
      "buffer_load_dwordx4 v212, s[44:47], s68 offen nt lds\n"
      "v_cvt_pk_bf16_f32 v139, v142, v143\n"
      "v_mfma_f32_16x16x32_bf16 v[36:39], a[100:103], v[168:171], v[36:39]\n"
      "v_exp_f32 v144, v144\n"
      "v_mfma_f32_16x16x32_bf16 v[40:43], a[104:107], v[168:171], v[40:43]\n"
      "buffer_load_dwordx4 v213, s[44:47], s68 offen offset:1024 nt lds\n"
      "v_and_b32 v225, s62, v225\n"
      "v_mfma_f32_16x16x32_bf16 v[44:47], a[108:111], v[168:171], v[44:47]\n"
      "v_exp_f32 v145, v145\n"
      "v_mfma_f32_16x16x32_bf16 v[48:51], a[112:115], v[168:171], v[48:51]\n"
      "v_cvt_pk_bf16_f32 v144, v144, v145\n"
      "v_mul_f32 v222, s61, v222\n"
      "v_mfma_f32_16x16x32_bf16 v[52:55], a[116:119], v[168:171], v[52:55]\n"
      "v_exp_f32 v146, v146\n"
      "v_mfma_f32_16x16x32_bf16 v[56:59], a[120:123], v[168:171], v[56:59]\n"
      "v_mul_f32 v225, s61, v225\n"
      "v_cvt_pk_bf16_f32 v225, v222, v225\n"
      "v_mfma_f32_16x16x32_bf16 v[60:63], a[124:127], v[168:171], v[60:63]\n"
      "v_exp_f32 v147, v147\n"
      "v_mfma_f32_16x16x32_bf16 a[136:139], a[144:147], v[176:179], a[136:139]\n"
      "s_add_u32 m0, s58, 0xc000\n"
      "v_cvt_pk_bf16_f32 v145, v146, v147\n"
      "v_accvgpr_write_b32 a17, v225\n"
      "v_mfma_f32_16x16x32_bf16 v[64:67], a[96:99], v[176:179], v[64:67]\n"
      "s_add_u32 s67, s57, 0xc000\n"
      "v_exp_f32 v148, v148\n"
      "v_mfma_f32_16x16x32_bf16 v[68:71], a[100:103], v[176:179], v[68:71]\n"
      "buffer_load_dwordx4 v212, s[48:51], s67 offen nt lds\n"
      "v_lshlrev_b32 v221, 16, v226\n"
      "v_mfma_f32_16x16x32_bf16 v[72:75], a[104:107], v[176:179], v[72:75]\n"
      "v_exp_f32 v149, v149\n"
      "v_mfma_f32_16x16x32_bf16 v[76:79], a[108:111], v[176:179], v[76:79]\n"
      "buffer_load_dwordx4 v213, s[48:51], s67 offen offset:1024 nt lds\n"
      "v_cvt_pk_bf16_f32 v146, v148, v149\n"
      "v_mfma_f32_16x16x32_bf16 v[80:83], a[112:115], v[176:179], v[80:83]\n"
      "v_exp_f32 v150, v150\n"
      "v_mfma_f32_16x16x32_bf16 v[84:87], a[116:119], v[176:179], v[84:87]\n"
      "ds_read_b128 v[228:231], v209 offset:4608\n"
      "v_and_b32 v226, s62, v226\n"
      "v_mfma_f32_16x16x32_bf16 v[88:91], a[120:123], v[176:179], v[88:91]\n"
      "v_exp_f32 v151, v151\n"
      "v_mfma_f32_16x16x32_bf16 v[92:95], a[124:127], v[176:179], v[92:95]\n"
      "s_add_u32 m0, s59, 0x800\n"
      "v_cvt_pk_bf16_f32 v147, v150, v151\n"
      "v_mul_f32 v221, s61, v221\n"
      "v_mfma_f32_16x16x32_bf16 a[140:143], a[144:147], v[184:187], a[140:143]\n"
      "s_add_u32 s67, s56, 0x1002800\n"
      "v_exp_f32 v152, v152\n"
      "v_mfma_f32_16x16x32_bf16 v[96:99], a[96:99], v[184:187], v[96:99]\n"
      "buffer_load_dwordx4 v212, s[40:43], s67 offen nt lds\n"
      "v_mul_f32 v226, s61, v226\n"
      "v_mfma_f32_16x16x32_bf16 v[100:103], a[100:103], v[184:187], v[100:103]\n"
      "v_exp_f32 v153, v153\n"
      "v_mfma_f32_16x16x32_bf16 v[104:107], a[104:107], v[184:187], v[104:107]\n"
      "buffer_load_dwordx4 v213, s[40:43], s67 offen offset:1024 nt lds\n"
      "v_cvt_pk_bf16_f32 v152, v152, v153\n"
      "v_mfma_f32_16x16x32_bf16 v[108:111], a[108:111], v[184:187], v[108:111]\n"
      "v_exp_f32 v154, v154\n"
      "v_mfma_f32_16x16x32_bf16 v[112:115], a[112:115], v[184:187], v[112:115]\n"
      "v_cvt_pk_bf16_f32 v226, v221, v226\n"
      "v_accvgpr_write_b32 a18, v226\n"
      "v_mfma_f32_16x16x32_bf16 v[116:119], a[116:119], v[184:187], v[116:119]\n"
      "v_lshlrev_b32 v222, 16, v227\n"
      "v_and_b32 v227, s62, v227\n"
      "v_mfma_f32_16x16x32_bf16 v[120:123], a[120:123], v[184:187], v[120:123]\n"
      "s_memrealtime s[84:85]\n"
      "v_mul_f32 v222, s61, v222\n"
      "v_mul_f32 v227, s61, v227\n"
      "v_mfma_f32_16x16x32_bf16 v[124:127], a[124:127], v[184:187], v[124:127]\n"
      "v_cvt_pk_bf16_f32 v227, v222, v227\n"
      "v_accvgpr_write_b32 a19, v227\n"
      "s_waitcnt lgkmcnt(0)\n"
      "s_sub_u32 s85, s84, s86\n"
      "s_cmp_ge_u32 s85, s87\n"
      "s_cbranch_scc1 L_PACED1_4\n"
      "L_PACE1_4:\n"
      "s_memrealtime s[84:85]\n"
      "s_waitcnt lgkmcnt(0)\n"
      "s_sub_u32 s85, s84, s86\n"
      "s_cmp_lt_u32 s85, s87\n"
      "s_cbranch_scc1 L_PACE1_4\n"
      "L_PACED1_4:\n"
      "s_mov_b32 s86, s84\n"
      "v_mfma_f32_16x16x32_bf16 v[160:163], a[64:67], a[160:163], v[192:195]\n"
      "s_waitcnt vmcnt(14)\n"
      "s_barrier\n"
      "v_lshlrev_b32 v221, 16, v228\n"
      "v_and_b32 v228, s62, v228\n"
      "v_mfma_f32_16x16x32_bf16 v[160:163], a[68:71], a[164:167], v[160:163]\n"
      "v_exp_f32 v155, v155\n"
      "v_mfma_f32_16x16x32_bf16 v[160:163], a[72:75], a[168:171], v[160:163]\n"
      "ds_read_b128 a[148:151], v219 offset:12288\n"
      "v_cvt_pk_bf16_f32 v153, v154, v155\n"
      "v_mfma_f32_16x16x32_bf16 v[160:163], a[76:79], a[172:175], v[160:163]\n"
      "v_exp_f32 v156, v156\n"
      "v_mfma_f32_16x16x32_bf16 v[164:167], a[80:83], a[160:163], v[192:195]\n"
      "ds_read_b64_tr_b16 a[96:97], v210 offset:32768\n"
      "ds_read_b64_tr_b16 a[98:99], v210 offset:36864\n"
      "v_mfma_f32_16x16x32_bf16 v[164:167], a[84:87], a[164:167], v[164:167]\n"
      "v_exp_f32 v157, v157\n"
      "v_mfma_f32_16x16x32_bf16 v[164:167], a[88:91], a[168:171], v[164:167]\n"
      "ds_read_b64_tr_b16 a[100:101], v211 offset:32768\n"
      "v_cvt_pk_bf16_f32 v154, v156, v157\n"
      "v_mfma_f32_16x16x32_bf16 v[164:167], a[92:95], a[172:175], v[164:167]\n"
      "v_exp_f32 v158, v158\n"
      "v_mfma_f32_16x16x32_bf16 v[168:171], a[64:67], a[176:179], v[196:199]\n"
      "ds_read_b64_tr_b16 a[102:103], v211 offset:36864\n"
      "ds_read_b64_tr_b16 a[104:105], v210 offset:33280\n"
      "v_mfma_f32_16x16x32_bf16 v[168:171], a[68:71], a[180:183], v[168:171]\n"
      "v_exp_f32 v159, v159\n"
      "v_mfma_f32_16x16x32_bf16 v[168:171], a[72:75], a[184:187], v[168:171]\n"
      "ds_read_b64_tr_b16 a[106:107], v210 offset:37376\n"
      "v_cvt_pk_bf16_f32 v155, v158, v159\n"
      "v_mfma_f32_16x16x32_bf16 v[168:171], a[76:79], a[188:191], v[168:171]\n"
      "v_exp_f32 v160, v160\n"
      "v_mfma_f32_16x16x32_bf16 v[172:175], a[80:83], a[176:179], v[196:199]\n"
      "ds_read_b64_tr_b16 a[108:109], v211 offset:33280\n"
      "ds_read_b64_tr_b16 a[110:111], v211 offset:37376\n"
      "v_mfma_f32_16x16x32_bf16 v[172:175], a[84:87], a[180:183], v[172:175]\n"
      "v_exp_f32 v161, v161\n"
      "v_mfma_f32_16x16x32_bf16 v[172:175], a[88:91], a[184:187], v[172:175]\n"
      "ds_read_b64_tr_b16 a[112:113], v210 offset:33792\n"
      "v_cvt_pk_bf16_f32 v160, v160, v161\n"
      "v_mfma_f32_16x16x32_bf16 v[172:175], a[92:95], a[188:191], v[172:175]\n"
      "v_exp_f32 v162, v162\n"
      "v_mfma_f32_16x16x32_bf16 v[176:179], a[64:67], a[192:195], v[200:203]\n"
      "ds_read_b64_tr_b16 a[114:115], v210 offset:37888\n"
      "ds_read_b64_tr_b16 a[116:117], v211 offset:33792\n"
      "v_mfma_f32_16x16x32_bf16 v[176:179], a[68:71], a[196:199], v[176:179]\n"
      "v_exp_f32 v163, v163\n"
      "v_mfma_f32_16x16x32_bf16 v[176:179], a[72:75], a[200:203], v[176:179]\n"
      "ds_read_b64_tr_b16 a[118:119], v211 offset:37888\n"
      "v_cvt_pk_bf16_f32 v161, v162, v163\n"
      "v_mfma_f32_16x16x32_bf16 v[176:179], a[76:79], a[204:207], v[176:179]\n"
      "v_exp_f32 v164, v164\n"
      "v_mfma_f32_16x16x32_bf16 v[180:183], a[80:83], a[192:195], v[200:203]\n"
      "ds_read_b64_tr_b16 a[120:121], v210 offset:34304\n"
      "ds_read_b64_tr_b16 a[122:123], v210 offset:38400\n"
      "v_mfma_f32_16x16x32_bf16 v[180:183], a[84:87], a[196:199], v[180:183]\n"
      "v_exp_f32 v165, v165\n"
      "v_mfma_f32_16x16x32_bf16 v[180:183], a[88:91], a[200:203], v[180:183]\n"
      "ds_read_b64_tr_b16 a[124:125], v211 offset:34304\n"
      "v_cvt_pk_bf16_f32 v162, v164, v165\n"
      "v_mfma_f32_16x16x32_bf16 v[180:183], a[92:95], a[204:207], v[180:183]\n"
      "v_exp_f32 v166, v166\n"
      "v_mfma_f32_16x16x32_bf16 v[184:187], a[64:67], a[208:211], v[204:207]\n"
      "ds_read_b64_tr_b16 a[126:127], v211 offset:38400\n"
      "v_mul_f32 v221, s61, v221\n"
      "v_mfma_f32_16x16x32_bf16 v[184:187], a[68:71], a[212:215], v[184:187]\n"
      "v_exp_f32 v167, v167\n"
      "v_mfma_f32_16x16x32_bf16 v[184:187], a[72:75], a[216:219], v[184:187]\n"
      "v_cvt_pk_bf16_f32 v163, v166, v167\n"
      "v_mul_f32 v228, s61, v228\n"
      "v_mfma_f32_16x16x32_bf16 v[184:187], a[76:79], a[220:223], v[184:187]\n"
      "v_exp_f32 v168, v168\n"
      "v_mfma_f32_16x16x32_bf16 v[188:191], a[80:83], a[208:211], v[204:207]\n"
      "ds_read_b128 a[64:67], v208 offset:16384\n"
      "ds_read_b128 a[68:71], v208 offset:16896\n"
      "v_mfma_f32_16x16x32_bf16 v[188:191], a[84:87], a[212:215], v[188:191]\n"
      "v_exp_f32 v169, v169\n"
      "v_mfma_f32_16x16x32_bf16 v[188:191], a[88:91], a[216:219], v[188:191]\n"
      "ds_read_b128 a[72:75], v208 offset:17408\n"
      "v_cvt_pk_bf16_f32 v168, v168, v169\n"
      "v_mfma_f32_16x16x32_bf16 v[188:191], a[92:95], a[220:223], v[188:191]\n"
      "v_exp_f32 v170, v170\n"
      "v_mfma_f32_16x16x32_bf16 a[128:131], a[144:147], v[128:131], a[128:131]\n"
      "ds_read_b128 a[76:79], v208 offset:17920\n"
      "v_cvt_pk_bf16_f32 v228, v221, v228\n"
      "s_waitcnt lgkmcnt(12)\n"
      "v_mfma_f32_16x16x32_bf16 v[0:3], a[96:99], v[128:131], v[0:3]\n"
      "v_exp_f32 v171, v171\n"
      "v_mfma_f32_16x16x32_bf16 v[4:7], a[100:103], v[128:131], v[4:7]\n"
      "ds_read_b128 a[80:83], v208 offset:20480\n"
      "v_cvt_pk_bf16_f32 v169, v170, v171\n"
      "v_mfma_f32_16x16x32_bf16 v[8:11], a[104:107], v[128:131], v[8:11]\n"
      "v_exp_f32 v172, v172\n"
      "v_mfma_f32_16x16x32_bf16 v[12:15], a[108:111], v[128:131], v[12:15]\n"
      "ds_read_b128 a[84:87], v208 offset:20992\n"
      "ds_read_b128 a[88:91], v208 offset:21504\n"
      "s_waitcnt lgkmcnt(7)\n"
      "v_mfma_f32_16x16x32_bf16 v[16:19], a[112:115], v[128:131], v[16:19]\n"
      "v_exp_f32 v173, v173\n"
      "v_mfma_f32_16x16x32_bf16 v[20:23], a[116:119], v[128:131], v[20:23]\n"
      "ds_read_b128 a[92:95], v208 offset:22016\n"
      "v_cvt_pk_bf16_f32 v170, v172, v173\n"
      "v_mfma_f32_16x16x32_bf16 v[24:27], a[120:123], v[128:131], v[24:27]\n"
      "v_exp_f32 v174, v174\n"
      "v_mfma_f32_16x16x32_bf16 v[28:31], a[124:127], v[128:131], v[28:31]\n"
      "buffer_store_dwordx4 a[148:151], v214, s[52:55], s77 offen offset:0 sc1\n"
      "v_mfma_f32_16x16x32_bf16 a[132:135], a[144:147], v[136:139], a[132:135]\n"
      "v_exp_f32 v175, v175\n"
      "v_mfma_f32_16x16x32_bf16 v[32:35], a[96:99], v[136:139], v[32:35]\n"
      "s_add_u32 m0, s58, 0x2000\n"
      "v_cvt_pk_bf16_f32 v171, v174, v175\n"
      "v_accvgpr_write_b32 a20, v228\n"
      "v_mfma_f32_16x16x32_bf16 v[36:39], a[100:103], v[136:139], v[36:39]\n"
      "s_add_u32 s68, s57, 0x12000\n"
      "v_exp_f32 v176, v176\n"
      "v_mfma_f32_16x16x32_bf16 v[40:43], a[104:107], v[136:139], v[40:43]\n"
      "buffer_load_dwordx4 v212, s[44:47], s68 offen nt lds\n"
      "v_lshlrev_b32 v222, 16, v229\n"
      "v_mfma_f32_16x16x32_bf16 v[44:47], a[108:111], v[136:139], v[44:47]\n"
      "v_exp_f32 v177, v177\n"
      "v_mfma_f32_16x16x32_bf16 v[48:51], a[112:115], v[136:139], v[48:51]\n"
      "buffer_load_dwordx4 v213, s[44:47], s68 offen offset:1024 nt lds\n"
      "v_cvt_pk_bf16_f32 v176, v176, v177\n"
      "v_mfma_f32_16x16x32_bf16 v[52:55], a[116:119], v[136:139], v[52:55]\n"
      "v_exp_f32 v178, v178\n"
      "v_mfma_f32_16x16x32_bf16 v[56:59], a[120:123], v[136:139], v[56:59]\n"
      "v_and_b32 v229, s62, v229\n"
      "v_mul_f32 v222, s61, v222\n"
      "v_mfma_f32_16x16x32_bf16 v[60:63], a[124:127], v[136:139], v[60:63]\n"
      "v_exp_f32 v179, v179\n"
      "v_mfma_f32_16x16x32_bf16 a[136:139], a[144:147], v[144:147], a[136:139]\n"
      "v_cvt_pk_bf16_f32 v177, v178, v179\n"
      "v_mul_f32 v229, s61, v229\n"
      "v_mfma_f32_16x16x32_bf16 v[64:67], a[96:99], v[144:147], v[64:67]\n"
      "v_exp_f32 v180, v180\n"
      "v_mfma_f32_16x16x32_bf16 v[68:71], a[100:103], v[144:147], v[68:71]\n"
      "v_cvt_pk_bf16_f32 v229, v222, v229\n"
      "v_accvgpr_write_b32 a21, v229\n"
      "v_mfma_f32_16x16x32_bf16 v[72:75], a[104:107], v[144:147], v[72:75]\n"
      "v_exp_f32 v181, v181\n"
      "v_mfma_f32_16x16x32_bf16 v[76:79], a[108:111], v[144:147], v[76:79]\n"
      "v_cvt_pk_bf16_f32 v178, v180, v181\n"
      "v_lshlrev_b32 v221, 16, v230\n"
      "v_mfma_f32_16x16x32_bf16 v[80:83], a[112:115], v[144:147], v[80:83]\n"
      "v_exp_f32 v182, v182\n"
      "v_mfma_f32_16x16x32_bf16 v[84:87], a[116:119], v[144:147], v[84:87]\n"
      "s_add_u32 m0, s58, 0xe000\n"
      "ds_read_b128 v[224:227], v209 offset:5120\n"
      "v_and_b32 v230, s62, v230\n"
      "v_mfma_f32_16x16x32_bf16 v[88:91], a[120:123], v[144:147], v[88:91]\n"
      "s_add_u32 s67, s57, 0xe000\n"
      "v_exp_f32 v183, v183\n"
      "v_mfma_f32_16x16x32_bf16 v[92:95], a[124:127], v[144:147], v[92:95]\n"
      "buffer_load_dwordx4 v212, s[48:51], s67 offen nt lds\n"
      "v_cvt_pk_bf16_f32 v179, v182, v183\n"
      "v_mfma_f32_16x16x32_bf16 a[140:143], a[144:147], v[152:155], a[140:143]\n"
      "v_exp_f32 v184, v184\n"
      "v_mfma_f32_16x16x32_bf16 v[96:99], a[96:99], v[152:155], v[96:99]\n"
      "buffer_load_dwordx4 v213, s[48:51], s67 offen offset:1024 nt lds\n"
      "v_mul_f32 v221, s61, v221\n"
      "v_mfma_f32_16x16x32_bf16 v[100:103], a[100:103], v[152:155], v[100:103]\n"
      "v_exp_f32 v185, v185\n"
      "v_mfma_f32_16x16x32_bf16 v[104:107], a[104:107], v[152:155], v[104:107]\n"
      "v_cvt_pk_bf16_f32 v184, v184, v185\n"
      "v_mul_f32 v230, s61, v230\n"
      "v_mfma_f32_16x16x32_bf16 v[108:111], a[108:111], v[152:155], v[108:111]\n"
      "v_exp_f32 v186, v186\n"
      "v_mfma_f32_16x16x32_bf16 v[112:115], a[112:115], v[152:155], v[112:115]\n"
      "v_cvt_pk_bf16_f32 v230, v221, v230\n"
      "v_accvgpr_write_b32 a22, v230\n"
      "v_mfma_f32_16x16x32_bf16 v[116:119], a[116:119], v[152:155], v[116:119]\n"
      "v_lshlrev_b32 v222, 16, v231\n"
      "v_and_b32 v231, s62, v231\n"
      "v_mfma_f32_16x16x32_bf16 v[120:123], a[120:123], v[152:155], v[120:123]\n"
      "v_mul_f32 v222, s61, v222\n"
      "v_mul_f32 v231, s61, v231\n"
      "v_mfma_f32_16x16x32_bf16 v[124:127], a[124:127], v[152:155], v[124:127]\n"
      "v_cvt_pk_bf16_f32 v231, v222, v231\n"
      "v_accvgpr_write_b32 a23, v231\n"
      "s_waitcnt lgkmcnt(0)\n"
      "v_mfma_f32_16x16x32_bf16 v[128:131], a[64:67], a[160:163], v[192:195]\n"
      "s_waitcnt vmcnt(14)\n"
      "s_barrier\n"
      "v_lshlrev_b32 v221, 16, v224\n"
      "v_and_b32 v224, s62, v224\n"
      "v_mfma_f32_16x16x32_bf16 v[128:131], a[68:71], a[164:167], v[128:131]\n"
      "v_exp_f32 v187, v187\n"
      "v_mfma_f32_16x16x32_bf16 v[128:131], a[72:75], a[168:171], v[128:131]\n"
      "ds_read_b128 a[152:155], v220 offset:13312\n"
      "v_cvt_pk_bf16_f32 v185, v186, v187\n"
      "v_mfma_f32_16x16x32_bf16 v[128:131], a[76:79], a[172:175], v[128:131]\n"
      "v_exp_f32 v188, v188\n"
      "v_mfma_f32_16x16x32_bf16 v[132:135], a[80:83], a[160:163], v[192:195]\n"
      "ds_read_b64_tr_b16 a[96:97], v210 offset:40960\n"
      "ds_read_b64_tr_b16 a[98:99], v210 offset:45056\n"
      "v_mfma_f32_16x16x32_bf16 v[132:135], a[84:87], a[164:167], v[132:135]\n"
      "v_exp_f32 v189, v189\n"
      "v_mfma_f32_16x16x32_bf16 v[132:135], a[88:91], a[168:171], v[132:135]\n"
      "ds_read_b64_tr_b16 a[100:101], v211 offset:40960\n"
      "v_cvt_pk_bf16_f32 v186, v188, v189\n"
      "v_mfma_f32_16x16x32_bf16 v[132:135], a[92:95], a[172:175], v[132:135]\n"
      "v_exp_f32 v190, v190\n"
      "v_mfma_f32_16x16x32_bf16 v[136:139], a[64:67], a[176:179], v[196:199]\n"
      "ds_read_b64_tr_b16 a[102:103], v211 offset:45056\n"
      "ds_read_b64_tr_b16 a[104:105], v210 offset:41472\n"
      "v_mfma_f32_16x16x32_bf16 v[136:139], a[68:71], a[180:183], v[136:139]\n"
      "v_exp_f32 v191, v191\n"
      "v_mfma_f32_16x16x32_bf16 v[136:139], a[72:75], a[184:187], v[136:139]\n"
      "ds_read_b64_tr_b16 a[106:107], v210 offset:45568\n"
      "v_cvt_pk_bf16_f32 v187, v190, v191\n"
      "v_mfma_f32_16x16x32_bf16 v[136:139], a[76:79], a[188:191], v[136:139]\n"
      "v_exp_f32 v128, v128\n"
      "v_mfma_f32_16x16x32_bf16 v[140:143], a[80:83], a[176:179], v[196:199]\n"
      "ds_read_b64_tr_b16 a[108:109], v211 offset:41472\n"
      "ds_read_b64_tr_b16 a[110:111], v211 offset:45568\n"
      "v_mfma_f32_16x16x32_bf16 v[140:143], a[84:87], a[180:183], v[140:143]\n"
      "v_exp_f32 v129, v129\n"
      "v_mfma_f32_16x16x32_bf16 v[140:143], a[88:91], a[184:187], v[140:143]\n"
      "ds_read_b64_tr_b16 a[112:113], v210 offset:41984\n"
      "v_cvt_pk_bf16_f32 v128, v128, v129\n"
      "v_mfma_f32_16x16x32_bf16 v[140:143], a[92:95], a[188:191], v[140:143]\n"
      "v_exp_f32 v130, v130\n"
      "v_mfma_f32_16x16x32_bf16 v[144:147], a[64:67], a[192:195], v[200:203]\n"
      "ds_read_b64_tr_b16 a[114:115], v210 offset:46080\n"
      "ds_read_b64_tr_b16 a[116:117], v211 offset:41984\n"
      "v_mfma_f32_16x16x32_bf16 v[144:147], a[68:71], a[196:199], v[144:147]\n"
      "v_exp_f32 v131, v131\n"
      "v_mfma_f32_16x16x32_bf16 v[144:147], a[72:75], a[200:203], v[144:147]\n"
      "ds_read_b64_tr_b16 a[118:119], v211 offset:46080\n"
      "v_cvt_pk_bf16_f32 v129, v130, v131\n"
      "v_mfma_f32_16x16x32_bf16 v[144:147], a[76:79], a[204:207], v[144:147]\n"
      "v_exp_f32 v132, v132\n"
      "v_mfma_f32_16x16x32_bf16 v[148:151], a[80:83], a[192:195], v[200:203]\n"
      "ds_read_b64_tr_b16 a[120:121], v210 offset:42496\n"
      "ds_read_b64_tr_b16 a[122:123], v210 offset:46592\n"
      "v_mfma_f32_16x16x32_bf16 v[148:151], a[84:87], a[196:199], v[148:151]\n"
      "v_exp_f32 v133, v133\n"
      "v_mfma_f32_16x16x32_bf16 v[148:151], a[88:91], a[200:203], v[148:151]\n"
      "ds_read_b64_tr_b16 a[124:125], v211 offset:42496\n"
      "v_cvt_pk_bf16_f32 v130, v132, v133\n"
      "v_mfma_f32_16x16x32_bf16 v[148:151], a[92:95], a[204:207], v[148:151]\n"
      "v_exp_f32 v134, v134\n"
      "v_mfma_f32_16x16x32_bf16 v[152:155], a[64:67], a[208:211], v[204:207]\n"
      "ds_read_b64_tr_b16 a[126:127], v211 offset:46592\n"
      "v_mul_f32 v221, s61, v221\n"
      "v_mfma_f32_16x16x32_bf16 v[152:155], a[68:71], a[212:215], v[152:155]\n"
      "v_exp_f32 v135, v135\n"
      "v_mfma_f32_16x16x32_bf16 v[152:155], a[72:75], a[216:219], v[152:155]\n"
      "v_cvt_pk_bf16_f32 v131, v134, v135\n"
      "v_mul_f32 v224, s61, v224\n"
      "v_mfma_f32_16x16x32_bf16 v[152:155], a[76:79], a[220:223], v[152:155]\n"
      "v_exp_f32 v136, v136\n"
      "v_mfma_f32_16x16x32_bf16 v[156:159], a[80:83], a[208:211], v[204:207]\n"
      "ds_read_b128 a[64:67], v208 offset:24576\n"
      "ds_read_b128 a[68:71], v208 offset:25088\n"
      "v_mfma_f32_16x16x32_bf16 v[156:159], a[84:87], a[212:215], v[156:159]\n"
      "v_exp_f32 v137, v137\n"
      "v_mfma_f32_16x16x32_bf16 v[156:159], a[88:91], a[216:219], v[156:159]\n"
      "ds_read_b128 a[72:75], v208 offset:25600\n"
      "v_cvt_pk_bf16_f32 v136, v136, v137\n"
      "v_mfma_f32_16x16x32_bf16 v[156:159], a[92:95], a[220:223], v[156:159]\n"
      "v_exp_f32 v138, v138\n"
      "v_mfma_f32_16x16x32_bf16 a[128:131], a[144:147], v[160:163], a[128:131]\n"
      "ds_read_b128 a[76:79], v208 offset:26112\n"
      "v_cvt_pk_bf16_f32 v224, v221, v224\n"
      "s_waitcnt lgkmcnt(12)\n"
      "v_mfma_f32_16x16x32_bf16 v[0:3], a[96:99], v[160:163], v[0:3]\n"
      "v_exp_f32 v139, v139\n"
      "v_mfma_f32_16x16x32_bf16 v[4:7], a[100:103], v[160:163], v[4:7]\n"
      "ds_read_b128 a[80:83], v208 offset:28672\n"
      "v_cvt_pk_bf16_f32 v137, v138, v139\n"
      "v_mfma_f32_16x16x32_bf16 v[8:11], a[104:107], v[160:163], v[8:11]\n"
      "v_exp_f32 v140, v140\n"
      "v_mfma_f32_16x16x32_bf16 v[12:15], a[108:111], v[160:163], v[12:15]\n"
      "ds_read_b128 a[84:87], v208 offset:29184\n"
      "ds_read_b128 a[88:91], v208 offset:29696\n"
      "s_waitcnt lgkmcnt(7)\n"
      "v_mfma_f32_16x16x32_bf16 v[16:19], a[112:115], v[160:163], v[16:19]\n"
      "v_exp_f32 v141, v141\n"
      "v_mfma_f32_16x16x32_bf16 v[20:23], a[116:119], v[160:163], v[20:23]\n"
      "ds_read_b128 a[92:95], v208 offset:30208\n"
      "v_cvt_pk_bf16_f32 v138, v140, v141\n"
      "v_mfma_f32_16x16x32_bf16 v[24:27], a[120:123], v[160:163], v[24:27]\n"
      "v_exp_f32 v142, v142\n"
      "v_mfma_f32_16x16x32_bf16 v[28:31], a[124:127], v[160:163], v[28:31]\n"
      "buffer_store_dwordx4 a[152:155], v214, s[52:55], s77 offen offset:1024 sc1\n"
      "v_mfma_f32_16x16x32_bf16 a[132:135], a[144:147], v[168:171], a[132:135]\n"
      "v_exp_f32 v143, v143\n"
      "v_mfma_f32_16x16x32_bf16 v[32:35], a[96:99], v[168:171], v[32:35]\n"
      "s_add_u32 m0, s58, 0x4000\n"
      "v_cvt_pk_bf16_f32 v139, v142, v143\n"
      "v_accvgpr_write_b32 a24, v224\n"
      "v_mfma_f32_16x16x32_bf16 v[36:39], a[100:103], v[168:171], v[36:39]\n"
      "s_add_u32 s68, s57, 0x14000\n"
      "v_exp_f32 v144, v144\n"
      "v_mfma_f32_16x16x32_bf16 v[40:43], a[104:107], v[168:171], v[40:43]\n"
      "buffer_load_dwordx4 v212, s[44:47], s68 offen nt lds\n"
      "v_lshlrev_b32 v222, 16, v225\n"
      "v_mfma_f32_16x16x32_bf16 v[44:47], a[108:111], v[168:171], v[44:47]\n"
      "v_exp_f32 v145, v145\n"
      "v_mfma_f32_16x16x32_bf16 v[48:51], a[112:115], v[168:171], v[48:51]\n"
      "buffer_load_dwordx4 v213, s[44:47], s68 offen offset:1024 nt lds\n"
      "v_cvt_pk_bf16_f32 v144, v144, v145\n"
      "v_mfma_f32_16x16x32_bf16 v[52:55], a[116:119], v[168:171], v[52:55]\n"
      "v_exp_f32 v146, v146\n"
      "v_mfma_f32_16x16x32_bf16 v[56:59], a[120:123], v[168:171], v[56:59]\n"
      "v_and_b32 v225, s62, v225\n"
      "v_mul_f32 v222, s61, v222\n"
      "v_mfma_f32_16x16x32_bf16 v[60:63], a[124:127], v[168:171], v[60:63]\n"
      "v_exp_f32 v147, v147\n"
      "v_mfma_f32_16x16x32_bf16 a[136:139], a[144:147], v[176:179], a[136:139]\n"
      "v_cvt_pk_bf16_f32 v145, v146, v147\n"
      "v_mul_f32 v225, s61, v225\n"
      "v_mfma_f32_16x16x32_bf16 v[64:67], a[96:99], v[176:179], v[64:67]\n"
      "v_exp_f32 v148, v148\n"
      "v_mfma_f32_16x16x32_bf16 v[68:71], a[100:103], v[176:179], v[68:71]\n"
      "v_cvt_pk_bf16_f32 v225, v222, v225\n"
      "v_accvgpr_write_b32 a25, v225\n"
      "v_mfma_f32_16x16x32_bf16 v[72:75], a[104:107], v[176:179], v[72:75]\n"
      "v_exp_f32 v149, v149\n"
      "v_mfma_f32_16x16x32_bf16 v[76:79], a[108:111], v[176:179], v[76:79]\n"
      "v_cvt_pk_bf16_f32 v146, v148, v149\n"
      "v_lshlrev_b32 v221, 16, v226\n"
      "v_mfma_f32_16x16x32_bf16 v[80:83], a[112:115], v[176:179], v[80:83]\n"
      "v_exp_f32 v150, v150\n"
      "v_mfma_f32_16x16x32_bf16 v[84:87], a[116:119], v[176:179], v[84:87]\n"
      "s_add_u32 m0, s58, 0x8000\n"
      "ds_read_b128 v[228:231], v209 offset:5632\n"
      "v_and_b32 v226, s62, v226\n"
      "v_mfma_f32_16x16x32_bf16 v[88:91], a[120:123], v[176:179], v[88:91]\n"
      "s_add_u32 s67, s57, 0x10000\n"
      "v_exp_f32 v151, v151\n"
      "v_mfma_f32_16x16x32_bf16 v[92:95], a[124:127], v[176:179], v[92:95]\n"
      "buffer_load_dwordx4 v212, s[48:51], s67 offen nt lds\n"
      "v_cvt_pk_bf16_f32 v147, v150, v151\n"
      "v_mfma_f32_16x16x32_bf16 a[140:143], a[144:147], v[184:187], a[140:143]\n"
      "v_exp_f32 v152, v152\n"
      "v_mfma_f32_16x16x32_bf16 v[96:99], a[96:99], v[184:187], v[96:99]\n"
      "buffer_load_dwordx4 v213, s[48:51], s67 offen offset:1024 nt lds\n"
      "v_mul_f32 v221, s61, v221\n"
      "v_mfma_f32_16x16x32_bf16 v[100:103], a[100:103], v[184:187], v[100:103]\n"
      "v_exp_f32 v153, v153\n"
      "v_mfma_f32_16x16x32_bf16 v[104:107], a[104:107], v[184:187], v[104:107]\n"
      "v_cvt_pk_bf16_f32 v152, v152, v153\n"
      "v_mul_f32 v226, s61, v226\n"
      "v_mfma_f32_16x16x32_bf16 v[108:111], a[108:111], v[184:187], v[108:111]\n"
      "v_exp_f32 v154, v154\n"
      "v_mfma_f32_16x16x32_bf16 v[112:115], a[112:115], v[184:187], v[112:115]\n"
      "v_cvt_pk_bf16_f32 v226, v221, v226\n"
      "v_accvgpr_write_b32 a26, v226\n"
      "v_mfma_f32_16x16x32_bf16 v[116:119], a[116:119], v[184:187], v[116:119]\n"
      "v_lshlrev_b32 v222, 16, v227\n"
      "v_and_b32 v227, s62, v227\n"
      "v_mfma_f32_16x16x32_bf16 v[120:123], a[120:123], v[184:187], v[120:123]\n"
      "s_memrealtime s[84:85]\n"
      "v_mul_f32 v222, s61, v222\n"
      "v_mul_f32 v227, s61, v227\n"
      "v_mfma_f32_16x16x32_bf16 v[124:127], a[124:127], v[184:187], v[124:127]\n"
      "v_cvt_pk_bf16_f32 v227, v222, v227\n"
      "v_accvgpr_write_b32 a27, v227\n"
      "s_waitcnt lgkmcnt(0)\n"
      "s_sub_u32 s85, s84, s86\n"
      "s_cmp_ge_u32 s85, s87\n"
      "s_cbranch_scc1 L_PACED1_6\n"
      "L_PACE1_6:\n"
      "s_memrealtime s[84:85]\n"
      "s_waitcnt lgkmcnt(0)\n"
      "s_sub_u32 s85, s84, s86\n"
      "s_cmp_lt_u32 s85, s87\n"
      "s_cbranch_scc1 L_PACE1_6\n"
      "L_PACED1_6:\n"
      "s_mov_b32 s86, s84\n"
      "v_mfma_f32_16x16x32_bf16 v[160:163], a[64:67], a[160:163], v[192:195]\n"
      "s_waitcnt vmcnt(10)\n"
      "s_barrier\n"
      "v_lshlrev_b32 v221, 16, v228\n"
      "v_and_b32 v228, s62, v228\n"
      "v_mul_f32 v221, s61, v221\n"
      "v_mfma_f32_16x16x32_bf16 v[160:163], a[68:71], a[164:167], v[160:163]\n"
      "v_exp_f32 v155, v155\n"
      "v_mfma_f32_16x16x32_bf16 v[160:163], a[72:75], a[168:171], v[160:163]\n"
      "ds_read_b128 a[148:151], v219 offset:14336\n"
      "v_cvt_pk_bf16_f32 v153, v154, v155\n"
      "v_mfma_f32_16x16x32_bf16 v[160:163], a[76:79], a[172:175], v[160:163]\n"
      "v_exp_f32 v156, v156\n"
      "v_mfma_f32_16x16x32_bf16 v[164:167], a[80:83], a[160:163], v[192:195]\n"
      "ds_read_b64_tr_b16 a[96:97], v210 offset:49152\n"
      "ds_read_b64_tr_b16 a[98:99], v210 offset:53248\n"
      "v_mfma_f32_16x16x32_bf16 v[164:167], a[84:87], a[164:167], v[164:167]\n"
      "v_exp_f32 v157, v157\n"
      "v_mfma_f32_16x16x32_bf16 v[164:167], a[88:91], a[168:171], v[164:167]\n"
      "ds_read_b64_tr_b16 a[100:101], v211 offset:49152\n"
      "v_cvt_pk_bf16_f32 v154, v156, v157\n"
      "v_mfma_f32_16x16x32_bf16 v[164:167], a[92:95], a[172:175], v[164:167]\n"
      "v_exp_f32 v158, v158\n"
      "v_mfma_f32_16x16x32_bf16 v[168:171], a[64:67], a[176:179], v[196:199]\n"
      "ds_read_b64_tr_b16 a[102:103], v211 offset:53248\n"
      "ds_read_b64_tr_b16 a[104:105], v210 offset:49664\n"
      "v_mfma_f32_16x16x32_bf16 v[168:171], a[68:71], a[180:183], v[168:171]\n"
      "v_exp_f32 v159, v159\n"
      "v_mfma_f32_16x16x32_bf16 v[168:171], a[72:75], a[184:187], v[168:171]\n"
      "ds_read_b64_tr_b16 a[106:107], v210 offset:53760\n"
      "v_cvt_pk_bf16_f32 v155, v158, v159\n"
      "v_mfma_f32_16x16x32_bf16 v[168:171], a[76:79], a[188:191], v[168:171]\n"
      "v_exp_f32 v160, v160\n"
      "v_mfma_f32_16x16x32_bf16 v[172:175], a[80:83], a[176:179], v[196:199]\n"
      "ds_read_b64_tr_b16 a[108:109], v211 offset:49664\n"
      "ds_read_b64_tr_b16 a[110:111], v211 offset:53760\n"
      "v_mfma_f32_16x16x32_bf16 v[172:175], a[84:87], a[180:183], v[172:175]\n"
      "v_exp_f32 v161, v161\n"
      "v_mfma_f32_16x16x32_bf16 v[172:175], a[88:91], a[184:187], v[172:175]\n"
      "ds_read_b64_tr_b16 a[112:113], v210 offset:50176\n"
      "v_cvt_pk_bf16_f32 v160, v160, v161\n"
      "v_mfma_f32_16x16x32_bf16 v[172:175], a[92:95], a[188:191], v[172:175]\n"
      "v_exp_f32 v162, v162\n"
      "v_mfma_f32_16x16x32_bf16 v[176:179], a[64:67], a[192:195], v[200:203]\n"
      "ds_read_b64_tr_b16 a[114:115], v210 offset:54272\n"
      "ds_read_b64_tr_b16 a[116:117], v211 offset:50176\n"
      "v_mfma_f32_16x16x32_bf16 v[176:179], a[68:71], a[196:199], v[176:179]\n"
      "v_exp_f32 v163, v163\n"
      "v_mfma_f32_16x16x32_bf16 v[176:179], a[72:75], a[200:203], v[176:179]\n"
      "ds_read_b64_tr_b16 a[118:119], v211 offset:54272\n"
      "v_cvt_pk_bf16_f32 v161, v162, v163\n"
      "v_mfma_f32_16x16x32_bf16 v[176:179], a[76:79], a[204:207], v[176:179]\n"
      "v_exp_f32 v164, v164\n"
      "v_mfma_f32_16x16x32_bf16 v[180:183], a[80:83], a[192:195], v[200:203]\n"
      "ds_read_b64_tr_b16 a[120:121], v210 offset:50688\n"
      "ds_read_b64_tr_b16 a[122:123], v210 offset:54784\n"
      "v_mfma_f32_16x16x32_bf16 v[180:183], a[84:87], a[196:199], v[180:183]\n"
      "v_exp_f32 v165, v165\n"
      "v_mfma_f32_16x16x32_bf16 v[180:183], a[88:91], a[200:203], v[180:183]\n"
      "ds_read_b64_tr_b16 a[124:125], v211 offset:50688\n"
      "v_cvt_pk_bf16_f32 v162, v164, v165\n"
      "v_mfma_f32_16x16x32_bf16 v[180:183], a[92:95], a[204:207], v[180:183]\n"
      "v_exp_f32 v166, v166\n"
      "v_mfma_f32_16x16x32_bf16 v[184:187], a[64:67], a[208:211], v[204:207]\n"
      "ds_read_b64_tr_b16 a[126:127], v211 offset:54784\n"
      "v_mul_f32 v228, s61, v228\n"
      "v_mfma_f32_16x16x32_bf16 v[184:187], a[68:71], a[212:215], v[184:187]\n"
      "v_exp_f32 v167, v167\n"
      "v_mfma_f32_16x16x32_bf16 v[184:187], a[72:75], a[216:219], v[184:187]\n"
      "v_cvt_pk_bf16_f32 v163, v166, v167\n"
      "v_cvt_pk_bf16_f32 v228, v221, v228\n"
      "v_mfma_f32_16x16x32_bf16 v[184:187], a[76:79], a[220:223], v[184:187]\n"
      "v_exp_f32 v168, v168\n"
      "v_mfma_f32_16x16x32_bf16 v[188:191], a[80:83], a[208:211], v[204:207]\n"
      "ds_read_b128 a[64:67], v208 offset:0\n"
      "ds_read_b128 a[68:71], v208 offset:512\n"
      "v_mfma_f32_16x16x32_bf16 v[188:191], a[84:87], a[212:215], v[188:191]\n"
      "v_exp_f32 v169, v169\n"
      "v_mfma_f32_16x16x32_bf16 v[188:191], a[88:91], a[216:219], v[188:191]\n"
      "ds_read_b128 a[72:75], v208 offset:1024\n"
      "v_cvt_pk_bf16_f32 v168, v168, v169\n"
      "v_mfma_f32_16x16x32_bf16 v[188:191], a[92:95], a[220:223], v[188:191]\n"
      "v_exp_f32 v170, v170\n"
      "v_mfma_f32_16x16x32_bf16 a[128:131], a[144:147], v[128:131], a[128:131]\n"
      "ds_read_b128 a[76:79], v208 offset:1536\n"
      "v_accvgpr_write_b32 a28, v228\n"
      "s_waitcnt lgkmcnt(12)\n"
      "v_mfma_f32_16x16x32_bf16 v[0:3], a[96:99], v[128:131], v[0:3]\n"
      "v_exp_f32 v171, v171\n"
      "v_mfma_f32_16x16x32_bf16 v[4:7], a[100:103], v[128:131], v[4:7]\n"
      "ds_read_b128 a[80:83], v208 offset:4096\n"
      "v_cvt_pk_bf16_f32 v169, v170, v171\n"
      "v_mfma_f32_16x16x32_bf16 v[8:11], a[104:107], v[128:131], v[8:11]\n"
      "v_exp_f32 v172, v172\n"
      "v_mfma_f32_16x16x32_bf16 v[12:15], a[108:111], v[128:131], v[12:15]\n"
      "ds_read_b128 a[84:87], v208 offset:4608\n"
      "ds_read_b128 a[88:91], v208 offset:5120\n"
      "v_lshlrev_b32 v222, 16, v229\n"
      "s_waitcnt lgkmcnt(7)\n"
      "v_mfma_f32_16x16x32_bf16 v[16:19], a[112:115], v[128:131], v[16:19]\n"
      "v_exp_f32 v173, v173\n"
      "v_mfma_f32_16x16x32_bf16 v[20:23], a[116:119], v[128:131], v[20:23]\n"
      "ds_read_b128 a[92:95], v208 offset:5632\n"
      "v_cvt_pk_bf16_f32 v170, v172, v173\n"
      "v_mfma_f32_16x16x32_bf16 v[24:27], a[120:123], v[128:131], v[24:27]\n"
      "v_exp_f32 v174, v174\n"
      "v_mfma_f32_16x16x32_bf16 v[28:31], a[124:127], v[128:131], v[28:31]\n"
      "s_add_u32 m0, s58, 0x6000\n"
      "buffer_store_dwordx4 a[148:151], v214, s[52:55], s77 offen offset:2048 sc1\n"
      "v_mfma_f32_16x16x32_bf16 a[132:135], a[144:147], v[136:139], a[132:135]\n"
      "s_add_u32 s67, s57, 0x16000\n"
      "v_exp_f32 v175, v175\n"
      "v_mfma_f32_16x16x32_bf16 v[32:35], a[96:99], v[136:139], v[32:35]\n"
      "buffer_load_dwordx4 v212, s[44:47], s67 offen nt lds\n"
      "v_cvt_pk_bf16_f32 v171, v174, v175\n"
      "v_mfma_f32_16x16x32_bf16 v[36:39], a[100:103], v[136:139], v[36:39]\n"
      "v_exp_f32 v176, v176\n"
      "v_mfma_f32_16x16x32_bf16 v[40:43], a[104:107], v[136:139], v[40:43]\n"
      "buffer_load_dwordx4 v213, s[44:47], s67 offen offset:1024 nt lds\n"
      "v_and_b32 v229, s62, v229\n"
      "v_mfma_f32_16x16x32_bf16 v[44:47], a[108:111], v[136:139], v[44:47]\n"
      "v_exp_f32 v177, v177\n"
      "v_mfma_f32_16x16x32_bf16 v[48:51], a[112:115], v[136:139], v[48:51]\n"
      "v_cvt_pk_bf16_f32 v176, v176, v177\n"
      "v_mul_f32 v222, s61, v222\n"
      "v_mfma_f32_16x16x32_bf16 v[52:55], a[116:119], v[136:139], v[52:55]\n"
      "v_exp_f32 v178, v178\n"
      "v_mfma_f32_16x16x32_bf16 v[56:59], a[120:123], v[136:139], v[56:59]\n"
      "v_mul_f32 v229, s61, v229\n"
      "v_cvt_pk_bf16_f32 v229, v222, v229\n"
      "v_mfma_f32_16x16x32_bf16 v[60:63], a[124:127], v[136:139], v[60:63]\n"
      "v_exp_f32 v179, v179\n"
      "v_mfma_f32_16x16x32_bf16 a[136:139], a[144:147], v[144:147], a[136:139]\n"
      "s_add_u32 m0, s58, 0xa000\n"
      "v_cvt_pk_bf16_f32 v177, v178, v179\n"
      "v_accvgpr_write_b32 a29, v229\n"
      "v_mfma_f32_16x16x32_bf16 v[64:67], a[96:99], v[144:147], v[64:67]\n"
      "s_add_u32 s68, s57, 0x12000\n"
      "v_exp_f32 v180, v180\n"
      "v_mfma_f32_16x16x32_bf16 v[68:71], a[100:103], v[144:147], v[68:71]\n"
      "buffer_load_dwordx4 v212, s[48:51], s68 offen nt lds\n"
      "v_lshlrev_b32 v221, 16, v230\n"
      "v_mfma_f32_16x16x32_bf16 v[72:75], a[104:107], v[144:147], v[72:75]\n"
      "v_exp_f32 v181, v181\n"
      "v_mfma_f32_16x16x32_bf16 v[76:79], a[108:111], v[144:147], v[76:79]\n"
      "buffer_load_dwordx4 v213, s[48:51], s68 offen offset:1024 nt lds\n"
      "v_cvt_pk_bf16_f32 v178, v180, v181\n"
      "v_mfma_f32_16x16x32_bf16 v[80:83], a[112:115], v[144:147], v[80:83]\n"
      "v_exp_f32 v182, v182\n"
      "v_mfma_f32_16x16x32_bf16 v[84:87], a[116:119], v[144:147], v[84:87]\n"
      "ds_read_b128 v[224:227], v209 offset:0\n"
      "v_and_b32 v230, s62, v230\n"
      "v_mfma_f32_16x16x32_bf16 v[88:91], a[120:123], v[144:147], v[88:91]\n"
      "v_exp_f32 v183, v183\n"
      "v_mfma_f32_16x16x32_bf16 v[92:95], a[124:127], v[144:147], v[92:95]\n"
      "s_add_u32 m0, s59, 0x1000\n"
      "v_cvt_pk_bf16_f32 v179, v182, v183\n"
      "v_mul_f32 v221, s61, v221\n"
      "v_mfma_f32_16x16x32_bf16 a[140:143], a[144:147], v[152:155], a[140:143]\n"
      "s_add_u32 s68, s56, 0x1003000\n"
      "v_exp_f32 v184, v184\n"
      "v_mfma_f32_16x16x32_bf16 v[96:99], a[96:99], v[152:155], v[96:99]\n"
      "buffer_load_dwordx4 v212, s[40:43], s68 offen nt lds\n"
      "v_mul_f32 v230, s61, v230\n"
      "v_mfma_f32_16x16x32_bf16 v[100:103], a[100:103], v[152:155], v[100:103]\n"
      "v_exp_f32 v185, v185\n"
      "v_mfma_f32_16x16x32_bf16 v[104:107], a[104:107], v[152:155], v[104:107]\n"
      "buffer_load_dwordx4 v213, s[40:43], s68 offen offset:1024 nt lds\n"
      "v_cvt_pk_bf16_f32 v184, v184, v185\n"
      "v_mfma_f32_16x16x32_bf16 v[108:111], a[108:111], v[152:155], v[108:111]\n"
      "v_exp_f32 v186, v186\n"
      "v_mfma_f32_16x16x32_bf16 v[112:115], a[112:115], v[152:155], v[112:115]\n"
      "v_cvt_pk_bf16_f32 v230, v221, v230\n"
      "v_accvgpr_write_b32 a30, v230\n"
      "v_mfma_f32_16x16x32_bf16 v[116:119], a[116:119], v[152:155], v[116:119]\n"
      "v_lshlrev_b32 v222, 16, v231\n"
      "v_and_b32 v231, s62, v231\n"
      "v_mfma_f32_16x16x32_bf16 v[120:123], a[120:123], v[152:155], v[120:123]\n"
      "v_mul_f32 v222, s61, v222\n"
      "v_mul_f32 v231, s61, v231\n"
      "v_mfma_f32_16x16x32_bf16 v[124:127], a[124:127], v[152:155], v[124:127]\n"
      "v_cvt_pk_bf16_f32 v231, v222, v231\n"
      "v_accvgpr_write_b32 a31, v231\n"
      "s_waitcnt lgkmcnt(0)\n"
      "v_mfma_f32_16x16x32_bf16 v[128:131], a[64:67], a[160:163], v[192:195]\n"
      "s_waitcnt vmcnt(12)\n"
      "s_barrier\n"
      "v_lshlrev_b32 v221, 16, v224\n"
      "v_and_b32 v224, s62, v224\n"
      "v_mul_f32 v221, s61, v221\n"
      "v_mfma_f32_16x16x32_bf16 v[128:131], a[68:71], a[164:167], v[128:131]\n"
      "v_exp_f32 v187, v187\n"
      "v_mfma_f32_16x16x32_bf16 v[128:131], a[72:75], a[168:171], v[128:131]\n"
      "ds_read_b128 a[152:155], v220 offset:15360\n"
      "v_cvt_pk_bf16_f32 v185, v186, v187\n"
      "v_mfma_f32_16x16x32_bf16 v[128:131], a[76:79], a[172:175], v[128:131]\n"
      "v_exp_f32 v188, v188\n"
      "v_mfma_f32_16x16x32_bf16 v[132:135], a[80:83], a[160:163], v[192:195]\n"
      "ds_read_b64_tr_b16 a[96:97], v210 offset:57344\n"
      "ds_read_b64_tr_b16 a[98:99], v210 offset:61440\n"
      "v_mfma_f32_16x16x32_bf16 v[132:135], a[84:87], a[164:167], v[132:135]\n"
      "v_exp_f32 v189, v189\n"
      "v_mfma_f32_16x16x32_bf16 v[132:135], a[88:91], a[168:171], v[132:135]\n"
      "ds_read_b64_tr_b16 a[100:101], v211 offset:57344\n"
      "v_cvt_pk_bf16_f32 v186, v188, v189\n"
      "v_mfma_f32_16x16x32_bf16 v[132:135], a[92:95], a[172:175], v[132:135]\n"
      "v_exp_f32 v190, v190\n"
      "v_mfma_f32_16x16x32_bf16 v[136:139], a[64:67], a[176:179], v[196:199]\n"
      "ds_read_b64_tr_b16 a[102:103], v211 offset:61440\n"
      "ds_read_b64_tr_b16 a[104:105], v210 offset:57856\n"
      "v_mfma_f32_16x16x32_bf16 v[136:139], a[68:71], a[180:183], v[136:139]\n"
      "v_exp_f32 v191, v191\n"
      "v_mfma_f32_16x16x32_bf16 v[136:139], a[72:75], a[184:187], v[136:139]\n"
      "ds_read_b64_tr_b16 a[106:107], v210 offset:61952\n"
      "v_cvt_pk_bf16_f32 v187, v190, v191\n"
      "v_mfma_f32_16x16x32_bf16 v[136:139], a[76:79], a[188:191], v[136:139]\n"
      "v_exp_f32 v128, v128\n"
      "v_mfma_f32_16x16x32_bf16 v[140:143], a[80:83], a[176:179], v[196:199]\n"
      "ds_read_b64_tr_b16 a[108:109], v211 offset:57856\n"
      "ds_read_b64_tr_b16 a[110:111], v211 offset:61952\n"
      "v_mfma_f32_16x16x32_bf16 v[140:143], a[84:87], a[180:183], v[140:143]\n"
      "v_exp_f32 v129, v129\n"
      "v_mfma_f32_16x16x32_bf16 v[140:143], a[88:91], a[184:187], v[140:143]\n"
      "ds_read_b64_tr_b16 a[112:113], v210 offset:58368\n"
      "v_cvt_pk_bf16_f32 v128, v128, v129\n"
      "v_mfma_f32_16x16x32_bf16 v[140:143], a[92:95], a[188:191], v[140:143]\n"
      "v_exp_f32 v130, v130\n"
      "v_mfma_f32_16x16x32_bf16 v[144:147], a[64:67], a[192:195], v[200:203]\n"
      "ds_read_b64_tr_b16 a[114:115], v210 offset:62464\n"
      "ds_read_b64_tr_b16 a[116:117], v211 offset:58368\n"
      "v_mfma_f32_16x16x32_bf16 v[144:147], a[68:71], a[196:199], v[144:147]\n"
      "v_exp_f32 v131, v131\n"
      "v_mfma_f32_16x16x32_bf16 v[144:147], a[72:75], a[200:203], v[144:147]\n"
      "ds_read_b64_tr_b16 a[118:119], v211 offset:62464\n"
      "v_cvt_pk_bf16_f32 v129, v130, v131\n"
      "v_mfma_f32_16x16x32_bf16 v[144:147], a[76:79], a[204:207], v[144:147]\n"
      "v_exp_f32 v132, v132\n"
      "v_mfma_f32_16x16x32_bf16 v[148:151], a[80:83], a[192:195], v[200:203]\n"
      "ds_read_b64_tr_b16 a[120:121], v210 offset:58880\n"
      "ds_read_b64_tr_b16 a[122:123], v210 offset:62976\n"
      "v_mfma_f32_16x16x32_bf16 v[148:151], a[84:87], a[196:199], v[148:151]\n"
      "v_exp_f32 v133, v133\n"
      "v_mfma_f32_16x16x32_bf16 v[148:151], a[88:91], a[200:203], v[148:151]\n"
      "ds_read_b64_tr_b16 a[124:125], v211 offset:58880\n"
      "v_cvt_pk_bf16_f32 v130, v132, v133\n"
      "v_mfma_f32_16x16x32_bf16 v[148:151], a[92:95], a[204:207], v[148:151]\n"
      "v_exp_f32 v134, v134\n"
      "v_mfma_f32_16x16x32_bf16 v[152:155], a[64:67], a[208:211], v[204:207]\n"
      "ds_read_b64_tr_b16 a[126:127], v211 offset:62976\n"
      "v_mul_f32 v224, s61, v224\n"
      "v_mfma_f32_16x16x32_bf16 v[152:155], a[68:71], a[212:215], v[152:155]\n"
      "v_exp_f32 v135, v135\n"
      "v_mfma_f32_16x16x32_bf16 v[152:155], a[72:75], a[216:219], v[152:155]\n"
      "v_cvt_pk_bf16_f32 v131, v134, v135\n"
      "v_cvt_pk_bf16_f32 v224, v221, v224\n"
      "v_mfma_f32_16x16x32_bf16 v[152:155], a[76:79], a[220:223], v[152:155]\n"
      "v_exp_f32 v136, v136\n"
      "v_mfma_f32_16x16x32_bf16 v[156:159], a[80:83], a[208:211], v[204:207]\n"
      "ds_read_b128 a[64:67], v208 offset:8192\n"
      "ds_read_b128 a[68:71], v208 offset:8704\n"
      "v_mfma_f32_16x16x32_bf16 v[156:159], a[84:87], a[212:215], v[156:159]\n"
      "v_exp_f32 v137, v137\n"
      "v_mfma_f32_16x16x32_bf16 v[156:159], a[88:91], a[216:219], v[156:159]\n"
      "ds_read_b128 a[72:75], v208 offset:9216\n"
      "v_cvt_pk_bf16_f32 v136, v136, v137\n"
      "v_mfma_f32_16x16x32_bf16 v[156:159], a[92:95], a[220:223], v[156:159]\n"
      "v_exp_f32 v138, v138\n"
      "v_mfma_f32_16x16x32_bf16 a[128:131], a[144:147], v[160:163], a[128:131]\n"
      "ds_read_b128 a[76:79], v208 offset:9728\n"
      "v_accvgpr_write_b32 a32, v224\n"
      "s_waitcnt lgkmcnt(12)\n"
      "v_mfma_f32_16x16x32_bf16 v[0:3], a[96:99], v[160:163], v[0:3]\n"
      "v_exp_f32 v139, v139\n"
      "v_mfma_f32_16x16x32_bf16 v[4:7], a[100:103], v[160:163], v[4:7]\n"
      "ds_read_b128 a[80:83], v208 offset:12288\n"
      "v_cvt_pk_bf16_f32 v137, v138, v139\n"
      "v_mfma_f32_16x16x32_bf16 v[8:11], a[104:107], v[160:163], v[8:11]\n"
      "v_exp_f32 v140, v140\n"
      "v_mfma_f32_16x16x32_bf16 v[12:15], a[108:111], v[160:163], v[12:15]\n"
      "ds_read_b128 a[84:87], v208 offset:12800\n"
      "ds_read_b128 a[88:91], v208 offset:13312\n"
      "v_lshlrev_b32 v222, 16, v225\n"
      "s_waitcnt lgkmcnt(7)\n"
      "v_mfma_f32_16x16x32_bf16 v[16:19], a[112:115], v[160:163], v[16:19]\n"
      "v_exp_f32 v141, v141\n"
      "v_mfma_f32_16x16x32_bf16 v[20:23], a[116:119], v[160:163], v[20:23]\n"
      "ds_read_b128 a[92:95], v208 offset:13824\n"
      "v_cvt_pk_bf16_f32 v138, v140, v141\n"
      "v_mfma_f32_16x16x32_bf16 v[24:27], a[120:123], v[160:163], v[24:27]\n"
      "v_exp_f32 v142, v142\n"
      "v_mfma_f32_16x16x32_bf16 v[28:31], a[124:127], v[160:163], v[28:31]\n"
      "s_mov_b32 m0, s58\n"
      "buffer_store_dwordx4 a[152:155], v214, s[52:55], s77 offen offset:3072 sc1\n"
      "v_mfma_f32_16x16x32_bf16 a[132:135], a[144:147], v[168:171], a[132:135]\n"
      "s_add_u32 s68, s57, 0x18000\n"
      "v_exp_f32 v143, v143\n"
      "v_mfma_f32_16x16x32_bf16 v[32:35], a[96:99], v[168:171], v[32:35]\n"
      "buffer_load_dwordx4 v212, s[44:47], s68 offen nt lds\n"
      "v_cvt_pk_bf16_f32 v139, v142, v143\n"
      "v_mfma_f32_16x16x32_bf16 v[36:39], a[100:103], v[168:171], v[36:39]\n"
      "v_exp_f32 v144, v144\n"
      "v_mfma_f32_16x16x32_bf16 v[40:43], a[104:107], v[168:171], v[40:43]\n"
      "buffer_load_dwordx4 v213, s[44:47], s68 offen offset:1024 nt lds\n"
      "v_and_b32 v225, s62, v225\n"
      "v_mfma_f32_16x16x32_bf16 v[44:47], a[108:111], v[168:171], v[44:47]\n"
      "v_exp_f32 v145, v145\n"
      "v_mfma_f32_16x16x32_bf16 v[48:51], a[112:115], v[168:171], v[48:51]\n"
      "v_cvt_pk_bf16_f32 v144, v144, v145\n"
      "v_mul_f32 v222, s61, v222\n"
      "v_mfma_f32_16x16x32_bf16 v[52:55], a[116:119], v[168:171], v[52:55]\n"
      "v_exp_f32 v146, v146\n"
      "v_mfma_f32_16x16x32_bf16 v[56:59], a[120:123], v[168:171], v[56:59]\n"
      "v_mul_f32 v225, s61, v225\n"
      "v_cvt_pk_bf16_f32 v225, v222, v225\n"
      "v_mfma_f32_16x16x32_bf16 v[60:63], a[124:127], v[168:171], v[60:63]\n"
      "v_exp_f32 v147, v147\n"
      "v_mfma_f32_16x16x32_bf16 a[136:139], a[144:147], v[176:179], a[136:139]\n"
      "s_add_u32 m0, s58, 0xc000\n"
      "v_cvt_pk_bf16_f32 v145, v146, v147\n"
      "v_accvgpr_write_b32 a33, v225\n"
      "v_mfma_f32_16x16x32_bf16 v[64:67], a[96:99], v[176:179], v[64:67]\n"
      "s_add_u32 s67, s57, 0x14000\n"
      "v_exp_f32 v148, v148\n"
      "v_mfma_f32_16x16x32_bf16 v[68:71], a[100:103], v[176:179], v[68:71]\n"
      "buffer_load_dwordx4 v212, s[48:51], s67 offen nt lds\n"
      "v_lshlrev_b32 v221, 16, v226\n"
      "v_mfma_f32_16x16x32_bf16 v[72:75], a[104:107], v[176:179], v[72:75]\n"
      "v_exp_f32 v149, v149\n"
      "v_mfma_f32_16x16x32_bf16 v[76:79], a[108:111], v[176:179], v[76:79]\n"
      "buffer_load_dwordx4 v213, s[48:51], s67 offen offset:1024 nt lds\n"
      "v_cvt_pk_bf16_f32 v146, v148, v149\n"
      "v_mfma_f32_16x16x32_bf16 v[80:83], a[112:115], v[176:179], v[80:83]\n"
      "v_exp_f32 v150, v150\n"
      "v_mfma_f32_16x16x32_bf16 v[84:87], a[116:119], v[176:179], v[84:87]\n"
      "ds_read_b128 v[228:231], v209 offset:512\n"
      "v_and_b32 v226, s62, v226\n"
      "v_mfma_f32_16x16x32_bf16 v[88:91], a[120:123], v[176:179], v[88:91]\n"
      "v_exp_f32 v151, v151\n"
      "v_mfma_f32_16x16x32_bf16 v[92:95], a[124:127], v[176:179], v[92:95]\n"
      "s_add_u32 m0, s59, 0x1800\n"
      "v_cvt_pk_bf16_f32 v147, v150, v151\n"
      "v_mul_f32 v221, s61, v221\n"
      "v_mfma_f32_16x16x32_bf16 a[140:143], a[144:147], v[184:187], a[140:143]\n"
      "s_add_u32 s67, s56, 0x1003800\n"
      "v_exp_f32 v152, v152\n"
      "v_mfma_f32_16x16x32_bf16 v[96:99], a[96:99], v[184:187], v[96:99]\n"
      "buffer_load_dwordx4 v212, s[40:43], s67 offen nt lds\n"
      "v_mul_f32 v226, s61, v226\n"
      "v_mfma_f32_16x16x32_bf16 v[100:103], a[100:103], v[184:187], v[100:103]\n"
      "v_exp_f32 v153, v153\n"
      "v_mfma_f32_16x16x32_bf16 v[104:107], a[104:107], v[184:187], v[104:107]\n"
      "buffer_load_dwordx4 v213, s[40:43], s67 offen offset:1024 nt lds\n"
      "v_cvt_pk_bf16_f32 v152, v152, v153\n"
      "v_mfma_f32_16x16x32_bf16 v[108:111], a[108:111], v[184:187], v[108:111]\n"
      "v_exp_f32 v154, v154\n"
      "v_mfma_f32_16x16x32_bf16 v[112:115], a[112:115], v[184:187], v[112:115]\n"
      "v_cvt_pk_bf16_f32 v226, v221, v226\n"
      "v_accvgpr_write_b32 a34, v226\n"
      "v_mfma_f32_16x16x32_bf16 v[116:119], a[116:119], v[184:187], v[116:119]\n"
      "v_lshlrev_b32 v222, 16, v227\n"
      "v_and_b32 v227, s62, v227\n"
      "v_mfma_f32_16x16x32_bf16 v[120:123], a[120:123], v[184:187], v[120:123]\n"
      "s_memrealtime s[84:85]\n"
      "v_mul_f32 v222, s61, v222\n"
      "v_mul_f32 v227, s61, v227\n"
      "v_mfma_f32_16x16x32_bf16 v[124:127], a[124:127], v[184:187], v[124:127]\n"
      "v_cvt_pk_bf16_f32 v227, v222, v227\n"
      "v_accvgpr_write_b32 a35, v227\n"
      "s_waitcnt lgkmcnt(0)\n"
      "s_sub_u32 s85, s84, s86\n"
      "s_cmp_ge_u32 s85, s87\n"
      "s_cbranch_scc1 L_PACED1_8\n"
      "L_PACE1_8:\n"
      "s_memrealtime s[84:85]\n"
      "s_waitcnt lgkmcnt(0)\n"
      "s_sub_u32 s85, s84, s86\n"
      "s_cmp_lt_u32 s85, s87\n"
      "s_cbranch_scc1 L_PACE1_8\n"
      "L_PACED1_8:\n"
      "s_mov_b32 s86, s84\n"
      "v_mfma_f32_16x16x32_bf16 v[160:163], a[64:67], a[160:163], v[192:195]\n"
      "s_waitcnt vmcnt(14)\n"
      "s_barrier\n"
      "v_lshlrev_b32 v221, 16, v228\n"
      "v_and_b32 v228, s62, v228\n"
      "v_mfma_f32_16x16x32_bf16 v[160:163], a[68:71], a[164:167], v[160:163]\n"
      "v_exp_f32 v155, v155\n"
      "v_mfma_f32_16x16x32_bf16 v[160:163], a[72:75], a[168:171], v[160:163]\n"
      "ds_read_b128 a[148:151], v219 offset:16384\n"
      "v_cvt_pk_bf16_f32 v153, v154, v155\n"
      "v_mfma_f32_16x16x32_bf16 v[160:163], a[76:79], a[172:175], v[160:163]\n"
      "v_exp_f32 v156, v156\n"
      "v_mfma_f32_16x16x32_bf16 v[164:167], a[80:83], a[160:163], v[192:195]\n"
      "ds_read_b64_tr_b16 a[96:97], v210 offset:32768\n"
      "ds_read_b64_tr_b16 a[98:99], v210 offset:36864\n"
      "v_mfma_f32_16x16x32_bf16 v[164:167], a[84:87], a[164:167], v[164:167]\n"
      "v_exp_f32 v157, v157\n"
      "v_mfma_f32_16x16x32_bf16 v[164:167], a[88:91], a[168:171], v[164:167]\n"
      "ds_read_b64_tr_b16 a[100:101], v211 offset:32768\n"
      "v_cvt_pk_bf16_f32 v154, v156, v157\n"
      "v_mfma_f32_16x16x32_bf16 v[164:167], a[92:95], a[172:175], v[164:167]\n"
      "v_exp_f32 v158, v158\n"
      "v_mfma_f32_16x16x32_bf16 v[168:171], a[64:67], a[176:179], v[196:199]\n"
      "ds_read_b64_tr_b16 a[102:103], v211 offset:36864\n"
      "ds_read_b64_tr_b16 a[104:105], v210 offset:33280\n"
      "v_mfma_f32_16x16x32_bf16 v[168:171], a[68:71], a[180:183], v[168:171]\n"
      "v_exp_f32 v159, v159\n"
      "v_mfma_f32_16x16x32_bf16 v[168:171], a[72:75], a[184:187], v[168:171]\n"
      "ds_read_b64_tr_b16 a[106:107], v210 offset:37376\n"
      "v_cvt_pk_bf16_f32 v155, v158, v159\n"
      "v_mfma_f32_16x16x32_bf16 v[168:171], a[76:79], a[188:191], v[168:171]\n"
      "v_exp_f32 v160, v160\n"
      "v_mfma_f32_16x16x32_bf16 v[172:175], a[80:83], a[176:179], v[196:199]\n"
      "ds_read_b64_tr_b16 a[108:109], v211 offset:33280\n"
      "ds_read_b64_tr_b16 a[110:111], v211 offset:37376\n"
      "v_mfma_f32_16x16x32_bf16 v[172:175], a[84:87], a[180:183], v[172:175]\n"
      "v_exp_f32 v161, v161\n"
      "v_mfma_f32_16x16x32_bf16 v[172:175], a[88:91], a[184:187], v[172:175]\n"
      "ds_read_b64_tr_b16 a[112:113], v210 offset:33792\n"
      "v_cvt_pk_bf16_f32 v160, v160, v161\n"
      "v_mfma_f32_16x16x32_bf16 v[172:175], a[92:95], a[188:191], v[172:175]\n"
      "v_exp_f32 v162, v162\n"
      "v_mfma_f32_16x16x32_bf16 v[176:179], a[64:67], a[192:195], v[200:203]\n"
      "ds_read_b64_tr_b16 a[114:115], v210 offset:37888\n"
      "ds_read_b64_tr_b16 a[116:117], v211 offset:33792\n"
      "v_mfma_f32_16x16x32_bf16 v[176:179], a[68:71], a[196:199], v[176:179]\n"
      "v_exp_f32 v163, v163\n"
      "v_mfma_f32_16x16x32_bf16 v[176:179], a[72:75], a[200:203], v[176:179]\n"
      "ds_read_b64_tr_b16 a[118:119], v211 offset:37888\n"
      "v_cvt_pk_bf16_f32 v161, v162, v163\n"
      "v_mfma_f32_16x16x32_bf16 v[176:179], a[76:79], a[204:207], v[176:179]\n"
      "v_exp_f32 v164, v164\n"
      "v_mfma_f32_16x16x32_bf16 v[180:183], a[80:83], a[192:195], v[200:203]\n"
      "ds_read_b64_tr_b16 a[120:121], v210 offset:34304\n"
      "ds_read_b64_tr_b16 a[122:123], v210 offset:38400\n"
      "v_mfma_f32_16x16x32_bf16 v[180:183], a[84:87], a[196:199], v[180:183]\n"
      "v_exp_f32 v165, v165\n"
      "v_mfma_f32_16x16x32_bf16 v[180:183], a[88:91], a[200:203], v[180:183]\n"
      "ds_read_b64_tr_b16 a[124:125], v211 offset:34304\n"
      "v_cvt_pk_bf16_f32 v162, v164, v165\n"
      "v_mfma_f32_16x16x32_bf16 v[180:183], a[92:95], a[204:207], v[180:183]\n"
      "v_exp_f32 v166, v166\n"
      "v_mfma_f32_16x16x32_bf16 v[184:187], a[64:67], a[208:211], v[204:207]\n"
      "ds_read_b64_tr_b16 a[126:127], v211 offset:38400\n"
      "v_mul_f32 v221, s61, v221\n"
      "v_mfma_f32_16x16x32_bf16 v[184:187], a[68:71], a[212:215], v[184:187]\n"
      "v_exp_f32 v167, v167\n"
      "v_mfma_f32_16x16x32_bf16 v[184:187], a[72:75], a[216:219], v[184:187]\n"
      "v_cvt_pk_bf16_f32 v163, v166, v167\n"
      "v_mul_f32 v228, s61, v228\n"
      "v_mfma_f32_16x16x32_bf16 v[184:187], a[76:79], a[220:223], v[184:187]\n"
      "v_exp_f32 v168, v168\n"
      "v_mfma_f32_16x16x32_bf16 v[188:191], a[80:83], a[208:211], v[204:207]\n"
      "ds_read_b128 a[64:67], v208 offset:16384\n"
      "ds_read_b128 a[68:71], v208 offset:16896\n"
      "v_mfma_f32_16x16x32_bf16 v[188:191], a[84:87], a[212:215], v[188:191]\n"
      "v_exp_f32 v169, v169\n"
      "v_mfma_f32_16x16x32_bf16 v[188:191], a[88:91], a[216:219], v[188:191]\n"
      "ds_read_b128 a[72:75], v208 offset:17408\n"
      "v_cvt_pk_bf16_f32 v168, v168, v169\n"
      "v_mfma_f32_16x16x32_bf16 v[188:191], a[92:95], a[220:223], v[188:191]\n"
      "v_exp_f32 v170, v170\n"
      "v_mfma_f32_16x16x32_bf16 a[128:131], a[144:147], v[128:131], a[128:131]\n"
      "ds_read_b128 a[76:79], v208 offset:17920\n"
      "v_cvt_pk_bf16_f32 v228, v221, v228\n"
      "s_waitcnt lgkmcnt(12)\n"
      "v_mfma_f32_16x16x32_bf16 v[0:3], a[96:99], v[128:131], v[0:3]\n"
      "v_exp_f32 v171, v171\n"
      "v_mfma_f32_16x16x32_bf16 v[4:7], a[100:103], v[128:131], v[4:7]\n"
      "ds_read_b128 a[80:83], v208 offset:20480\n"
      "v_cvt_pk_bf16_f32 v169, v170, v171\n"
      "v_mfma_f32_16x16x32_bf16 v[8:11], a[104:107], v[128:131], v[8:11]\n"
      "v_exp_f32 v172, v172\n"
      "v_mfma_f32_16x16x32_bf16 v[12:15], a[108:111], v[128:131], v[12:15]\n"
      "ds_read_b128 a[84:87], v208 offset:20992\n"
      "ds_read_b128 a[88:91], v208 offset:21504\n"
      "s_waitcnt lgkmcnt(7)\n"
      "v_mfma_f32_16x16x32_bf16 v[16:19], a[112:115], v[128:131], v[16:19]\n"
      "v_exp_f32 v173, v173\n"
      "v_mfma_f32_16x16x32_bf16 v[20:23], a[116:119], v[128:131], v[20:23]\n"
      "ds_read_b128 a[92:95], v208 offset:22016\n"
      "v_cvt_pk_bf16_f32 v170, v172, v173\n"
      "v_mfma_f32_16x16x32_bf16 v[24:27], a[120:123], v[128:131], v[24:27]\n"
      "v_exp_f32 v174, v174\n"
      "v_mfma_f32_16x16x32_bf16 v[28:31], a[124:127], v[128:131], v[28:31]\n"
      "buffer_store_dwordx4 a[148:151], v214, s[52:55], s78 offen offset:0 sc1\n"
      "v_mfma_f32_16x16x32_bf16 a[132:135], a[144:147], v[136:139], a[132:135]\n"
      "v_exp_f32 v175, v175\n"
      "v_mfma_f32_16x16x32_bf16 v[32:35], a[96:99], v[136:139], v[32:35]\n"
      "s_add_u32 m0, s58, 0x2000\n"
      "v_cvt_pk_bf16_f32 v171, v174, v175\n"
      "v_accvgpr_write_b32 a36, v228\n"
      "v_mfma_f32_16x16x32_bf16 v[36:39], a[100:103], v[136:139], v[36:39]\n"
      "s_add_u32 s68, s57, 0x1a000\n"
      "v_exp_f32 v176, v176\n"
      "v_mfma_f32_16x16x32_bf16 v[40:43], a[104:107], v[136:139], v[40:43]\n"
      "buffer_load_dwordx4 v212, s[44:47], s68 offen nt lds\n"
      "v_lshlrev_b32 v222, 16, v229\n"
      "v_mfma_f32_16x16x32_bf16 v[44:47], a[108:111], v[136:139], v[44:47]\n"
      "v_exp_f32 v177, v177\n"
      "v_mfma_f32_16x16x32_bf16 v[48:51], a[112:115], v[136:139], v[48:51]\n"
      "buffer_load_dwordx4 v213, s[44:47], s68 offen offset:1024 nt lds\n"
      "v_cvt_pk_bf16_f32 v176, v176, v177\n"
      "v_mfma_f32_16x16x32_bf16 v[52:55], a[116:119], v[136:139], v[52:55]\n"
      "v_exp_f32 v178, v178\n"
      "v_mfma_f32_16x16x32_bf16 v[56:59], a[120:123], v[136:139], v[56:59]\n"
      "v_and_b32 v229, s62, v229\n"
      "v_mul_f32 v222, s61, v222\n"
      "v_mfma_f32_16x16x32_bf16 v[60:63], a[124:127], v[136:139], v[60:63]\n"
      "v_exp_f32 v179, v179\n"
      "v_mfma_f32_16x16x32_bf16 a[136:139], a[144:147], v[144:147], a[136:139]\n"
      "v_cvt_pk_bf16_f32 v177, v178, v179\n"
      "v_mul_f32 v229, s61, v229\n"
      "v_mfma_f32_16x16x32_bf16 v[64:67], a[96:99], v[144:147], v[64:67]\n"
      "v_exp_f32 v180, v180\n"
      "v_mfma_f32_16x16x32_bf16 v[68:71], a[100:103], v[144:147], v[68:71]\n"
      "v_cvt_pk_bf16_f32 v229, v222, v229\n"
      "v_accvgpr_write_b32 a37, v229\n"
      "v_mfma_f32_16x16x32_bf16 v[72:75], a[104:107], v[144:147], v[72:75]\n"
      "v_exp_f32 v181, v181\n"
      "v_mfma_f32_16x16x32_bf16 v[76:79], a[108:111], v[144:147], v[76:79]\n"
      "v_cvt_pk_bf16_f32 v178, v180, v181\n"
      "v_lshlrev_b32 v221, 16, v230\n"
      "v_mfma_f32_16x16x32_bf16 v[80:83], a[112:115], v[144:147], v[80:83]\n"
      "v_exp_f32 v182, v182\n"
      "v_mfma_f32_16x16x32_bf16 v[84:87], a[116:119], v[144:147], v[84:87]\n"
      "s_add_u32 m0, s58, 0xe000\n"
      "ds_read_b128 v[224:227], v209 offset:1024\n"
      "v_and_b32 v230, s62, v230\n"
      "v_mfma_f32_16x16x32_bf16 v[88:91], a[120:123], v[144:147], v[88:91]\n"
      "s_add_u32 s67, s57, 0x16000\n"
      "v_exp_f32 v183, v183\n"
      "v_mfma_f32_16x16x32_bf16 v[92:95], a[124:127], v[144:147], v[92:95]\n"
      "buffer_load_dwordx4 v212, s[48:51], s67 offen nt lds\n"
      "v_cvt_pk_bf16_f32 v179, v182, v183\n"
      "v_mfma_f32_16x16x32_bf16 a[140:143], a[144:147], v[152:155], a[140:143]\n"
      "v_exp_f32 v184, v184\n"
      "v_mfma_f32_16x16x32_bf16 v[96:99], a[96:99], v[152:155], v[96:99]\n"
      "buffer_load_dwordx4 v213, s[48:51], s67 offen offset:1024 nt lds\n"
      "v_mul_f32 v221, s61, v221\n"
      "v_mfma_f32_16x16x32_bf16 v[100:103], a[100:103], v[152:155], v[100:103]\n"
      "v_exp_f32 v185, v185\n"
      "v_mfma_f32_16x16x32_bf16 v[104:107], a[104:107], v[152:155], v[104:107]\n"
      "v_cvt_pk_bf16_f32 v184, v184, v185\n"
      "v_mul_f32 v230, s61, v230\n"
      "v_mfma_f32_16x16x32_bf16 v[108:111], a[108:111], v[152:155], v[108:111]\n"
      "v_exp_f32 v186, v186\n"
      "v_mfma_f32_16x16x32_bf16 v[112:115], a[112:115], v[152:155], v[112:115]\n"
      "v_cvt_pk_bf16_f32 v230, v221, v230\n"
      "v_accvgpr_write_b32 a38, v230\n"
      "v_mfma_f32_16x16x32_bf16 v[116:119], a[116:119], v[152:155], v[116:119]\n"
      "v_lshlrev_b32 v222, 16, v231\n"
      "v_and_b32 v231, s62, v231\n"
      "v_mfma_f32_16x16x32_bf16 v[120:123], a[120:123], v[152:155], v[120:123]\n"
      "v_mul_f32 v222, s61, v222\n"
      "v_mul_f32 v231, s61, v231\n"
      "v_mfma_f32_16x16x32_bf16 v[124:127], a[124:127], v[152:155], v[124:127]\n"
      "v_cvt_pk_bf16_f32 v231, v222, v231\n"
      "v_accvgpr_write_b32 a39, v231\n"
      "s_waitcnt lgkmcnt(0)\n"
      "v_mfma_f32_16x16x32_bf16 v[128:131], a[64:67], a[160:163], v[192:195]\n"
      "s_waitcnt vmcnt(14)\n"
      "s_barrier\n"
      "v_lshlrev_b32 v221, 16, v224\n"
      "v_and_b32 v224, s62, v224\n"
      "v_mfma_f32_16x16x32_bf16 v[128:131], a[68:71], a[164:167], v[128:131]\n"
      "v_exp_f32 v187, v187\n"
      "v_mfma_f32_16x16x32_bf16 v[128:131], a[72:75], a[168:171], v[128:131]\n"
      "ds_read_b128 a[152:155], v220 offset:17408\n"
      "v_cvt_pk_bf16_f32 v185, v186, v187\n"
      "v_mfma_f32_16x16x32_bf16 v[128:131], a[76:79], a[172:175], v[128:131]\n"
      "v_exp_f32 v188, v188\n"
      "v_mfma_f32_16x16x32_bf16 v[132:135], a[80:83], a[160:163], v[192:195]\n"
      "ds_read_b64_tr_b16 a[96:97], v210 offset:40960\n"
      "ds_read_b64_tr_b16 a[98:99], v210 offset:45056\n"
      "v_mfma_f32_16x16x32_bf16 v[132:135], a[84:87], a[164:167], v[132:135]\n"
      "v_exp_f32 v189, v189\n"
      "v_mfma_f32_16x16x32_bf16 v[132:135], a[88:91], a[168:171], v[132:135]\n"
      "ds_read_b64_tr_b16 a[100:101], v211 offset:40960\n"
      "v_cvt_pk_bf16_f32 v186, v188, v189\n"
      "v_mfma_f32_16x16x32_bf16 v[132:135], a[92:95], a[172:175], v[132:135]\n"
      "v_exp_f32 v190, v190\n"
      "v_mfma_f32_16x16x32_bf16 v[136:139], a[64:67], a[176:179], v[196:199]\n"
      "ds_read_b64_tr_b16 a[102:103], v211 offset:45056\n"
      "ds_read_b64_tr_b16 a[104:105], v210 offset:41472\n"
      "v_mfma_f32_16x16x32_bf16 v[136:139], a[68:71], a[180:183], v[136:139]\n"
      "v_exp_f32 v191, v191\n"
      "v_mfma_f32_16x16x32_bf16 v[136:139], a[72:75], a[184:187], v[136:139]\n"
      "ds_read_b64_tr_b16 a[106:107], v210 offset:45568\n"
      "v_cvt_pk_bf16_f32 v187, v190, v191\n"
      "v_mfma_f32_16x16x32_bf16 v[136:139], a[76:79], a[188:191], v[136:139]\n"
      "v_exp_f32 v128, v128\n"
      "v_mfma_f32_16x16x32_bf16 v[140:143], a[80:83], a[176:179], v[196:199]\n"
      "ds_read_b64_tr_b16 a[108:109], v211 offset:41472\n"
      "ds_read_b64_tr_b16 a[110:111], v211 offset:45568\n"
      "v_mfma_f32_16x16x32_bf16 v[140:143], a[84:87], a[180:183], v[140:143]\n"
      "v_exp_f32 v129, v129\n"
      "v_mfma_f32_16x16x32_bf16 v[140:143], a[88:91], a[184:187], v[140:143]\n"
      "ds_read_b64_tr_b16 a[112:113], v210 offset:41984\n"
      "v_cvt_pk_bf16_f32 v128, v128, v129\n"
      "v_mfma_f32_16x16x32_bf16 v[140:143], a[92:95], a[188:191], v[140:143]\n"
      "v_exp_f32 v130, v130\n"
      "v_mfma_f32_16x16x32_bf16 v[144:147], a[64:67], a[192:195], v[200:203]\n"
      "ds_read_b64_tr_b16 a[114:115], v210 offset:46080\n"
      "ds_read_b64_tr_b16 a[116:117], v211 offset:41984\n"
      "v_mfma_f32_16x16x32_bf16 v[144:147], a[68:71], a[196:199], v[144:147]\n"
      "v_exp_f32 v131, v131\n"
      "v_mfma_f32_16x16x32_bf16 v[144:147], a[72:75], a[200:203], v[144:147]\n"
      "ds_read_b64_tr_b16 a[118:119], v211 offset:46080\n"
      "v_cvt_pk_bf16_f32 v129, v130, v131\n"
      "v_mfma_f32_16x16x32_bf16 v[144:147], a[76:79], a[204:207], v[144:147]\n"
      "v_exp_f32 v132, v132\n"
      "v_mfma_f32_16x16x32_bf16 v[148:151], a[80:83], a[192:195], v[200:203]\n"
      "ds_read_b64_tr_b16 a[120:121], v210 offset:42496\n"
      "ds_read_b64_tr_b16 a[122:123], v210 offset:46592\n"
      "v_mfma_f32_16x16x32_bf16 v[148:151], a[84:87], a[196:199], v[148:151]\n"
      "v_exp_f32 v133, v133\n"
      "v_mfma_f32_16x16x32_bf16 v[148:151], a[88:91], a[200:203], v[148:151]\n"
      "ds_read_b64_tr_b16 a[124:125], v211 offset:42496\n"
      "v_cvt_pk_bf16_f32 v130, v132, v133\n"
      "v_mfma_f32_16x16x32_bf16 v[148:151], a[92:95], a[204:207], v[148:151]\n"
      "v_exp_f32 v134, v134\n"
      "v_mfma_f32_16x16x32_bf16 v[152:155], a[64:67], a[208:211], v[204:207]\n"
      "ds_read_b64_tr_b16 a[126:127], v211 offset:46592\n"
      "v_mul_f32 v221, s61, v221\n"
      "v_mfma_f32_16x16x32_bf16 v[152:155], a[68:71], a[212:215], v[152:155]\n"
      "v_exp_f32 v135, v135\n"
      "v_mfma_f32_16x16x32_bf16 v[152:155], a[72:75], a[216:219], v[152:155]\n"
      "v_cvt_pk_bf16_f32 v131, v134, v135\n"
      "v_mul_f32 v224, s61, v224\n"
      "v_mfma_f32_16x16x32_bf16 v[152:155], a[76:79], a[220:223], v[152:155]\n"
      "v_exp_f32 v136, v136\n"
      "v_mfma_f32_16x16x32_bf16 v[156:159], a[80:83], a[208:211], v[204:207]\n"
      "ds_read_b128 a[64:67], v208 offset:24576\n"
      "ds_read_b128 a[68:71], v208 offset:25088\n"
      "v_mfma_f32_16x16x32_bf16 v[156:159], a[84:87], a[212:215], v[156:159]\n"
      "v_exp_f32 v137, v137\n"
      "v_mfma_f32_16x16x32_bf16 v[156:159], a[88:91], a[216:219], v[156:159]\n"
      "ds_read_b128 a[72:75], v208 offset:25600\n"
      "v_cvt_pk_bf16_f32 v136, v136, v137\n"
      "v_mfma_f32_16x16x32_bf16 v[156:159], a[92:95], a[220:223], v[156:159]\n"
      "v_exp_f32 v138, v138\n"
      "v_mfma_f32_16x16x32_bf16 a[128:131], a[144:147], v[160:163], a[128:131]\n"
      "ds_read_b128 a[76:79], v208 offset:26112\n"
      "v_cvt_pk_bf16_f32 v224, v221, v224\n"
      "s_waitcnt lgkmcnt(12)\n"
      "v_mfma_f32_16x16x32_bf16 v[0:3], a[96:99], v[160:163], v[0:3]\n"
      "v_exp_f32 v139, v139\n"
      "v_mfma_f32_16x16x32_bf16 v[4:7], a[100:103], v[160:163], v[4:7]\n"
      "ds_read_b128 a[80:83], v208 offset:28672\n"
      "v_cvt_pk_bf16_f32 v137, v138, v139\n"
      "v_mfma_f32_16x16x32_bf16 v[8:11], a[104:107], v[160:163], v[8:11]\n"
      "v_exp_f32 v140, v140\n"
      "v_mfma_f32_16x16x32_bf16 v[12:15], a[108:111], v[160:163], v[12:15]\n"
      "ds_read_b128 a[84:87], v208 offset:29184\n"
      "ds_read_b128 a[88:91], v208 offset:29696\n"
      "s_waitcnt lgkmcnt(7)\n"
      "v_mfma_f32_16x16x32_bf16 v[16:19], a[112:115], v[160:163], v[16:19]\n"
      "v_exp_f32 v141, v141\n"
      "v_mfma_f32_16x16x32_bf16 v[20:23], a[116:119], v[160:163], v[20:23]\n"
      "ds_read_b128 a[92:95], v208 offset:30208\n"
      "v_cvt_pk_bf16_f32 v138, v140, v141\n"
      "v_mfma_f32_16x16x32_bf16 v[24:27], a[120:123], v[160:163], v[24:27]\n"
      "v_exp_f32 v142, v142\n"
      "v_mfma_f32_16x16x32_bf16 v[28:31], a[124:127], v[160:163], v[28:31]\n"
      "buffer_store_dwordx4 a[152:155], v214, s[52:55], s78 offen offset:1024 sc1\n"
      "v_mfma_f32_16x16x32_bf16 a[132:135], a[144:147], v[168:171], a[132:135]\n"
      "v_exp_f32 v143, v143\n"
      "v_mfma_f32_16x16x32_bf16 v[32:35], a[96:99], v[168:171], v[32:35]\n"
      "s_add_u32 m0, s58, 0x4000\n"
      "v_cvt_pk_bf16_f32 v139, v142, v143\n"
      "v_accvgpr_write_b32 a40, v224\n"
      "v_mfma_f32_16x16x32_bf16 v[36:39], a[100:103], v[168:171], v[36:39]\n"
      "s_add_u32 s68, s57, 0x1c000\n"
      "v_exp_f32 v144, v144\n"
      "v_mfma_f32_16x16x32_bf16 v[40:43], a[104:107], v[168:171], v[40:43]\n"
      "buffer_load_dwordx4 v212, s[44:47], s68 offen nt lds\n"
      "v_lshlrev_b32 v222, 16, v225\n"
      "v_mfma_f32_16x16x32_bf16 v[44:47], a[108:111], v[168:171], v[44:47]\n"
      "v_exp_f32 v145, v145\n"
      "v_mfma_f32_16x16x32_bf16 v[48:51], a[112:115], v[168:171], v[48:51]\n"
      "buffer_load_dwordx4 v213, s[44:47], s68 offen offset:1024 nt lds\n"
      "v_cvt_pk_bf16_f32 v144, v144, v145\n"
      "v_mfma_f32_16x16x32_bf16 v[52:55], a[116:119], v[168:171], v[52:55]\n"
      "v_exp_f32 v146, v146\n"
      "v_mfma_f32_16x16x32_bf16 v[56:59], a[120:123], v[168:171], v[56:59]\n"
      "v_and_b32 v225, s62, v225\n"
      "v_mul_f32 v222, s61, v222\n"
      "v_mfma_f32_16x16x32_bf16 v[60:63], a[124:127], v[168:171], v[60:63]\n"
      "v_exp_f32 v147, v147\n"
      "v_mfma_f32_16x16x32_bf16 a[136:139], a[144:147], v[176:179], a[136:139]\n"
      "v_cvt_pk_bf16_f32 v145, v146, v147\n"
      "v_mul_f32 v225, s61, v225\n"
      "v_mfma_f32_16x16x32_bf16 v[64:67], a[96:99], v[176:179], v[64:67]\n"
      "v_exp_f32 v148, v148\n"
      "v_mfma_f32_16x16x32_bf16 v[68:71], a[100:103], v[176:179], v[68:71]\n"
      "v_cvt_pk_bf16_f32 v225, v222, v225\n"
      "v_accvgpr_write_b32 a41, v225\n"
      "v_mfma_f32_16x16x32_bf16 v[72:75], a[104:107], v[176:179], v[72:75]\n"
      "v_exp_f32 v149, v149\n"
      "v_mfma_f32_16x16x32_bf16 v[76:79], a[108:111], v[176:179], v[76:79]\n"
      "v_cvt_pk_bf16_f32 v146, v148, v149\n"
      "v_lshlrev_b32 v221, 16, v226\n"
      "v_mfma_f32_16x16x32_bf16 v[80:83], a[112:115], v[176:179], v[80:83]\n"
      "v_exp_f32 v150, v150\n"
      "v_mfma_f32_16x16x32_bf16 v[84:87], a[116:119], v[176:179], v[84:87]\n"
      "s_add_u32 m0, s58, 0x8000\n"
      "ds_read_b128 v[228:231], v209 offset:1536\n"
      "v_and_b32 v226, s62, v226\n"
      "v_mfma_f32_16x16x32_bf16 v[88:91], a[120:123], v[176:179], v[88:91]\n"
      "s_add_u32 s67, s57, 0x18000\n"
      "v_exp_f32 v151, v151\n"
      "v_mfma_f32_16x16x32_bf16 v[92:95], a[124:127], v[176:179], v[92:95]\n"
      "buffer_load_dwordx4 v212, s[48:51], s67 offen nt lds\n"
      "v_cvt_pk_bf16_f32 v147, v150, v151\n"
      "v_mfma_f32_16x16x32_bf16 a[140:143], a[144:147], v[184:187], a[140:143]\n"
      "v_exp_f32 v152, v152\n"
      "v_mfma_f32_16x16x32_bf16 v[96:99], a[96:99], v[184:187], v[96:99]\n"
      "buffer_load_dwordx4 v213, s[48:51], s67 offen offset:1024 nt lds\n"
      "v_mul_f32 v221, s61, v221\n"
      "v_mfma_f32_16x16x32_bf16 v[100:103], a[100:103], v[184:187], v[100:103]\n"
      "v_exp_f32 v153, v153\n"
      "v_mfma_f32_16x16x32_bf16 v[104:107], a[104:107], v[184:187], v[104:107]\n"
      "v_cvt_pk_bf16_f32 v152, v152, v153\n"
      "v_mul_f32 v226, s61, v226\n"
      "v_mfma_f32_16x16x32_bf16 v[108:111], a[108:111], v[184:187], v[108:111]\n"
      "v_exp_f32 v154, v154\n"
      "v_mfma_f32_16x16x32_bf16 v[112:115], a[112:115], v[184:187], v[112:115]\n"
      "v_cvt_pk_bf16_f32 v226, v221, v226\n"
      "v_accvgpr_write_b32 a42, v226\n"
      "v_mfma_f32_16x16x32_bf16 v[116:119], a[116:119], v[184:187], v[116:119]\n"
      "v_lshlrev_b32 v222, 16, v227\n"
      "v_and_b32 v227, s62, v227\n"
      "v_mfma_f32_16x16x32_bf16 v[120:123], a[120:123], v[184:187], v[120:123]\n"
      "s_memrealtime s[84:85]\n"
      "v_mul_f32 v222, s61, v222\n"
      "v_mul_f32 v227, s61, v227\n"
      "v_mfma_f32_16x16x32_bf16 v[124:127], a[124:127], v[184:187], v[124:127]\n"
      "v_cvt_pk_bf16_f32 v227, v222, v227\n"
      "v_accvgpr_write_b32 a43, v227\n"
      "s_waitcnt lgkmcnt(0)\n"
      "s_sub_u32 s85, s84, s86\n"
      "s_cmp_ge_u32 s85, s87\n"
      "s_cbranch_scc1 L_PACED1_10\n"
      "L_PACE1_10:\n"
      "s_memrealtime s[84:85]\n"
      "s_waitcnt lgkmcnt(0)\n"
      "s_sub_u32 s85, s84, s86\n"
      "s_cmp_lt_u32 s85, s87\n"
      "s_cbranch_scc1 L_PACE1_10\n"
      "L_PACED1_10:\n"
      "s_mov_b32 s86, s84\n"
      "v_mfma_f32_16x16x32_bf16 v[160:163], a[64:67], a[160:163], v[192:195]\n"
      "s_waitcnt vmcnt(10)\n"
      "s_barrier\n"
      "v_lshlrev_b32 v221, 16, v228\n"
      "v_and_b32 v228, s62, v228\n"
      "v_mul_f32 v221, s61, v221\n"
      "v_mfma_f32_16x16x32_bf16 v[160:163], a[68:71], a[164:167], v[160:163]\n"
      "v_exp_f32 v155, v155\n"
      "v_mfma_f32_16x16x32_bf16 v[160:163], a[72:75], a[168:171], v[160:163]\n"
      "ds_read_b128 a[148:151], v219 offset:18432\n"
      "v_cvt_pk_bf16_f32 v153, v154, v155\n"
      "v_mfma_f32_16x16x32_bf16 v[160:163], a[76:79], a[172:175], v[160:163]\n"
      "v_exp_f32 v156, v156\n"
      "v_mfma_f32_16x16x32_bf16 v[164:167], a[80:83], a[160:163], v[192:195]\n"
      "ds_read_b64_tr_b16 a[96:97], v210 offset:49152\n"
      "ds_read_b64_tr_b16 a[98:99], v210 offset:53248\n"
      "v_mfma_f32_16x16x32_bf16 v[164:167], a[84:87], a[164:167], v[164:167]\n"
      "v_exp_f32 v157, v157\n"
      "v_mfma_f32_16x16x32_bf16 v[164:167], a[88:91], a[168:171], v[164:167]\n"
      "ds_read_b64_tr_b16 a[100:101], v211 offset:49152\n"
      "v_cvt_pk_bf16_f32 v154, v156, v157\n"
      "v_mfma_f32_16x16x32_bf16 v[164:167], a[92:95], a[172:175], v[164:167]\n"
      "v_exp_f32 v158, v158\n"
      "v_mfma_f32_16x16x32_bf16 v[168:171], a[64:67], a[176:179], v[196:199]\n"
      "ds_read_b64_tr_b16 a[102:103], v211 offset:53248\n"
      "ds_read_b64_tr_b16 a[104:105], v210 offset:49664\n"
      "v_mfma_f32_16x16x32_bf16 v[168:171], a[68:71], a[180:183], v[168:171]\n"
      "v_exp_f32 v159, v159\n"
      "v_mfma_f32_16x16x32_bf16 v[168:171], a[72:75], a[184:187], v[168:171]\n"
      "ds_read_b64_tr_b16 a[106:107], v210 offset:53760\n"
      "v_cvt_pk_bf16_f32 v155, v158, v159\n"
      "v_mfma_f32_16x16x32_bf16 v[168:171], a[76:79], a[188:191], v[168:171]\n"
      "v_exp_f32 v160, v160\n"
      "v_mfma_f32_16x16x32_bf16 v[172:175], a[80:83], a[176:179], v[196:199]\n"
      "ds_read_b64_tr_b16 a[108:109], v211 offset:49664\n"
      "ds_read_b64_tr_b16 a[110:111], v211 offset:53760\n"
      "v_mfma_f32_16x16x32_bf16 v[172:175], a[84:87], a[180:183], v[172:175]\n"
      "v_exp_f32 v161, v161\n"
      "v_mfma_f32_16x16x32_bf16 v[172:175], a[88:91], a[184:187], v[172:175]\n"
      "ds_read_b64_tr_b16 a[112:113], v210 offset:50176\n"
      "v_cvt_pk_bf16_f32 v160, v160, v161\n"
      "v_mfma_f32_16x16x32_bf16 v[172:175], a[92:95], a[188:191], v[172:175]\n"
      "v_exp_f32 v162, v162\n"
      "v_mfma_f32_16x16x32_bf16 v[176:179], a[64:67], a[192:195], v[200:203]\n"
      "ds_read_b64_tr_b16 a[114:115], v210 offset:54272\n"
      "ds_read_b64_tr_b16 a[116:117], v211 offset:50176\n"
      "v_mfma_f32_16x16x32_bf16 v[176:179], a[68:71], a[196:199], v[176:179]\n"
      "v_exp_f32 v163, v163\n"
      "v_mfma_f32_16x16x32_bf16 v[176:179], a[72:75], a[200:203], v[176:179]\n"
      "ds_read_b64_tr_b16 a[118:119], v211 offset:54272\n"
      "v_cvt_pk_bf16_f32 v161, v162, v163\n"
      "v_mfma_f32_16x16x32_bf16 v[176:179], a[76:79], a[204:207], v[176:179]\n"
      "v_exp_f32 v164, v164\n"
      "v_mfma_f32_16x16x32_bf16 v[180:183], a[80:83], a[192:195], v[200:203]\n"
      "ds_read_b64_tr_b16 a[120:121], v210 offset:50688\n"
      "ds_read_b64_tr_b16 a[122:123], v210 offset:54784\n"
      "v_mfma_f32_16x16x32_bf16 v[180:183], a[84:87], a[196:199], v[180:183]\n"
      "v_exp_f32 v165, v165\n"
      "v_mfma_f32_16x16x32_bf16 v[180:183], a[88:91], a[200:203], v[180:183]\n"
      "ds_read_b64_tr_b16 a[124:125], v211 offset:50688\n"
      "v_cvt_pk_bf16_f32 v162, v164, v165\n"
      "v_mfma_f32_16x16x32_bf16 v[180:183], a[92:95], a[204:207], v[180:183]\n"
      "v_exp_f32 v166, v166\n"
      "v_mfma_f32_16x16x32_bf16 v[184:187], a[64:67], a[208:211], v[204:207]\n"
      "ds_read_b64_tr_b16 a[126:127], v211 offset:54784\n"
      "v_mul_f32 v228, s61, v228\n"
      "v_mfma_f32_16x16x32_bf16 v[184:187], a[68:71], a[212:215], v[184:187]\n"
      "v_exp_f32 v167, v167\n"
      "v_mfma_f32_16x16x32_bf16 v[184:187], a[72:75], a[216:219], v[184:187]\n"
      "v_cvt_pk_bf16_f32 v163, v166, v167\n"
      "v_cvt_pk_bf16_f32 v228, v221, v228\n"
      "v_mfma_f32_16x16x32_bf16 v[184:187], a[76:79], a[220:223], v[184:187]\n"
      "v_exp_f32 v168, v168\n"
      "v_mfma_f32_16x16x32_bf16 v[188:191], a[80:83], a[208:211], v[204:207]\n"
      "ds_read_b128 a[64:67], v208 offset:0\n"
      "ds_read_b128 a[68:71], v208 offset:512\n"
      "v_mfma_f32_16x16x32_bf16 v[188:191], a[84:87], a[212:215], v[188:191]\n"
      "v_exp_f32 v169, v169\n"
      "v_mfma_f32_16x16x32_bf16 v[188:191], a[88:91], a[216:219], v[188:191]\n"
      "ds_read_b128 a[72:75], v208 offset:1024\n"
      "v_cvt_pk_bf16_f32 v168, v168, v169\n"
      "v_mfma_f32_16x16x32_bf16 v[188:191], a[92:95], a[220:223], v[188:191]\n"
      "v_exp_f32 v170, v170\n"
      "v_mfma_f32_16x16x32_bf16 a[128:131], a[144:147], v[128:131], a[128:131]\n"
      "ds_read_b128 a[76:79], v208 offset:1536\n"
      "v_accvgpr_write_b32 a44, v228\n"
      "s_waitcnt lgkmcnt(12)\n"
      "v_mfma_f32_16x16x32_bf16 v[0:3], a[96:99], v[128:131], v[0:3]\n"
      "v_exp_f32 v171, v171\n"
      "v_mfma_f32_16x16x32_bf16 v[4:7], a[100:103], v[128:131], v[4:7]\n"
      "ds_read_b128 a[80:83], v208 offset:4096\n"
      "v_cvt_pk_bf16_f32 v169, v170, v171\n"
      "v_mfma_f32_16x16x32_bf16 v[8:11], a[104:107], v[128:131], v[8:11]\n"
      "v_exp_f32 v172, v172\n"
      "v_mfma_f32_16x16x32_bf16 v[12:15], a[108:111], v[128:131], v[12:15]\n"
      "ds_read_b128 a[84:87], v208 offset:4608\n"
      "ds_read_b128 a[88:91], v208 offset:5120\n"
      "v_lshlrev_b32 v222, 16, v229\n"
      "s_waitcnt lgkmcnt(7)\n"
      "v_mfma_f32_16x16x32_bf16 v[16:19], a[112:115], v[128:131], v[16:19]\n"
      "v_exp_f32 v173, v173\n"
      "v_mfma_f32_16x16x32_bf16 v[20:23], a[116:119], v[128:131], v[20:23]\n"
      "ds_read_b128 a[92:95], v208 offset:5632\n"
      "v_cvt_pk_bf16_f32 v170, v172, v173\n"
      "v_mfma_f32_16x16x32_bf16 v[24:27], a[120:123], v[128:131], v[24:27]\n"
      "v_exp_f32 v174, v174\n"
      "v_mfma_f32_16x16x32_bf16 v[28:31], a[124:127], v[128:131], v[28:31]\n"
      "s_add_u32 m0, s58, 0x6000\n"
      "buffer_store_dwordx4 a[148:151], v214, s[52:55], s78 offen offset:2048 sc1\n"
      "v_mfma_f32_16x16x32_bf16 a[132:135], a[144:147], v[136:139], a[132:135]\n"
      "s_add_u32 s67, s57, 0x1e000\n"
      "v_exp_f32 v175, v175\n"
      "v_mfma_f32_16x16x32_bf16 v[32:35], a[96:99], v[136:139], v[32:35]\n"
      "buffer_load_dwordx4 v212, s[44:47], s67 offen nt lds\n"
      "v_cvt_pk_bf16_f32 v171, v174, v175\n"
      "v_mfma_f32_16x16x32_bf16 v[36:39], a[100:103], v[136:139], v[36:39]\n"
      "v_exp_f32 v176, v176\n"
      "v_mfma_f32_16x16x32_bf16 v[40:43], a[104:107], v[136:139], v[40:43]\n"
      "buffer_load_dwordx4 v213, s[44:47], s67 offen offset:1024 nt lds\n"
      "v_and_b32 v229, s62, v229\n"
      "v_mfma_f32_16x16x32_bf16 v[44:47], a[108:111], v[136:139], v[44:47]\n"
      "v_exp_f32 v177, v177\n"
      "v_mfma_f32_16x16x32_bf16 v[48:51], a[112:115], v[136:139], v[48:51]\n"
      "v_cvt_pk_bf16_f32 v176, v176, v177\n"
      "v_mul_f32 v222, s61, v222\n"
      "v_mfma_f32_16x16x32_bf16 v[52:55], a[116:119], v[136:139], v[52:55]\n"
      "v_exp_f32 v178, v178\n"
      "v_mfma_f32_16x16x32_bf16 v[56:59], a[120:123], v[136:139], v[56:59]\n"
      "v_mul_f32 v229, s61, v229\n"
      "v_cvt_pk_bf16_f32 v229, v222, v229\n"
      "v_mfma_f32_16x16x32_bf16 v[60:63], a[124:127], v[136:139], v[60:63]\n"
      "v_exp_f32 v179, v179\n"
      "v_mfma_f32_16x16x32_bf16 a[136:139], a[144:147], v[144:147], a[136:139]\n"
      "s_add_u32 m0, s58, 0xa000\n"
      "v_cvt_pk_bf16_f32 v177, v178, v179\n"
      "v_accvgpr_write_b32 a45, v229\n"
      "v_mfma_f32_16x16x32_bf16 v[64:67], a[96:99], v[144:147], v[64:67]\n"
      "s_add_u32 s68, s57, 0x1a000\n"
      "v_exp_f32 v180, v180\n"
      "v_mfma_f32_16x16x32_bf16 v[68:71], a[100:103], v[144:147], v[68:71]\n"
      "buffer_load_dwordx4 v212, s[48:51], s68 offen nt lds\n"
      "v_lshlrev_b32 v221, 16, v230\n"
      "v_mfma_f32_16x16x32_bf16 v[72:75], a[104:107], v[144:147], v[72:75]\n"
      "v_exp_f32 v181, v181\n"
      "v_mfma_f32_16x16x32_bf16 v[76:79], a[108:111], v[144:147], v[76:79]\n"
      "buffer_load_dwordx4 v213, s[48:51], s68 offen offset:1024 nt lds\n"
      "v_cvt_pk_bf16_f32 v178, v180, v181\n"
      "v_mfma_f32_16x16x32_bf16 v[80:83], a[112:115], v[144:147], v[80:83]\n"
      "v_exp_f32 v182, v182\n"
      "v_mfma_f32_16x16x32_bf16 v[84:87], a[116:119], v[144:147], v[84:87]\n"
      "ds_read_b128 v[224:227], v209 offset:4096\n"
      "v_and_b32 v230, s62, v230\n"
      "v_mfma_f32_16x16x32_bf16 v[88:91], a[120:123], v[144:147], v[88:91]\n"
      "v_exp_f32 v183, v183\n"
      "v_mfma_f32_16x16x32_bf16 v[92:95], a[124:127], v[144:147], v[92:95]\n"
      "s_mov_b32 m0, s59\n"
      "v_cvt_pk_bf16_f32 v179, v182, v183\n"
      "v_mul_f32 v221, s61, v221\n"
      "v_mfma_f32_16x16x32_bf16 a[140:143], a[144:147], v[152:155], a[140:143]\n"
      "s_add_u32 s68, s56, 0x2000000\n"
      "v_exp_f32 v184, v184\n"
      "v_mfma_f32_16x16x32_bf16 v[96:99], a[96:99], v[152:155], v[96:99]\n"
      "buffer_load_dwordx4 v212, s[40:43], s68 offen nt lds\n"
      "v_mul_f32 v230, s61, v230\n"
      "v_mfma_f32_16x16x32_bf16 v[100:103], a[100:103], v[152:155], v[100:103]\n"
      "v_exp_f32 v185, v185\n"
      "v_mfma_f32_16x16x32_bf16 v[104:107], a[104:107], v[152:155], v[104:107]\n"
      "buffer_load_dwordx4 v213, s[40:43], s68 offen offset:1024 nt lds\n"
      "v_cvt_pk_bf16_f32 v184, v184, v185\n"
      "v_mfma_f32_16x16x32_bf16 v[108:111], a[108:111], v[152:155], v[108:111]\n"
      "v_exp_f32 v186, v186\n"
      "v_mfma_f32_16x16x32_bf16 v[112:115], a[112:115], v[152:155], v[112:115]\n"
      "v_cvt_pk_bf16_f32 v230, v221, v230\n"
      "v_accvgpr_write_b32 a46, v230\n"
      "v_mfma_f32_16x16x32_bf16 v[116:119], a[116:119], v[152:155], v[116:119]\n"
      "v_lshlrev_b32 v222, 16, v231\n"
      "v_and_b32 v231, s62, v231\n"
      "v_mfma_f32_16x16x32_bf16 v[120:123], a[120:123], v[152:155], v[120:123]\n"
      "v_mul_f32 v222, s61, v222\n"
      "v_mul_f32 v231, s61, v231\n"
      "v_mfma_f32_16x16x32_bf16 v[124:127], a[124:127], v[152:155], v[124:127]\n"
      "v_cvt_pk_bf16_f32 v231, v222, v231\n"
      "v_accvgpr_write_b32 a47, v231\n"
      "s_waitcnt lgkmcnt(0)\n"
      "v_mfma_f32_16x16x32_bf16 v[128:131], a[64:67], a[160:163], v[192:195]\n"
      "s_waitcnt vmcnt(12)\n"
      "s_barrier\n"
      "v_lshlrev_b32 v221, 16, v224\n"
      "v_and_b32 v224, s62, v224\n"
      "v_mul_f32 v221, s61, v221\n"
      "v_mfma_f32_16x16x32_bf16 v[128:131], a[68:71], a[164:167], v[128:131]\n"
      "v_exp_f32 v187, v187\n"
      "v_mfma_f32_16x16x32_bf16 v[128:131], a[72:75], a[168:171], v[128:131]\n"
      "ds_read_b128 a[152:155], v220 offset:19456\n"
      "v_cvt_pk_bf16_f32 v185, v186, v187\n"
      "v_mfma_f32_16x16x32_bf16 v[128:131], a[76:79], a[172:175], v[128:131]\n"
      "v_exp_f32 v188, v188\n"
      "v_mfma_f32_16x16x32_bf16 v[132:135], a[80:83], a[160:163], v[192:195]\n"
      "ds_read_b64_tr_b16 a[96:97], v210 offset:57344\n"
      "ds_read_b64_tr_b16 a[98:99], v210 offset:61440\n"
      "v_mfma_f32_16x16x32_bf16 v[132:135], a[84:87], a[164:167], v[132:135]\n"
      "v_exp_f32 v189, v189\n"
      "v_mfma_f32_16x16x32_bf16 v[132:135], a[88:91], a[168:171], v[132:135]\n"
      "ds_read_b64_tr_b16 a[100:101], v211 offset:57344\n"
      "v_cvt_pk_bf16_f32 v186, v188, v189\n"
      "v_mfma_f32_16x16x32_bf16 v[132:135], a[92:95], a[172:175], v[132:135]\n"
      "v_exp_f32 v190, v190\n"
      "v_mfma_f32_16x16x32_bf16 v[136:139], a[64:67], a[176:179], v[196:199]\n"
      "ds_read_b64_tr_b16 a[102:103], v211 offset:61440\n"
      "ds_read_b64_tr_b16 a[104:105], v210 offset:57856\n"
      "v_mfma_f32_16x16x32_bf16 v[136:139], a[68:71], a[180:183], v[136:139]\n"
      "v_exp_f32 v191, v191\n"
      "v_mfma_f32_16x16x32_bf16 v[136:139], a[72:75], a[184:187], v[136:139]\n"
      "ds_read_b64_tr_b16 a[106:107], v210 offset:61952\n"
      "v_cvt_pk_bf16_f32 v187, v190, v191\n"
      "v_mfma_f32_16x16x32_bf16 v[136:139], a[76:79], a[188:191], v[136:139]\n"
      "v_exp_f32 v128, v128\n"
      "v_mfma_f32_16x16x32_bf16 v[140:143], a[80:83], a[176:179], v[196:199]\n"
      "ds_read_b64_tr_b16 a[108:109], v211 offset:57856\n"
      "ds_read_b64_tr_b16 a[110:111], v211 offset:61952\n"
      "v_mfma_f32_16x16x32_bf16 v[140:143], a[84:87], a[180:183], v[140:143]\n"
      "v_exp_f32 v129, v129\n"
      "v_mfma_f32_16x16x32_bf16 v[140:143], a[88:91], a[184:187], v[140:143]\n"
      "ds_read_b64_tr_b16 a[112:113], v210 offset:58368\n"
      "v_cvt_pk_bf16_f32 v128, v128, v129\n"
      "v_mfma_f32_16x16x32_bf16 v[140:143], a[92:95], a[188:191], v[140:143]\n"
      "v_exp_f32 v130, v130\n"
      "v_mfma_f32_16x16x32_bf16 v[144:147], a[64:67], a[192:195], v[200:203]\n"
      "ds_read_b64_tr_b16 a[114:115], v210 offset:62464\n"
      "ds_read_b64_tr_b16 a[116:117], v211 offset:58368\n"
      "v_mfma_f32_16x16x32_bf16 v[144:147], a[68:71], a[196:199], v[144:147]\n"
      "v_exp_f32 v131, v131\n"
      "v_mfma_f32_16x16x32_bf16 v[144:147], a[72:75], a[200:203], v[144:147]\n"
      "ds_read_b64_tr_b16 a[118:119], v211 offset:62464\n"
      "v_cvt_pk_bf16_f32 v129, v130, v131\n"
      "v_mfma_f32_16x16x32_bf16 v[144:147], a[76:79], a[204:207], v[144:147]\n"
      "v_exp_f32 v132, v132\n"
      "v_mfma_f32_16x16x32_bf16 v[148:151], a[80:83], a[192:195], v[200:203]\n"
      "ds_read_b64_tr_b16 a[120:121], v210 offset:58880\n"
      "ds_read_b64_tr_b16 a[122:123], v210 offset:62976\n"
      "v_mfma_f32_16x16x32_bf16 v[148:151], a[84:87], a[196:199], v[148:151]\n"
      "v_exp_f32 v133, v133\n"
      "v_mfma_f32_16x16x32_bf16 v[148:151], a[88:91], a[200:203], v[148:151]\n"
      "ds_read_b64_tr_b16 a[124:125], v211 offset:58880\n"
      "v_cvt_pk_bf16_f32 v130, v132, v133\n"
      "v_mfma_f32_16x16x32_bf16 v[148:151], a[92:95], a[204:207], v[148:151]\n"
      "v_exp_f32 v134, v134\n"
      "v_mfma_f32_16x16x32_bf16 v[152:155], a[64:67], a[208:211], v[204:207]\n"
      "ds_read_b64_tr_b16 a[126:127], v211 offset:62976\n"
      "v_mul_f32 v224, s61, v224\n"
      "v_mfma_f32_16x16x32_bf16 v[152:155], a[68:71], a[212:215], v[152:155]\n"
      "v_exp_f32 v135, v135\n"
      "v_mfma_f32_16x16x32_bf16 v[152:155], a[72:75], a[216:219], v[152:155]\n"
      "v_cvt_pk_bf16_f32 v131, v134, v135\n"
      "v_cvt_pk_bf16_f32 v224, v221, v224\n"
      "v_mfma_f32_16x16x32_bf16 v[152:155], a[76:79], a[220:223], v[152:155]\n"
      "v_exp_f32 v136, v136\n"
      "v_mfma_f32_16x16x32_bf16 v[156:159], a[80:83], a[208:211], v[204:207]\n"
      "ds_read_b128 a[64:67], v208 offset:8192\n"
      "ds_read_b128 a[68:71], v208 offset:8704\n"
      "v_mfma_f32_16x16x32_bf16 v[156:159], a[84:87], a[212:215], v[156:159]\n"
      "v_exp_f32 v137, v137\n"
      "v_mfma_f32_16x16x32_bf16 v[156:159], a[88:91], a[216:219], v[156:159]\n"
      "ds_read_b128 a[72:75], v208 offset:9216\n"
      "v_cvt_pk_bf16_f32 v136, v136, v137\n"
      "v_mfma_f32_16x16x32_bf16 v[156:159], a[92:95], a[220:223], v[156:159]\n"
      "v_exp_f32 v138, v138\n"
      "v_mfma_f32_16x16x32_bf16 a[128:131], a[144:147], v[160:163], a[128:131]\n"
      "ds_read_b128 a[76:79], v208 offset:9728\n"
      "v_accvgpr_write_b32 a48, v224\n"
      "s_waitcnt lgkmcnt(12)\n"
      "v_mfma_f32_16x16x32_bf16 v[0:3], a[96:99], v[160:163], v[0:3]\n"
      "v_exp_f32 v139, v139\n"
      "v_mfma_f32_16x16x32_bf16 v[4:7], a[100:103], v[160:163], v[4:7]\n"
      "ds_read_b128 a[80:83], v208 offset:12288\n"
      "v_cvt_pk_bf16_f32 v137, v138, v139\n"
      "v_mfma_f32_16x16x32_bf16 v[8:11], a[104:107], v[160:163], v[8:11]\n"
      "v_exp_f32 v140, v140\n"
      "v_mfma_f32_16x16x32_bf16 v[12:15], a[108:111], v[160:163], v[12:15]\n"
      "ds_read_b128 a[84:87], v208 offset:12800\n"
      "ds_read_b128 a[88:91], v208 offset:13312\n"
      "v_lshlrev_b32 v222, 16, v225\n"
      "s_waitcnt lgkmcnt(7)\n"
      "v_mfma_f32_16x16x32_bf16 v[16:19], a[112:115], v[160:163], v[16:19]\n"
      "v_exp_f32 v141, v141\n"
      "v_mfma_f32_16x16x32_bf16 v[20:23], a[116:119], v[160:163], v[20:23]\n"
      "ds_read_b128 a[92:95], v208 offset:13824\n"
      "v_cvt_pk_bf16_f32 v138, v140, v141\n"
      "v_mfma_f32_16x16x32_bf16 v[24:27], a[120:123], v[160:163], v[24:27]\n"
      "v_exp_f32 v142, v142\n"
      "v_mfma_f32_16x16x32_bf16 v[28:31], a[124:127], v[160:163], v[28:31]\n"
      "s_mov_b32 m0, s58\n"
      "buffer_store_dwordx4 a[152:155], v214, s[52:55], s78 offen offset:3072 sc1\n"
      "v_mfma_f32_16x16x32_bf16 a[132:135], a[144:147], v[168:171], a[132:135]\n"
      "s_add_u32 s68, s57, 0x2000000\n"
      "v_exp_f32 v143, v143\n"
      "v_mfma_f32_16x16x32_bf16 v[32:35], a[96:99], v[168:171], v[32:35]\n"
      "buffer_load_dwordx4 v212, s[44:47], s68 offen nt lds\n"
      "v_cvt_pk_bf16_f32 v139, v142, v143\n"
      "v_mfma_f32_16x16x32_bf16 v[36:39], a[100:103], v[168:171], v[36:39]\n"
      "v_exp_f32 v144, v144\n"
      "v_mfma_f32_16x16x32_bf16 v[40:43], a[104:107], v[168:171], v[40:43]\n"
      "buffer_load_dwordx4 v213, s[44:47], s68 offen offset:1024 nt lds\n"
      "v_and_b32 v225, s62, v225\n"
      "v_mfma_f32_16x16x32_bf16 v[44:47], a[108:111], v[168:171], v[44:47]\n"
      "v_exp_f32 v145, v145\n"
      "v_mfma_f32_16x16x32_bf16 v[48:51], a[112:115], v[168:171], v[48:51]\n"
      "v_cvt_pk_bf16_f32 v144, v144, v145\n"
      "v_mul_f32 v222, s61, v222\n"
      "v_mfma_f32_16x16x32_bf16 v[52:55], a[116:119], v[168:171], v[52:55]\n"
      "v_exp_f32 v146, v146\n"
      "v_mfma_f32_16x16x32_bf16 v[56:59], a[120:123], v[168:171], v[56:59]\n"
      "v_mul_f32 v225, s61, v225\n"
      "v_cvt_pk_bf16_f32 v225, v222, v225\n"
      "v_mfma_f32_16x16x32_bf16 v[60:63], a[124:127], v[168:171], v[60:63]\n"
      "v_exp_f32 v147, v147\n"
      "v_mfma_f32_16x16x32_bf16 a[136:139], a[144:147], v[176:179], a[136:139]\n"
      "s_add_u32 m0, s58, 0xc000\n"
      "v_cvt_pk_bf16_f32 v145, v146, v147\n"
      "v_accvgpr_write_b32 a49, v225\n"
      "v_mfma_f32_16x16x32_bf16 v[64:67], a[96:99], v[176:179], v[64:67]\n"
      "s_add_u32 s67, s57, 0x1c000\n"
      "v_exp_f32 v148, v148\n"
      "v_mfma_f32_16x16x32_bf16 v[68:71], a[100:103], v[176:179], v[68:71]\n"
      "buffer_load_dwordx4 v212, s[48:51], s67 offen nt lds\n"
      "v_lshlrev_b32 v221, 16, v226\n"
      "v_mfma_f32_16x16x32_bf16 v[72:75], a[104:107], v[176:179], v[72:75]\n"
      "v_exp_f32 v149, v149\n"
      "v_mfma_f32_16x16x32_bf16 v[76:79], a[108:111], v[176:179], v[76:79]\n"
      "buffer_load_dwordx4 v213, s[48:51], s67 offen offset:1024 nt lds\n"
      "v_cvt_pk_bf16_f32 v146, v148, v149\n"
      "v_mfma_f32_16x16x32_bf16 v[80:83], a[112:115], v[176:179], v[80:83]\n"
      "v_exp_f32 v150, v150\n"
      "v_mfma_f32_16x16x32_bf16 v[84:87], a[116:119], v[176:179], v[84:87]\n"
      "ds_read_b128 v[228:231], v209 offset:4608\n"
      "v_and_b32 v226, s62, v226\n"
      "v_mfma_f32_16x16x32_bf16 v[88:91], a[120:123], v[176:179], v[88:91]\n"
      "v_exp_f32 v151, v151\n"
      "v_mfma_f32_16x16x32_bf16 v[92:95], a[124:127], v[176:179], v[92:95]\n"
      "s_add_u32 m0, s59, 0x800\n"
      "v_cvt_pk_bf16_f32 v147, v150, v151\n"
      "v_mul_f32 v221, s61, v221\n"
      "v_mfma_f32_16x16x32_bf16 a[140:143], a[144:147], v[184:187], a[140:143]\n"
      "s_add_u32 s67, s56, 0x2000800\n"
      "v_exp_f32 v152, v152\n"
      "v_mfma_f32_16x16x32_bf16 v[96:99], a[96:99], v[184:187], v[96:99]\n"
      "buffer_load_dwordx4 v212, s[40:43], s67 offen nt lds\n"
      "v_mul_f32 v226, s61, v226\n"
      "v_mfma_f32_16x16x32_bf16 v[100:103], a[100:103], v[184:187], v[100:103]\n"
      "v_exp_f32 v153, v153\n"
      "v_mfma_f32_16x16x32_bf16 v[104:107], a[104:107], v[184:187], v[104:107]\n"
      "buffer_load_dwordx4 v213, s[40:43], s67 offen offset:1024 nt lds\n"
      "v_cvt_pk_bf16_f32 v152, v152, v153\n"
      "v_mfma_f32_16x16x32_bf16 v[108:111], a[108:111], v[184:187], v[108:111]\n"
      "v_exp_f32 v154, v154\n"
      "v_mfma_f32_16x16x32_bf16 v[112:115], a[112:115], v[184:187], v[112:115]\n"
      "v_cvt_pk_bf16_f32 v226, v221, v226\n"
      "v_accvgpr_write_b32 a50, v226\n"
      "v_mfma_f32_16x16x32_bf16 v[116:119], a[116:119], v[184:187], v[116:119]\n"
      "v_lshlrev_b32 v222, 16, v227\n"
      "v_and_b32 v227, s62, v227\n"
      "v_mfma_f32_16x16x32_bf16 v[120:123], a[120:123], v[184:187], v[120:123]\n"
      "s_memrealtime s[84:85]\n"
      "v_mul_f32 v222, s61, v222\n"
      "v_mul_f32 v227, s61, v227\n"
      "v_mfma_f32_16x16x32_bf16 v[124:127], a[124:127], v[184:187], v[124:127]\n"
      "v_cvt_pk_bf16_f32 v227, v222, v227\n"
      "v_accvgpr_write_b32 a51, v227\n"
      "s_waitcnt lgkmcnt(0)\n"
      "s_sub_u32 s85, s84, s86\n"
      "s_cmp_ge_u32 s85, s87\n"
      "s_cbranch_scc1 L_PACED1_12\n"
      "L_PACE1_12:\n"
      "s_memrealtime s[84:85]\n"
      "s_waitcnt lgkmcnt(0)\n"
      "s_sub_u32 s85, s84, s86\n"
      "s_cmp_lt_u32 s85, s87\n"
      "s_cbranch_scc1 L_PACE1_12\n"
      "L_PACED1_12:\n"
      "s_mov_b32 s86, s84\n"
      "v_mfma_f32_16x16x32_bf16 v[160:163], a[64:67], a[160:163], v[192:195]\n"
      "s_waitcnt vmcnt(14)\n"
      "s_barrier\n"
      "v_lshlrev_b32 v221, 16, v228\n"
      "v_and_b32 v228, s62, v228\n"
      "v_mfma_f32_16x16x32_bf16 v[160:163], a[68:71], a[164:167], v[160:163]\n"
      "v_exp_f32 v155, v155\n"
      "v_mfma_f32_16x16x32_bf16 v[160:163], a[72:75], a[168:171], v[160:163]\n"
      "ds_read_b128 a[148:151], v219 offset:20480\n"
      "v_cvt_pk_bf16_f32 v153, v154, v155\n"
      "v_mfma_f32_16x16x32_bf16 v[160:163], a[76:79], a[172:175], v[160:163]\n"
      "v_exp_f32 v156, v156\n"
      "v_mfma_f32_16x16x32_bf16 v[164:167], a[80:83], a[160:163], v[192:195]\n"
      "ds_read_b64_tr_b16 a[96:97], v210 offset:32768\n"
      "ds_read_b64_tr_b16 a[98:99], v210 offset:36864\n"
      "v_mfma_f32_16x16x32_bf16 v[164:167], a[84:87], a[164:167], v[164:167]\n"
      "v_exp_f32 v157, v157\n"
      "v_mfma_f32_16x16x32_bf16 v[164:167], a[88:91], a[168:171], v[164:167]\n"
      "ds_read_b64_tr_b16 a[100:101], v211 offset:32768\n"
      "v_cvt_pk_bf16_f32 v154, v156, v157\n"
      "v_mfma_f32_16x16x32_bf16 v[164:167], a[92:95], a[172:175], v[164:167]\n"
      "v_exp_f32 v158, v158\n"
      "v_mfma_f32_16x16x32_bf16 v[168:171], a[64:67], a[176:179], v[196:199]\n"
      "ds_read_b64_tr_b16 a[102:103], v211 offset:36864\n"
      "ds_read_b64_tr_b16 a[104:105], v210 offset:33280\n"
      "v_mfma_f32_16x16x32_bf16 v[168:171], a[68:71], a[180:183], v[168:171]\n"
      "v_exp_f32 v159, v159\n"
      "v_mfma_f32_16x16x32_bf16 v[168:171], a[72:75], a[184:187], v[168:171]\n"
      "ds_read_b64_tr_b16 a[106:107], v210 offset:37376\n"
      "v_cvt_pk_bf16_f32 v155, v158, v159\n"
      "v_mfma_f32_16x16x32_bf16 v[168:171], a[76:79], a[188:191], v[168:171]\n"
      "v_exp_f32 v160, v160\n"
      "v_mfma_f32_16x16x32_bf16 v[172:175], a[80:83], a[176:179], v[196:199]\n"
      "ds_read_b64_tr_b16 a[108:109], v211 offset:33280\n"
      "ds_read_b64_tr_b16 a[110:111], v211 offset:37376\n"
      "v_mfma_f32_16x16x32_bf16 v[172:175], a[84:87], a[180:183], v[172:175]\n"
      "v_exp_f32 v161, v161\n"
      "v_mfma_f32_16x16x32_bf16 v[172:175], a[88:91], a[184:187], v[172:175]\n"
      "ds_read_b64_tr_b16 a[112:113], v210 offset:33792\n"
      "v_cvt_pk_bf16_f32 v160, v160, v161\n"
      "v_mfma_f32_16x16x32_bf16 v[172:175], a[92:95], a[188:191], v[172:175]\n"
      "v_exp_f32 v162, v162\n"
      "v_mfma_f32_16x16x32_bf16 v[176:179], a[64:67], a[192:195], v[200:203]\n"
      "ds_read_b64_tr_b16 a[114:115], v210 offset:37888\n"
      "ds_read_b64_tr_b16 a[116:117], v211 offset:33792\n"
      "v_mfma_f32_16x16x32_bf16 v[176:179], a[68:71], a[196:199], v[176:179]\n"
      "v_exp_f32 v163, v163\n"
      "v_mfma_f32_16x16x32_bf16 v[176:179], a[72:75], a[200:203], v[176:179]\n"
      "ds_read_b64_tr_b16 a[118:119], v211 offset:37888\n"
      "v_cvt_pk_bf16_f32 v161, v162, v163\n"
      "v_mfma_f32_16x16x32_bf16 v[176:179], a[76:79], a[204:207], v[176:179]\n"
      "v_exp_f32 v164, v164\n"
      "v_mfma_f32_16x16x32_bf16 v[180:183], a[80:83], a[192:195], v[200:203]\n"
      "ds_read_b64_tr_b16 a[120:121], v210 offset:34304\n"
      "ds_read_b64_tr_b16 a[122:123], v210 offset:38400\n"
      "v_mfma_f32_16x16x32_bf16 v[180:183], a[84:87], a[196:199], v[180:183]\n"
      "v_exp_f32 v165, v165\n"
      "v_mfma_f32_16x16x32_bf16 v[180:183], a[88:91], a[200:203], v[180:183]\n"
      "ds_read_b64_tr_b16 a[124:125], v211 offset:34304\n"
      "v_cvt_pk_bf16_f32 v162, v164, v165\n"
      "v_mfma_f32_16x16x32_bf16 v[180:183], a[92:95], a[204:207], v[180:183]\n"
      "v_exp_f32 v166, v166\n"
      "v_mfma_f32_16x16x32_bf16 v[184:187], a[64:67], a[208:211], v[204:207]\n"
      "ds_read_b64_tr_b16 a[126:127], v211 offset:38400\n"
      "v_mul_f32 v221, s61, v221\n"
      "v_mfma_f32_16x16x32_bf16 v[184:187], a[68:71], a[212:215], v[184:187]\n"
      "v_exp_f32 v167, v167\n"
      "v_mfma_f32_16x16x32_bf16 v[184:187], a[72:75], a[216:219], v[184:187]\n"
      "v_cvt_pk_bf16_f32 v163, v166, v167\n"
      "v_mul_f32 v228, s61, v228\n"
      "v_mfma_f32_16x16x32_bf16 v[184:187], a[76:79], a[220:223], v[184:187]\n"
      "v_exp_f32 v168, v168\n"
      "v_mfma_f32_16x16x32_bf16 v[188:191], a[80:83], a[208:211], v[204:207]\n"
      "ds_read_b128 a[64:67], v208 offset:16384\n"
      "ds_read_b128 a[68:71], v208 offset:16896\n"
      "v_mfma_f32_16x16x32_bf16 v[188:191], a[84:87], a[212:215], v[188:191]\n"
      "v_exp_f32 v169, v169\n"
      "v_mfma_f32_16x16x32_bf16 v[188:191], a[88:91], a[216:219], v[188:191]\n"
      "ds_read_b128 a[72:75], v208 offset:17408\n"
      "v_cvt_pk_bf16_f32 v168, v168, v169\n"
      "v_mfma_f32_16x16x32_bf16 v[188:191], a[92:95], a[220:223], v[188:191]\n"
      "v_exp_f32 v170, v170\n"
      "v_mfma_f32_16x16x32_bf16 a[128:131], a[144:147], v[128:131], a[128:131]\n"
      "ds_read_b128 a[76:79], v208 offset:17920\n"
      "v_cvt_pk_bf16_f32 v228, v221, v228\n"
      "s_waitcnt lgkmcnt(12)\n"
      "v_mfma_f32_16x16x32_bf16 v[0:3], a[96:99], v[128:131], v[0:3]\n"
      "v_exp_f32 v171, v171\n"
      "v_mfma_f32_16x16x32_bf16 v[4:7], a[100:103], v[128:131], v[4:7]\n"
      "ds_read_b128 a[80:83], v208 offset:20480\n"
      "v_cvt_pk_bf16_f32 v169, v170, v171\n"
      "v_mfma_f32_16x16x32_bf16 v[8:11], a[104:107], v[128:131], v[8:11]\n"
      "v_exp_f32 v172, v172\n"
      "v_mfma_f32_16x16x32_bf16 v[12:15], a[108:111], v[128:131], v[12:15]\n"
      "ds_read_b128 a[84:87], v208 offset:20992\n"
      "ds_read_b128 a[88:91], v208 offset:21504\n"
      "s_waitcnt lgkmcnt(7)\n"
      "v_mfma_f32_16x16x32_bf16 v[16:19], a[112:115], v[128:131], v[16:19]\n"
      "v_exp_f32 v173, v173\n"
      "v_mfma_f32_16x16x32_bf16 v[20:23], a[116:119], v[128:131], v[20:23]\n"
      "ds_read_b128 a[92:95], v208 offset:22016\n"
      "v_cvt_pk_bf16_f32 v170, v172, v173\n"
      "v_mfma_f32_16x16x32_bf16 v[24:27], a[120:123], v[128:131], v[24:27]\n"
      "v_exp_f32 v174, v174\n"
      "v_mfma_f32_16x16x32_bf16 v[28:31], a[124:127], v[128:131], v[28:31]\n"
      "buffer_store_dwordx4 a[148:151], v214, s[52:55], s79 offen offset:0 sc1\n"
      "v_mfma_f32_16x16x32_bf16 a[132:135], a[144:147], v[136:139], a[132:135]\n"
      "v_exp_f32 v175, v175\n"
      "v_mfma_f32_16x16x32_bf16 v[32:35], a[96:99], v[136:139], v[32:35]\n"
      "s_add_u32 m0, s58, 0x2000\n"
      "v_cvt_pk_bf16_f32 v171, v174, v175\n"
      "v_accvgpr_write_b32 a52, v228\n"
      "v_mfma_f32_16x16x32_bf16 v[36:39], a[100:103], v[136:139], v[36:39]\n"
      "s_add_u32 s68, s57, 0x2002000\n"
      "v_exp_f32 v176, v176\n"
      "v_mfma_f32_16x16x32_bf16 v[40:43], a[104:107], v[136:139], v[40:43]\n"
      "buffer_load_dwordx4 v212, s[44:47], s68 offen nt lds\n"
      "v_lshlrev_b32 v222, 16, v229\n"
      "v_mfma_f32_16x16x32_bf16 v[44:47], a[108:111], v[136:139], v[44:47]\n"
      "v_exp_f32 v177, v177\n"
      "v_mfma_f32_16x16x32_bf16 v[48:51], a[112:115], v[136:139], v[48:51]\n"
      "buffer_load_dwordx4 v213, s[44:47], s68 offen offset:1024 nt lds\n"
      "v_cvt_pk_bf16_f32 v176, v176, v177\n"
      "v_mfma_f32_16x16x32_bf16 v[52:55], a[116:119], v[136:139], v[52:55]\n"
      "v_exp_f32 v178, v178\n"
      "v_mfma_f32_16x16x32_bf16 v[56:59], a[120:123], v[136:139], v[56:59]\n"
      "v_and_b32 v229, s62, v229\n"
      "v_mul_f32 v222, s61, v222\n"
      "v_mfma_f32_16x16x32_bf16 v[60:63], a[124:127], v[136:139], v[60:63]\n"
      "v_exp_f32 v179, v179\n"
      "v_mfma_f32_16x16x32_bf16 a[136:139], a[144:147], v[144:147], a[136:139]\n"
      "v_cvt_pk_bf16_f32 v177, v178, v179\n"
      "v_mul_f32 v229, s61, v229\n"
      "v_mfma_f32_16x16x32_bf16 v[64:67], a[96:99], v[144:147], v[64:67]\n"
      "v_exp_f32 v180, v180\n"
      "v_mfma_f32_16x16x32_bf16 v[68:71], a[100:103], v[144:147], v[68:71]\n"
      "v_cvt_pk_bf16_f32 v229, v222, v229\n"
      "v_accvgpr_write_b32 a53, v229\n"
      "v_mfma_f32_16x16x32_bf16 v[72:75], a[104:107], v[144:147], v[72:75]\n"
      "v_exp_f32 v181, v181\n"
      "v_mfma_f32_16x16x32_bf16 v[76:79], a[108:111], v[144:147], v[76:79]\n"
      "v_cvt_pk_bf16_f32 v178, v180, v181\n"
      "v_lshlrev_b32 v221, 16, v230\n"
      "v_mfma_f32_16x16x32_bf16 v[80:83], a[112:115], v[144:147], v[80:83]\n"
      "v_exp_f32 v182, v182\n"
      "v_mfma_f32_16x16x32_bf16 v[84:87], a[116:119], v[144:147], v[84:87]\n"
      "s_add_u32 m0, s58, 0xe000\n"
      "ds_read_b128 v[224:227], v209 offset:5120\n"
      "v_and_b32 v230, s62, v230\n"
      "v_mfma_f32_16x16x32_bf16 v[88:91], a[120:123], v[144:147], v[88:91]\n"
      "s_add_u32 s67, s57, 0x1e000\n"
      "v_exp_f32 v183, v183\n"
      "v_mfma_f32_16x16x32_bf16 v[92:95], a[124:127], v[144:147], v[92:95]\n"
      "buffer_load_dwordx4 v212, s[48:51], s67 offen nt lds\n"
      "v_cvt_pk_bf16_f32 v179, v182, v183\n"
      "v_mfma_f32_16x16x32_bf16 a[140:143], a[144:147], v[152:155], a[140:143]\n"
      "v_exp_f32 v184, v184\n"
      "v_mfma_f32_16x16x32_bf16 v[96:99], a[96:99], v[152:155], v[96:99]\n"
      "buffer_load_dwordx4 v213, s[48:51], s67 offen offset:1024 nt lds\n"
      "v_mul_f32 v221, s61, v221\n"
      "v_mfma_f32_16x16x32_bf16 v[100:103], a[100:103], v[152:155], v[100:103]\n"
      "v_exp_f32 v185, v185\n"
      "v_mfma_f32_16x16x32_bf16 v[104:107], a[104:107], v[152:155], v[104:107]\n"
      "v_cvt_pk_bf16_f32 v184, v184, v185\n"
      "v_mul_f32 v230, s61, v230\n"
      "v_mfma_f32_16x16x32_bf16 v[108:111], a[108:111], v[152:155], v[108:111]\n"
      "v_exp_f32 v186, v186\n"
      "v_mfma_f32_16x16x32_bf16 v[112:115], a[112:115], v[152:155], v[112:115]\n"
      "v_cvt_pk_bf16_f32 v230, v221, v230\n"
      "v_accvgpr_write_b32 a54, v230\n"
      "v_mfma_f32_16x16x32_bf16 v[116:119], a[116:119], v[152:155], v[116:119]\n"
      "v_lshlrev_b32 v222, 16, v231\n"
      "v_and_b32 v231, s62, v231\n"
      "v_mfma_f32_16x16x32_bf16 v[120:123], a[120:123], v[152:155], v[120:123]\n"
      "v_mul_f32 v222, s61, v222\n"
      "v_mul_f32 v231, s61, v231\n"
      "v_mfma_f32_16x16x32_bf16 v[124:127], a[124:127], v[152:155], v[124:127]\n"
      "v_cvt_pk_bf16_f32 v231, v222, v231\n"
      "v_accvgpr_write_b32 a55, v231\n"
      "s_waitcnt lgkmcnt(0)\n"
      "v_mfma_f32_16x16x32_bf16 v[128:131], a[64:67], a[160:163], v[192:195]\n"
      "s_waitcnt vmcnt(14)\n"
      "s_barrier\n"
      "v_lshlrev_b32 v221, 16, v224\n"
      "v_and_b32 v224, s62, v224\n"
      "v_mfma_f32_16x16x32_bf16 v[128:131], a[68:71], a[164:167], v[128:131]\n"
      "v_exp_f32 v187, v187\n"
      "v_mfma_f32_16x16x32_bf16 v[128:131], a[72:75], a[168:171], v[128:131]\n"
      "ds_read_b128 a[152:155], v220 offset:21504\n"
      "v_cvt_pk_bf16_f32 v185, v186, v187\n"
      "v_mfma_f32_16x16x32_bf16 v[128:131], a[76:79], a[172:175], v[128:131]\n"
      "v_exp_f32 v188, v188\n"
      "v_mfma_f32_16x16x32_bf16 v[132:135], a[80:83], a[160:163], v[192:195]\n"
      "ds_read_b64_tr_b16 a[96:97], v210 offset:40960\n"
      "ds_read_b64_tr_b16 a[98:99], v210 offset:45056\n"
      "v_mfma_f32_16x16x32_bf16 v[132:135], a[84:87], a[164:167], v[132:135]\n"
      "v_exp_f32 v189, v189\n"
      "v_mfma_f32_16x16x32_bf16 v[132:135], a[88:91], a[168:171], v[132:135]\n"
      "ds_read_b64_tr_b16 a[100:101], v211 offset:40960\n"
      "v_cvt_pk_bf16_f32 v186, v188, v189\n"
      "v_mfma_f32_16x16x32_bf16 v[132:135], a[92:95], a[172:175], v[132:135]\n"
      "v_exp_f32 v190, v190\n"
      "v_mfma_f32_16x16x32_bf16 v[136:139], a[64:67], a[176:179], v[196:199]\n"
      "ds_read_b64_tr_b16 a[102:103], v211 offset:45056\n"
      "ds_read_b64_tr_b16 a[104:105], v210 offset:41472\n"
      "v_mfma_f32_16x16x32_bf16 v[136:139], a[68:71], a[180:183], v[136:139]\n"
      "v_exp_f32 v191, v191\n"
      "v_mfma_f32_16x16x32_bf16 v[136:139], a[72:75], a[184:187], v[136:139]\n"
      "ds_read_b64_tr_b16 a[106:107], v210 offset:45568\n"
      "v_cvt_pk_bf16_f32 v187, v190, v191\n"
      "v_mfma_f32_16x16x32_bf16 v[136:139], a[76:79], a[188:191], v[136:139]\n"
      "v_exp_f32 v128, v128\n"
      "v_mfma_f32_16x16x32_bf16 v[140:143], a[80:83], a[176:179], v[196:199]\n"
      "ds_read_b64_tr_b16 a[108:109], v211 offset:41472\n"
      "ds_read_b64_tr_b16 a[110:111], v211 offset:45568\n"
      "v_mfma_f32_16x16x32_bf16 v[140:143], a[84:87], a[180:183], v[140:143]\n"
      "v_exp_f32 v129, v129\n"
      "v_mfma_f32_16x16x32_bf16 v[140:143], a[88:91], a[184:187], v[140:143]\n"
      "ds_read_b64_tr_b16 a[112:113], v210 offset:41984\n"
      "v_cvt_pk_bf16_f32 v128, v128, v129\n"
      "v_mfma_f32_16x16x32_bf16 v[140:143], a[92:95], a[188:191], v[140:143]\n"
      "v_exp_f32 v130, v130\n"
      "v_mfma_f32_16x16x32_bf16 v[144:147], a[64:67], a[192:195], v[200:203]\n"
      "ds_read_b64_tr_b16 a[114:115], v210 offset:46080\n"
      "ds_read_b64_tr_b16 a[116:117], v211 offset:41984\n"
      "v_mfma_f32_16x16x32_bf16 v[144:147], a[68:71], a[196:199], v[144:147]\n"
      "v_exp_f32 v131, v131\n"
      "v_mfma_f32_16x16x32_bf16 v[144:147], a[72:75], a[200:203], v[144:147]\n"
      "ds_read_b64_tr_b16 a[118:119], v211 offset:46080\n"
      "v_cvt_pk_bf16_f32 v129, v130, v131\n"
      "v_mfma_f32_16x16x32_bf16 v[144:147], a[76:79], a[204:207], v[144:147]\n"
      "v_exp_f32 v132, v132\n"
      "v_mfma_f32_16x16x32_bf16 v[148:151], a[80:83], a[192:195], v[200:203]\n"
      "ds_read_b64_tr_b16 a[120:121], v210 offset:42496\n"
      "ds_read_b64_tr_b16 a[122:123], v210 offset:46592\n"
      "v_mfma_f32_16x16x32_bf16 v[148:151], a[84:87], a[196:199], v[148:151]\n"
      "v_exp_f32 v133, v133\n"
      "v_mfma_f32_16x16x32_bf16 v[148:151], a[88:91], a[200:203], v[148:151]\n"
      "ds_read_b64_tr_b16 a[124:125], v211 offset:42496\n"
      "v_cvt_pk_bf16_f32 v130, v132, v133\n"
      "v_mfma_f32_16x16x32_bf16 v[148:151], a[92:95], a[204:207], v[148:151]\n"
      "v_exp_f32 v134, v134\n"
      "v_mfma_f32_16x16x32_bf16 v[152:155], a[64:67], a[208:211], v[204:207]\n"
      "ds_read_b64_tr_b16 a[126:127], v211 offset:46592\n"
      "v_mul_f32 v221, s61, v221\n"
      "v_mfma_f32_16x16x32_bf16 v[152:155], a[68:71], a[212:215], v[152:155]\n"
      "v_exp_f32 v135, v135\n"
      "v_mfma_f32_16x16x32_bf16 v[152:155], a[72:75], a[216:219], v[152:155]\n"
      "v_cvt_pk_bf16_f32 v131, v134, v135\n"
      "v_mul_f32 v224, s61, v224\n"
      "v_mfma_f32_16x16x32_bf16 v[152:155], a[76:79], a[220:223], v[152:155]\n"
      "v_exp_f32 v136, v136\n"
      "v_mfma_f32_16x16x32_bf16 v[156:159], a[80:83], a[208:211], v[204:207]\n"
      "ds_read_b128 a[64:67], v208 offset:24576\n"
      "ds_read_b128 a[68:71], v208 offset:25088\n"
      "v_mfma_f32_16x16x32_bf16 v[156:159], a[84:87], a[212:215], v[156:159]\n"
      "v_exp_f32 v137, v137\n"
      "v_mfma_f32_16x16x32_bf16 v[156:159], a[88:91], a[216:219], v[156:159]\n"
      "ds_read_b128 a[72:75], v208 offset:25600\n"
      "v_cvt_pk_bf16_f32 v136, v136, v137\n"
      "v_mfma_f32_16x16x32_bf16 v[156:159], a[92:95], a[220:223], v[156:159]\n"
      "v_exp_f32 v138, v138\n"
      "v_mfma_f32_16x16x32_bf16 a[128:131], a[144:147], v[160:163], a[128:131]\n"
      "ds_read_b128 a[76:79], v208 offset:26112\n"
      "v_cvt_pk_bf16_f32 v224, v221, v224\n"
      "s_waitcnt lgkmcnt(12)\n"
      "v_mfma_f32_16x16x32_bf16 v[0:3], a[96:99], v[160:163], v[0:3]\n"
      "v_exp_f32 v139, v139\n"
      "v_mfma_f32_16x16x32_bf16 v[4:7], a[100:103], v[160:163], v[4:7]\n"
      "ds_read_b128 a[80:83], v208 offset:28672\n"
      "v_cvt_pk_bf16_f32 v137, v138, v139\n"
      "v_mfma_f32_16x16x32_bf16 v[8:11], a[104:107], v[160:163], v[8:11]\n"
      "v_exp_f32 v140, v140\n"
      "v_mfma_f32_16x16x32_bf16 v[12:15], a[108:111], v[160:163], v[12:15]\n"
      "ds_read_b128 a[84:87], v208 offset:29184\n"
      "ds_read_b128 a[88:91], v208 offset:29696\n"
      "s_waitcnt lgkmcnt(7)\n"
      "v_mfma_f32_16x16x32_bf16 v[16:19], a[112:115], v[160:163], v[16:19]\n"
      "v_exp_f32 v141, v141\n"
      "v_mfma_f32_16x16x32_bf16 v[20:23], a[116:119], v[160:163], v[20:23]\n"
      "ds_read_b128 a[92:95], v208 offset:30208\n"
      "v_cvt_pk_bf16_f32 v138, v140, v141\n"
      "v_mfma_f32_16x16x32_bf16 v[24:27], a[120:123], v[160:163], v[24:27]\n"
      "v_exp_f32 v142, v142\n"
      "v_mfma_f32_16x16x32_bf16 v[28:31], a[124:127], v[160:163], v[28:31]\n"
      "buffer_store_dwordx4 a[152:155], v214, s[52:55], s79 offen offset:1024 sc1\n"
      "v_mfma_f32_16x16x32_bf16 a[132:135], a[144:147], v[168:171], a[132:135]\n"
      "v_exp_f32 v143, v143\n"
      "v_mfma_f32_16x16x32_bf16 v[32:35], a[96:99], v[168:171], v[32:35]\n"
      "s_add_u32 m0, s58, 0x4000\n"
      "v_cvt_pk_bf16_f32 v139, v142, v143\n"
      "v_accvgpr_write_b32 a56, v224\n"
      "v_mfma_f32_16x16x32_bf16 v[36:39], a[100:103], v[168:171], v[36:39]\n"
      "s_add_u32 s68, s57, 0x2004000\n"
      "v_exp_f32 v144, v144\n"
      "v_mfma_f32_16x16x32_bf16 v[40:43], a[104:107], v[168:171], v[40:43]\n"
      "buffer_load_dwordx4 v212, s[44:47], s68 offen nt lds\n"
      "v_lshlrev_b32 v222, 16, v225\n"
      "v_mfma_f32_16x16x32_bf16 v[44:47], a[108:111], v[168:171], v[44:47]\n"
      "v_exp_f32 v145, v145\n"
      "v_mfma_f32_16x16x32_bf16 v[48:51], a[112:115], v[168:171], v[48:51]\n"
      "buffer_load_dwordx4 v213, s[44:47], s68 offen offset:1024 nt lds\n"
      "v_cvt_pk_bf16_f32 v144, v144, v145\n"
      "v_mfma_f32_16x16x32_bf16 v[52:55], a[116:119], v[168:171], v[52:55]\n"
      "v_exp_f32 v146, v146\n"
      "v_mfma_f32_16x16x32_bf16 v[56:59], a[120:123], v[168:171], v[56:59]\n"
      "v_and_b32 v225, s62, v225\n"
      "v_mul_f32 v222, s61, v222\n"
      "v_mfma_f32_16x16x32_bf16 v[60:63], a[124:127], v[168:171], v[60:63]\n"
      "v_exp_f32 v147, v147\n"
      "v_mfma_f32_16x16x32_bf16 a[136:139], a[144:147], v[176:179], a[136:139]\n"
      "v_cvt_pk_bf16_f32 v145, v146, v147\n"
      "v_mul_f32 v225, s61, v225\n"
      "v_mfma_f32_16x16x32_bf16 v[64:67], a[96:99], v[176:179], v[64:67]\n"
      "v_exp_f32 v148, v148\n"
      "v_mfma_f32_16x16x32_bf16 v[68:71], a[100:103], v[176:179], v[68:71]\n"
      "v_cvt_pk_bf16_f32 v225, v222, v225\n"
      "v_accvgpr_write_b32 a57, v225\n"
      "v_mfma_f32_16x16x32_bf16 v[72:75], a[104:107], v[176:179], v[72:75]\n"
      "v_exp_f32 v149, v149\n"
      "v_mfma_f32_16x16x32_bf16 v[76:79], a[108:111], v[176:179], v[76:79]\n"
      "v_cvt_pk_bf16_f32 v146, v148, v149\n"
      "v_lshlrev_b32 v221, 16, v226\n"
      "v_mfma_f32_16x16x32_bf16 v[80:83], a[112:115], v[176:179], v[80:83]\n"
      "v_exp_f32 v150, v150\n"
      "v_mfma_f32_16x16x32_bf16 v[84:87], a[116:119], v[176:179], v[84:87]\n"
      "s_add_u32 m0, s58, 0x8000\n"
      "ds_read_b128 v[228:231], v209 offset:5632\n"
      "v_and_b32 v226, s62, v226\n"
      "v_mfma_f32_16x16x32_bf16 v[88:91], a[120:123], v[176:179], v[88:91]\n"
      "s_add_u32 s67, s57, 0x2000000\n"
      "v_exp_f32 v151, v151\n"
      "v_mfma_f32_16x16x32_bf16 v[92:95], a[124:127], v[176:179], v[92:95]\n"
      "buffer_load_dwordx4 v212, s[48:51], s67 offen nt lds\n"
      "v_cvt_pk_bf16_f32 v147, v150, v151\n"
      "v_mfma_f32_16x16x32_bf16 a[140:143], a[144:147], v[184:187], a[140:143]\n"
      "v_exp_f32 v152, v152\n"
      "v_mfma_f32_16x16x32_bf16 v[96:99], a[96:99], v[184:187], v[96:99]\n"
      "buffer_load_dwordx4 v213, s[48:51], s67 offen offset:1024 nt lds\n"
      "v_mul_f32 v221, s61, v221\n"
      "v_mfma_f32_16x16x32_bf16 v[100:103], a[100:103], v[184:187], v[100:103]\n"
      "v_exp_f32 v153, v153\n"
      "v_mfma_f32_16x16x32_bf16 v[104:107], a[104:107], v[184:187], v[104:107]\n"
      "v_cvt_pk_bf16_f32 v152, v152, v153\n"
      "v_mul_f32 v226, s61, v226\n"
      "v_mfma_f32_16x16x32_bf16 v[108:111], a[108:111], v[184:187], v[108:111]\n"
      "v_exp_f32 v154, v154\n"
      "v_mfma_f32_16x16x32_bf16 v[112:115], a[112:115], v[184:187], v[112:115]\n"
      "v_cvt_pk_bf16_f32 v226, v221, v226\n"
      "v_accvgpr_write_b32 a58, v226\n"
      "v_mfma_f32_16x16x32_bf16 v[116:119], a[116:119], v[184:187], v[116:119]\n"
      "v_lshlrev_b32 v222, 16, v227\n"
      "v_and_b32 v227, s62, v227\n"
      "v_mfma_f32_16x16x32_bf16 v[120:123], a[120:123], v[184:187], v[120:123]\n"
      "s_memrealtime s[84:85]\n"
      "v_mul_f32 v222, s61, v222\n"
      "v_mul_f32 v227, s61, v227\n"
      "v_mfma_f32_16x16x32_bf16 v[124:127], a[124:127], v[184:187], v[124:127]\n"
      "v_cvt_pk_bf16_f32 v227, v222, v227\n"
      "v_accvgpr_write_b32 a59, v227\n"
      "s_waitcnt lgkmcnt(0)\n"
      "s_sub_u32 s85, s84, s86\n"
      "s_cmp_ge_u32 s85, s87\n"
      "s_cbranch_scc1 L_PACED1_14\n"
      "L_PACE1_14:\n"
      "s_memrealtime s[84:85]\n"
      "s_waitcnt lgkmcnt(0)\n"
      "s_sub_u32 s85, s84, s86\n"
      "s_cmp_lt_u32 s85, s87\n"
      "s_cbranch_scc1 L_PACE1_14\n"
      "L_PACED1_14:\n"
      "s_mov_b32 s86, s84\n"
      "v_mfma_f32_16x16x32_bf16 v[160:163], a[64:67], a[160:163], v[192:195]\n"
      "s_waitcnt vmcnt(12)\n"
      "s_barrier\n"
      "v_lshlrev_b32 v221, 16, v228\n"
      "v_and_b32 v228, s62, v228\n"
      "v_mul_f32 v221, s61, v221\n"
      "v_mfma_f32_16x16x32_bf16 v[160:163], a[68:71], a[164:167], v[160:163]\n"
      "v_exp_f32 v155, v155\n"
      "v_mfma_f32_16x16x32_bf16 v[160:163], a[72:75], a[168:171], v[160:163]\n"
      "ds_read_b128 a[148:151], v219 offset:22528\n"
      "v_cvt_pk_bf16_f32 v153, v154, v155\n"
      "v_mfma_f32_16x16x32_bf16 v[160:163], a[76:79], a[172:175], v[160:163]\n"
      "v_exp_f32 v156, v156\n"
      "v_mfma_f32_16x16x32_bf16 v[164:167], a[80:83], a[160:163], v[192:195]\n"
      "ds_read_b64_tr_b16 a[96:97], v210 offset:49152\n"
      "ds_read_b64_tr_b16 a[98:99], v210 offset:53248\n"
      "v_mfma_f32_16x16x32_bf16 v[164:167], a[84:87], a[164:167], v[164:167]\n"
      "v_exp_f32 v157, v157\n"
      "v_mfma_f32_16x16x32_bf16 v[164:167], a[88:91], a[168:171], v[164:167]\n"
      "ds_read_b64_tr_b16 a[100:101], v211 offset:49152\n"
      "v_cvt_pk_bf16_f32 v154, v156, v157\n"
      "v_mfma_f32_16x16x32_bf16 v[164:167], a[92:95], a[172:175], v[164:167]\n"
      "v_exp_f32 v158, v158\n"
      "v_mfma_f32_16x16x32_bf16 v[168:171], a[64:67], a[176:179], v[196:199]\n"
      "ds_read_b64_tr_b16 a[102:103], v211 offset:53248\n"
      "ds_read_b64_tr_b16 a[104:105], v210 offset:49664\n"
      "v_mfma_f32_16x16x32_bf16 v[168:171], a[68:71], a[180:183], v[168:171]\n"
      "v_exp_f32 v159, v159\n"
      "v_mfma_f32_16x16x32_bf16 v[168:171], a[72:75], a[184:187], v[168:171]\n"
      "ds_read_b64_tr_b16 a[106:107], v210 offset:53760\n"
      "v_cvt_pk_bf16_f32 v155, v158, v159\n"
      "v_mfma_f32_16x16x32_bf16 v[168:171], a[76:79], a[188:191], v[168:171]\n"
      "v_exp_f32 v160, v160\n"
      "v_mfma_f32_16x16x32_bf16 v[172:175], a[80:83], a[176:179], v[196:199]\n"
      "ds_read_b64_tr_b16 a[108:109], v211 offset:49664\n"
      "ds_read_b64_tr_b16 a[110:111], v211 offset:53760\n"
      "v_mfma_f32_16x16x32_bf16 v[172:175], a[84:87], a[180:183], v[172:175]\n"
      "v_exp_f32 v161, v161\n"
      "v_mfma_f32_16x16x32_bf16 v[172:175], a[88:91], a[184:187], v[172:175]\n"
      "ds_read_b64_tr_b16 a[112:113], v210 offset:50176\n"
      "v_cvt_pk_bf16_f32 v160, v160, v161\n"
      "v_mfma_f32_16x16x32_bf16 v[172:175], a[92:95], a[188:191], v[172:175]\n"
      "v_exp_f32 v162, v162\n"
      "v_mfma_f32_16x16x32_bf16 v[176:179], a[64:67], a[192:195], v[200:203]\n"
      "ds_read_b64_tr_b16 a[114:115], v210 offset:54272\n"
      "ds_read_b64_tr_b16 a[116:117], v211 offset:50176\n"
      "v_mfma_f32_16x16x32_bf16 v[176:179], a[68:71], a[196:199], v[176:179]\n"
      "v_exp_f32 v163, v163\n"
      "v_mfma_f32_16x16x32_bf16 v[176:179], a[72:75], a[200:203], v[176:179]\n"
      "ds_read_b64_tr_b16 a[118:119], v211 offset:54272\n"
      "v_cvt_pk_bf16_f32 v161, v162, v163\n"
      "v_mfma_f32_16x16x32_bf16 v[176:179], a[76:79], a[204:207], v[176:179]\n"
      "v_exp_f32 v164, v164\n"
      "v_mfma_f32_16x16x32_bf16 v[180:183], a[80:83], a[192:195], v[200:203]\n"
      "ds_read_b64_tr_b16 a[120:121], v210 offset:50688\n"
      "ds_read_b64_tr_b16 a[122:123], v210 offset:54784\n"
      "v_mfma_f32_16x16x32_bf16 v[180:183], a[84:87], a[196:199], v[180:183]\n"
      "v_exp_f32 v165, v165\n"
      "v_mfma_f32_16x16x32_bf16 v[180:183], a[88:91], a[200:203], v[180:183]\n"
      "ds_read_b64_tr_b16 a[124:125], v211 offset:50688\n"
      "v_cvt_pk_bf16_f32 v162, v164, v165\n"
      "v_mfma_f32_16x16x32_bf16 v[180:183], a[92:95], a[204:207], v[180:183]\n"
      "v_exp_f32 v166, v166\n"
      "v_mfma_f32_16x16x32_bf16 v[184:187], a[64:67], a[208:211], v[204:207]\n"
      "ds_read_b64_tr_b16 a[126:127], v211 offset:54784\n"
      "v_mul_f32 v228, s61, v228\n"
      "v_mfma_f32_16x16x32_bf16 v[184:187], a[68:71], a[212:215], v[184:187]\n"
      "v_exp_f32 v167, v167\n"
      "v_mfma_f32_16x16x32_bf16 v[184:187], a[72:75], a[216:219], v[184:187]\n"
      "v_cvt_pk_bf16_f32 v163, v166, v167\n"
      "v_cvt_pk_bf16_f32 v228, v221, v228\n"
      "v_mfma_f32_16x16x32_bf16 v[184:187], a[76:79], a[220:223], v[184:187]\n"
      "v_exp_f32 v168, v168\n"
      "v_mfma_f32_16x16x32_bf16 v[188:191], a[80:83], a[208:211], v[204:207]\n"
      "ds_read_b128 a[64:67], v208 offset:0\n"
      "ds_read_b128 a[68:71], v208 offset:512\n"
      "v_mfma_f32_16x16x32_bf16 v[188:191], a[84:87], a[212:215], v[188:191]\n"
      "v_exp_f32 v169, v169\n"
      "v_mfma_f32_16x16x32_bf16 v[188:191], a[88:91], a[216:219], v[188:191]\n"
      "ds_read_b128 a[72:75], v208 offset:1024\n"
      "v_cvt_pk_bf16_f32 v168, v168, v169\n"
      "v_mfma_f32_16x16x32_bf16 v[188:191], a[92:95], a[220:223], v[188:191]\n"
      "v_exp_f32 v170, v170\n"
      "v_mfma_f32_16x16x32_bf16 a[128:131], a[144:147], v[128:131], a[128:131]\n"
      "ds_read_b128 a[76:79], v208 offset:1536\n"
      "v_accvgpr_write_b32 a60, v228\n"
      "s_waitcnt lgkmcnt(12)\n"
      "v_mfma_f32_16x16x32_bf16 v[0:3], a[96:99], v[128:131], v[0:3]\n"
      "v_exp_f32 v171, v171\n"
      "v_mfma_f32_16x16x32_bf16 v[4:7], a[100:103], v[128:131], v[4:7]\n"
      "ds_read_b128 a[80:83], v208 offset:4096\n"
      "v_cvt_pk_bf16_f32 v169, v170, v171\n"
      "v_mfma_f32_16x16x32_bf16 v[8:11], a[104:107], v[128:131], v[8:11]\n"
      "v_exp_f32 v172, v172\n"
      "v_mfma_f32_16x16x32_bf16 v[12:15], a[108:111], v[128:131], v[12:15]\n"
      "ds_read_b128 a[84:87], v208 offset:4608\n"
      "ds_read_b128 a[88:91], v208 offset:5120\n"
      "s_waitcnt lgkmcnt(7)\n"
      "v_mfma_f32_16x16x32_bf16 v[16:19], a[112:115], v[128:131], v[16:19]\n"
      "v_exp_f32 v173, v173\n"
      "v_mfma_f32_16x16x32_bf16 v[20:23], a[116:119], v[128:131], v[20:23]\n"
      "ds_read_b128 a[92:95], v208 offset:5632\n"
      "v_cvt_pk_bf16_f32 v170, v172, v173\n"
      "v_mfma_f32_16x16x32_bf16 v[24:27], a[120:123], v[128:131], v[24:27]\n"
      "v_exp_f32 v174, v174\n"
      "v_mfma_f32_16x16x32_bf16 v[28:31], a[124:127], v[128:131], v[28:31]\n"
      "s_add_u32 m0, s58, 0x6000\n"
      "buffer_store_dwordx4 a[148:151], v214, s[52:55], s79 offen offset:2048 sc1\n"
      "v_mfma_f32_16x16x32_bf16 a[132:135], a[144:147], v[136:139], a[132:135]\n"
      "s_add_u32 s67, s57, 0x2006000\n"
      "v_exp_f32 v175, v175\n"
      "v_mfma_f32_16x16x32_bf16 v[32:35], a[96:99], v[136:139], v[32:35]\n"
      "buffer_load_dwordx4 v212, s[44:47], s67 offen nt lds\n"
      "v_cvt_pk_bf16_f32 v171, v174, v175\n"
      "v_mfma_f32_16x16x32_bf16 v[36:39], a[100:103], v[136:139], v[36:39]\n"
      "v_exp_f32 v176, v176\n"
      "v_mfma_f32_16x16x32_bf16 v[40:43], a[104:107], v[136:139], v[40:43]\n"
      "buffer_load_dwordx4 v213, s[44:47], s67 offen offset:1024 nt lds\n"
      "v_lshlrev_b32 v222, 16, v229\n"
      "v_mfma_f32_16x16x32_bf16 v[44:47], a[108:111], v[136:139], v[44:47]\n"
      "v_exp_f32 v177, v177\n"
      "v_mfma_f32_16x16x32_bf16 v[48:51], a[112:115], v[136:139], v[48:51]\n"
      "v_cvt_pk_bf16_f32 v176, v176, v177\n"
      "v_and_b32 v229, s62, v229\n"
      "v_mfma_f32_16x16x32_bf16 v[52:55], a[116:119], v[136:139], v[52:55]\n"
      "v_exp_f32 v178, v178\n"
      "v_mfma_f32_16x16x32_bf16 v[56:59], a[120:123], v[136:139], v[56:59]\n"
      "v_mul_f32 v222, s61, v222\n"
      "v_mul_f32 v229, s61, v229\n"
      "v_mfma_f32_16x16x32_bf16 v[60:63], a[124:127], v[136:139], v[60:63]\n"
      "v_exp_f32 v179, v179\n"
      "v_mfma_f32_16x16x32_bf16 a[136:139], a[144:147], v[144:147], a[136:139]\n"
      "s_add_u32 m0, s58, 0xa000\n"
      "v_cvt_pk_bf16_f32 v177, v178, v179\n"
      "v_cvt_pk_bf16_f32 v229, v222, v229\n"
      "v_mfma_f32_16x16x32_bf16 v[64:67], a[96:99], v[144:147], v[64:67]\n"
      "s_add_u32 s68, s57, 0x2002000\n"
      "v_exp_f32 v180, v180\n"
      "v_mfma_f32_16x16x32_bf16 v[68:71], a[100:103], v[144:147], v[68:71]\n"
      "buffer_load_dwordx4 v212, s[48:51], s68 offen nt lds\n"
      "v_accvgpr_write_b32 a61, v229\n"
      "v_mfma_f32_16x16x32_bf16 v[72:75], a[104:107], v[144:147], v[72:75]\n"
      "v_exp_f32 v181, v181\n"
      "v_mfma_f32_16x16x32_bf16 v[76:79], a[108:111], v[144:147], v[76:79]\n"
      "buffer_load_dwordx4 v213, s[48:51], s68 offen offset:1024 nt lds\n"
      "v_cvt_pk_bf16_f32 v178, v180, v181\n"
      "v_mfma_f32_16x16x32_bf16 v[80:83], a[112:115], v[144:147], v[80:83]\n"
      "v_exp_f32 v182, v182\n"
      "v_mfma_f32_16x16x32_bf16 v[84:87], a[116:119], v[144:147], v[84:87]\n"
      "v_lshlrev_b32 v221, 16, v230\n"
      "v_and_b32 v230, s62, v230\n"
      "v_mfma_f32_16x16x32_bf16 v[88:91], a[120:123], v[144:147], v[88:91]\n"
      "v_exp_f32 v183, v183\n"
      "v_mfma_f32_16x16x32_bf16 v[92:95], a[124:127], v[144:147], v[92:95]\n"
      "s_add_u32 m0, s59, 0x1000\n"
      "v_cvt_pk_bf16_f32 v179, v182, v183\n"
      "v_mul_f32 v221, s61, v221\n"
      "v_mfma_f32_16x16x32_bf16 a[140:143], a[144:147], v[152:155], a[140:143]\n"
      "s_add_u32 s68, s56, 0x2001000\n"
      "v_exp_f32 v184, v184\n"
      "v_mfma_f32_16x16x32_bf16 v[96:99], a[96:99], v[152:155], v[96:99]\n"
      "buffer_load_dwordx4 v212, s[40:43], s68 offen nt lds\n"
      "v_mul_f32 v230, s61, v230\n"
      "v_mfma_f32_16x16x32_bf16 v[100:103], a[100:103], v[152:155], v[100:103]\n"
      "v_exp_f32 v185, v185\n"
      "v_mfma_f32_16x16x32_bf16 v[104:107], a[104:107], v[152:155], v[104:107]\n"
      "buffer_load_dwordx4 v213, s[40:43], s68 offen offset:1024 nt lds\n"
      "v_cvt_pk_bf16_f32 v184, v184, v185\n"
      "v_mfma_f32_16x16x32_bf16 v[108:111], a[108:111], v[152:155], v[108:111]\n"
      "v_exp_f32 v186, v186\n"
      "v_mfma_f32_16x16x32_bf16 v[112:115], a[112:115], v[152:155], v[112:115]\n"
      "v_cvt_pk_bf16_f32 v230, v221, v230\n"
      "v_accvgpr_write_b32 a62, v230\n"
      "v_mfma_f32_16x16x32_bf16 v[116:119], a[116:119], v[152:155], v[116:119]\n"
      "v_lshlrev_b32 v222, 16, v231\n"
      "v_and_b32 v231, s62, v231\n"
      "v_mfma_f32_16x16x32_bf16 v[120:123], a[120:123], v[152:155], v[120:123]\n"
      "v_mul_f32 v222, s61, v222\n"
      "v_mul_f32 v231, s61, v231\n"
      "v_mfma_f32_16x16x32_bf16 v[124:127], a[124:127], v[152:155], v[124:127]\n"
      "v_cvt_pk_bf16_f32 v231, v222, v231\n"
      "v_accvgpr_write_b32 a63, v231\n"
      "s_cmp_eq_u32 s66, 1\n"
      "s_cbranch_scc1 L_TAIL\n"
      "s_waitcnt lgkmcnt(0)\n"
      "v_mfma_f32_16x16x32_bf16 v[128:131], a[64:67], a[0:3], 0\n"
      "s_waitcnt vmcnt(12)\n"
      "s_barrier\n"
      "v_mfma_f32_16x16x32_bf16 v[128:131], a[68:71], a[4:7], v[128:131]\n"
      "v_exp_f32 v187, v187\n"
      "v_mfma_f32_16x16x32_bf16 v[128:131], a[72:75], a[8:11], v[128:131]\n"
      "ds_read_b128 a[152:155], v220 offset:23552\n"
      "v_cvt_pk_bf16_f32 v185, v186, v187\n"
      "v_mfma_f32_16x16x32_bf16 v[128:131], a[76:79], a[12:15], v[128:131]\n"
      "v_exp_f32 v188, v188\n"
      "v_mfma_f32_16x16x32_bf16 v[132:135], a[80:83], a[0:3], 0\n"
      "ds_read_b64_tr_b16 a[96:97], v210 offset:57344\n"
      "ds_read_b64_tr_b16 a[98:99], v210 offset:61440\n"
      "v_mfma_f32_16x16x32_bf16 v[132:135], a[84:87], a[4:7], v[132:135]\n"
      "v_exp_f32 v189, v189\n"
      "v_mfma_f32_16x16x32_bf16 v[132:135], a[88:91], a[8:11], v[132:135]\n"
      "ds_read_b64_tr_b16 a[100:101], v211 offset:57344\n"
      "v_cvt_pk_bf16_f32 v186, v188, v189\n"
      "v_mfma_f32_16x16x32_bf16 v[132:135], a[92:95], a[12:15], v[132:135]\n"
      "v_exp_f32 v190, v190\n"
      "v_mfma_f32_16x16x32_bf16 v[136:139], a[64:67], a[16:19], 0\n"
      "ds_read_b64_tr_b16 a[102:103], v211 offset:61440\n"
      "ds_read_b64_tr_b16 a[104:105], v210 offset:57856\n"
      "v_mfma_f32_16x16x32_bf16 v[136:139], a[68:71], a[20:23], v[136:139]\n"
      "v_exp_f32 v191, v191\n"
      "v_mfma_f32_16x16x32_bf16 v[136:139], a[72:75], a[24:27], v[136:139]\n"
      "ds_read_b64_tr_b16 a[106:107], v210 offset:61952\n"
      "v_cvt_pk_bf16_f32 v187, v190, v191\n"
      "v_mfma_f32_16x16x32_bf16 v[136:139], a[76:79], a[28:31], v[136:139]\n"
      "ds_read_b64_tr_b16 a[108:109], v211 offset:57856\n"
      "ds_read_b64_tr_b16 a[110:111], v211 offset:61952\n"
      "v_mfma_f32_16x16x32_bf16 v[140:143], a[80:83], a[16:19], 0\n"
      "ds_read_b64_tr_b16 a[112:113], v210 offset:58368\n"
      "ds_read_b64_tr_b16 a[114:115], v210 offset:62464\n"
      "v_mfma_f32_16x16x32_bf16 v[140:143], a[84:87], a[20:23], v[140:143]\n"
      "ds_read_b64_tr_b16 a[116:117], v211 offset:58368\n"
      "ds_read_b64_tr_b16 a[118:119], v211 offset:62464\n"
      "v_mfma_f32_16x16x32_bf16 v[140:143], a[88:91], a[24:27], v[140:143]\n"
      "ds_read_b64_tr_b16 a[120:121], v210 offset:58880\n"
      "ds_read_b64_tr_b16 a[122:123], v210 offset:62976\n"
      "v_mfma_f32_16x16x32_bf16 v[140:143], a[92:95], a[28:31], v[140:143]\n"
      "ds_read_b64_tr_b16 a[124:125], v211 offset:58880\n"
      "ds_read_b64_tr_b16 a[126:127], v211 offset:62976\n"
      "v_mfma_f32_16x16x32_bf16 v[144:147], a[64:67], a[32:35], 0\n"
      "v_mfma_f32_16x16x32_bf16 v[144:147], a[68:71], a[36:39], v[144:147]\n"
      "v_mfma_f32_16x16x32_bf16 v[144:147], a[72:75], a[40:43], v[144:147]\n"
      "v_mfma_f32_16x16x32_bf16 v[144:147], a[76:79], a[44:47], v[144:147]\n"
      "v_max3_f32 v232, v128, v129, v130\n"
      "v_max3_f32 v233, v131, v132, v133\n"
      "v_mfma_f32_16x16x32_bf16 v[148:151], a[80:83], a[32:35], 0\n"
      "v_max3_f32 v232, v232, v134, v135\n"
      "v_max_f32 v232, v232, v233\n"
      "v_mfma_f32_16x16x32_bf16 v[148:151], a[84:87], a[36:39], v[148:151]\n"
      "v_max3_f32 v234, v136, v137, v138\n"
      "v_max3_f32 v235, v139, v140, v141\n"
      "v_mfma_f32_16x16x32_bf16 v[148:151], a[88:91], a[40:43], v[148:151]\n"
      "v_max3_f32 v234, v234, v142, v143\n"
      "v_max_f32 v234, v234, v235\n"
      "v_mfma_f32_16x16x32_bf16 v[148:151], a[92:95], a[44:47], v[148:151]\n"
      "v_mov_b32 v233, v232\n"
      "v_mov_b32 v235, v234\n"
      "v_mfma_f32_16x16x32_bf16 v[152:155], a[64:67], a[48:51], 0\n"
      "v_permlane32_swap_b32 v232, v233\n"
      "v_permlane32_swap_b32 v234, v235\n"
      "v_mfma_f32_16x16x32_bf16 v[152:155], a[68:71], a[52:55], v[152:155]\n"
      "v_max_f32 v232, v232, v233\n"
      "v_max_f32 v234, v234, v235\n"
      "v_mfma_f32_16x16x32_bf16 v[152:155], a[72:75], a[56:59], v[152:155]\n"
      "v_mov_b32 v233, v232\n"
      "v_mov_b32 v235, v234\n"
      "v_mfma_f32_16x16x32_bf16 v[152:155], a[76:79], a[60:63], v[152:155]\n"
      "v_permlane16_swap_b32 v232, v233\n"
      "v_permlane16_swap_b32 v234, v235\n"
      "v_mfma_f32_16x16x32_bf16 v[156:159], a[80:83], a[48:51], 0\n"
      "ds_read_b128 a[64:67], v208 offset:8192\n"
      "ds_read_b128 a[68:71], v208 offset:8704\n"
      "v_mfma_f32_16x16x32_bf16 v[156:159], a[84:87], a[52:55], v[156:159]\n"
      "ds_read_b128 a[72:75], v208 offset:9216\n"
      "ds_read_b128 a[76:79], v208 offset:9728\n"
      "v_mfma_f32_16x16x32_bf16 v[156:159], a[88:91], a[56:59], v[156:159]\n"
      "v_max_f32 v232, v232, v233\n"
      "v_max_f32 v234, v234, v235\n"
      "v_mfma_f32_16x16x32_bf16 v[156:159], a[92:95], a[60:63], v[156:159]\n"
      "v_sub_f32 v128, v128, v232\n"
      "v_sub_f32 v129, v129, v232\n"
      "v_mfma_f32_16x16x32_bf16 a[128:131], a[144:147], v[160:163], a[128:131]\n"
      "v_sub_f32 v130, v130, v232\n"
      "v_sub_f32 v131, v131, v232\n"
      "s_waitcnt lgkmcnt(12)\n"
      "v_mfma_f32_16x16x32_bf16 v[0:3], a[96:99], v[160:163], v[0:3]\n"
      "v_sub_f32 v132, v132, v232\n"
      "v_sub_f32 v133, v133, v232\n"
      "v_mfma_f32_16x16x32_bf16 v[4:7], a[100:103], v[160:163], v[4:7]\n"
      "ds_read_b128 a[80:83], v208 offset:12288\n"
      "ds_read_b128 a[84:87], v208 offset:12800\n"
      "v_mfma_f32_16x16x32_bf16 v[8:11], a[104:107], v[160:163], v[8:11]\n"
      "ds_read_b128 a[88:91], v208 offset:13312\n"
      "ds_read_b128 a[92:95], v208 offset:13824\n"
      "v_mfma_f32_16x16x32_bf16 v[12:15], a[108:111], v[160:163], v[12:15]\n"
      "v_sub_f32 v134, v134, v232\n"
      "v_sub_f32 v135, v135, v232\n"
      "v_max3_f32 v236, v144, v145, v146\n"
      "s_waitcnt lgkmcnt(8)\n"
      "v_mfma_f32_16x16x32_bf16 v[16:19], a[112:115], v[160:163], v[16:19]\n"
      "v_mul_f32 v192, -1.0, v232\n"
      "v_mul_f32 v193, -1.0, v232\n"
      "v_mfma_f32_16x16x32_bf16 v[20:23], a[116:119], v[160:163], v[20:23]\n"
      "v_mul_f32 v194, -1.0, v232\n"
      "v_mul_f32 v195, -1.0, v232\n"
      "v_mfma_f32_16x16x32_bf16 v[24:27], a[120:123], v[160:163], v[24:27]\n"
      "v_sub_f32 v136, v136, v234\n"
      "v_sub_f32 v137, v137, v234\n"
      "v_mfma_f32_16x16x32_bf16 v[28:31], a[124:127], v[160:163], v[28:31]\n"
      "s_mov_b32 m0, s58\n"
      "buffer_store_dwordx4 a[152:155], v214, s[52:55], s79 offen offset:3072 sc1\n"
      "v_mfma_f32_16x16x32_bf16 a[132:135], a[144:147], v[168:171], a[132:135]\n"
      "s_add_u32 s68, s57, 0x2008000\n"
      "v_sub_f32 v138, v138, v234\n"
      "v_sub_f32 v139, v139, v234\n"
      "v_max3_f32 v237, v147, v148, v149\n"
      "v_mfma_f32_16x16x32_bf16 v[32:35], a[96:99], v[168:171], v[32:35]\n"
      "buffer_load_dwordx4 v212, s[44:47], s68 offen nt lds\n"
      "v_sub_f32 v140, v140, v234\n"
      "v_mfma_f32_16x16x32_bf16 v[36:39], a[100:103], v[168:171], v[36:39]\n"
      "buffer_load_dwordx4 v213, s[44:47], s68 offen offset:1024 nt lds\n"
      "v_sub_f32 v141, v141, v234\n"
      "v_mfma_f32_16x16x32_bf16 v[40:43], a[104:107], v[168:171], v[40:43]\n"
      "v_sub_f32 v142, v142, v234\n"
      "v_sub_f32 v143, v143, v234\n"
      "v_mfma_f32_16x16x32_bf16 v[44:47], a[108:111], v[168:171], v[44:47]\n"
      "v_mul_f32 v196, -1.0, v234\n"
      "v_mul_f32 v197, -1.0, v234\n"
      "v_mfma_f32_16x16x32_bf16 v[48:51], a[112:115], v[168:171], v[48:51]\n"
      "v_mul_f32 v198, -1.0, v234\n"
      "v_mul_f32 v199, -1.0, v234\n"
      "v_max3_f32 v236, v236, v150, v151\n"
      "v_mfma_f32_16x16x32_bf16 v[52:55], a[116:119], v[168:171], v[52:55]\n"
      "v_max_f32 v236, v236, v237\n"
      "v_max3_f32 v238, v152, v153, v154\n"
      "v_mfma_f32_16x16x32_bf16 v[56:59], a[120:123], v[168:171], v[56:59]\n"
      "v_max3_f32 v239, v155, v156, v157\n"
      "v_max3_f32 v238, v238, v158, v159\n"
      "v_mfma_f32_16x16x32_bf16 v[60:63], a[124:127], v[168:171], v[60:63]\n"
      "v_max_f32 v238, v238, v239\n"
      "v_mov_b32 v237, v236\n"
      "v_mfma_f32_16x16x32_bf16 a[136:139], a[144:147], v[176:179], a[136:139]\n"
      "s_add_u32 m0, s58, 0xc000\n"
      "v_mov_b32 v239, v238\n"
      "v_permlane32_swap_b32 v236, v237\n"
      "v_mfma_f32_16x16x32_bf16 v[64:67], a[96:99], v[176:179], v[64:67]\n"
      "s_add_u32 s67, s57, 0x2004000\n"
      "v_permlane32_swap_b32 v238, v239\n"
      "v_max_f32 v236, v236, v237\n"
      "v_mfma_f32_16x16x32_bf16 v[68:71], a[100:103], v[176:179], v[68:71]\n"
      "buffer_load_dwordx4 v212, s[48:51], s67 offen nt lds\n"
      "v_max_f32 v238, v238, v239\n"
      "v_mov_b32 v237, v236\n"
      "v_mfma_f32_16x16x32_bf16 v[72:75], a[104:107], v[176:179], v[72:75]\n"
      "buffer_load_dwordx4 v213, s[48:51], s67 offen offset:1024 nt lds\n"
      "v_mov_b32 v239, v238\n"
      "v_mfma_f32_16x16x32_bf16 v[76:79], a[108:111], v[176:179], v[76:79]\n"
      "v_permlane16_swap_b32 v236, v237\n"
      "v_permlane16_swap_b32 v238, v239\n"
      "v_mfma_f32_16x16x32_bf16 v[80:83], a[112:115], v[176:179], v[80:83]\n"
      "v_max_f32 v236, v236, v237\n"
      "v_max_f32 v238, v238, v239\n"
      "v_mfma_f32_16x16x32_bf16 v[84:87], a[116:119], v[176:179], v[84:87]\n"
      "v_sub_f32 v144, v144, v236\n"
      "v_sub_f32 v145, v145, v236\n"
      "v_mfma_f32_16x16x32_bf16 v[88:91], a[120:123], v[176:179], v[88:91]\n"
      "v_sub_f32 v146, v146, v236\n"
      "v_sub_f32 v147, v147, v236\n"
      "v_sub_f32 v148, v148, v236\n"
      "v_mfma_f32_16x16x32_bf16 v[92:95], a[124:127], v[176:179], v[92:95]\n"
      "s_add_u32 m0, s59, 0x1800\n"
      "v_sub_f32 v149, v149, v236\n"
      "v_sub_f32 v150, v150, v236\n"
      "v_mfma_f32_16x16x32_bf16 a[140:143], a[144:147], v[184:187], a[140:143]\n"
      "s_add_u32 s67, s56, 0x2001800\n"
      "v_sub_f32 v151, v151, v236\n"
      "v_mul_f32 v200, -1.0, v236\n"
      "v_mfma_f32_16x16x32_bf16 v[96:99], a[96:99], v[184:187], v[96:99]\n"
      "buffer_load_dwordx4 v212, s[40:43], s67 offen nt lds\n"
      "v_mul_f32 v201, -1.0, v236\n"
      "v_mfma_f32_16x16x32_bf16 v[100:103], a[100:103], v[184:187], v[100:103]\n"
      "buffer_load_dwordx4 v213, s[40:43], s67 offen offset:1024 nt lds\n"
      "v_mul_f32 v202, -1.0, v236\n"
      "v_mfma_f32_16x16x32_bf16 v[104:107], a[104:107], v[184:187], v[104:107]\n"
      "v_mul_f32 v203, -1.0, v236\n"
      "v_sub_f32 v152, v152, v238\n"
      "v_sub_f32 v153, v153, v238\n"
      "v_mfma_f32_16x16x32_bf16 v[108:111], a[108:111], v[184:187], v[108:111]\n"
      "v_sub_f32 v154, v154, v238\n"
      "v_sub_f32 v155, v155, v238\n"
      "v_mfma_f32_16x16x32_bf16 v[112:115], a[112:115], v[184:187], v[112:115]\n"
      "v_sub_f32 v156, v156, v238\n"
      "v_sub_f32 v157, v157, v238\n"
      "v_mfma_f32_16x16x32_bf16 v[116:119], a[116:119], v[184:187], v[116:119]\n"
      "v_sub_f32 v158, v158, v238\n"
      "v_sub_f32 v159, v159, v238\n"
      "v_mfma_f32_16x16x32_bf16 v[120:123], a[120:123], v[184:187], v[120:123]\n"
      "s_memrealtime s[84:85]\n"
      "v_mul_f32 v204, -1.0, v238\n"
      "v_mul_f32 v205, -1.0, v238\n"
      "v_mfma_f32_16x16x32_bf16 v[124:127], a[124:127], v[184:187], v[124:127]\n"
      "v_mul_f32 v206, -1.0, v238\n"
      "v_mul_f32 v207, -1.0, v238\n"
      "s_waitcnt lgkmcnt(0)\n"
      "s_waitcnt vmcnt(24)\n"
      "ds_read_b128 v[224:227], v209 offset:0\n"
      "ds_read_b128 v[228:231], v209 offset:512\n"
      "v_exp_f32 v128, v128\n"
      "v_exp_f32 v129, v129\n"
      "v_exp_f32 v130, v130\n"
      "v_exp_f32 v131, v131\n"
      "v_exp_f32 v132, v132\n"
      "v_exp_f32 v133, v133\n"
      "v_exp_f32 v134, v134\n"
      "v_exp_f32 v135, v135\n"
      "v_exp_f32 v136, v136\n"
      "v_exp_f32 v137, v137\n"
      "v_exp_f32 v138, v138\n"
      "v_exp_f32 v139, v139\n"
      "s_waitcnt lgkmcnt(0)\n"
      "v_lshlrev_b32 v221, 16, v224\n"
      "v_and_b32 v224, s62, v224\n"
      "v_mul_f32 v221, s61, v221\n"
      "v_mul_f32 v224, s61, v224\n"
      "v_cvt_pk_bf16_f32 v224, v221, v224\n"
      "v_accvgpr_write_b32 a160, v224\n"
      "v_lshlrev_b32 v222, 16, v225\n"
      "v_and_b32 v225, s62, v225\n"
      "v_mul_f32 v222, s61, v222\n"
      "v_mul_f32 v225, s61, v225\n"
      "v_cvt_pk_bf16_f32 v225, v222, v225\n"
      "v_accvgpr_write_b32 a161, v225\n"
      "v_lshlrev_b32 v221, 16, v226\n"
      "v_and_b32 v226, s62, v226\n"
      "v_mul_f32 v221, s61, v221\n"
      "v_mul_f32 v226, s61, v226\n"
      "v_cvt_pk_bf16_f32 v226, v221, v226\n"
      "v_accvgpr_write_b32 a162, v226\n"
      "v_lshlrev_b32 v222, 16, v227\n"
      "v_and_b32 v227, s62, v227\n"
      "v_mul_f32 v222, s61, v222\n"
      "v_mul_f32 v227, s61, v227\n"
      "v_cvt_pk_bf16_f32 v227, v222, v227\n"
      "v_accvgpr_write_b32 a163, v227\n"
      "v_accvgpr_read_b32 v232, a128\n"
      "v_cmp_nge_f32 vcc, s63, v232\n"
      "v_rcp_f32 v234, v232\n"
      "s_nop 0\n"
      "v_mov_b32 v235, v234\n"
      "s_nop 1\n"
      "s_cmp_lg_u64 vcc, 0\n"
      "s_cselect_b32 s69, s65, 0\n"
      "s_or_b32 s64, s64, s69\n"
      "v_pk_mul_f32 v[0:1], v[0:1], v[234:235]\n"
      "v_pk_mul_f32 v[2:3], v[2:3], v[234:235]\n"
      "v_cvt_pk_bf16_f32 v0, v0, v1\n"
      "v_cvt_pk_bf16_f32 v1, v2, v3\n"
      "ds_write_b64 v215, v[0:1] offset:8192\n"
      "v_pk_mul_f32 v[4:5], v[4:5], v[234:235]\n"
      "v_pk_mul_f32 v[6:7], v[6:7], v[234:235]\n"
      "v_cvt_pk_bf16_f32 v4, v4, v5\n"
      "v_cvt_pk_bf16_f32 v5, v6, v7\n"
      "ds_write_b64 v216, v[4:5] offset:8192\n"
      "v_exp_f32 v140, v140\n"
      "v_pk_mul_f32 v[8:9], v[8:9], v[234:235]\n"
      "v_pk_mul_f32 v[10:11], v[10:11], v[234:235]\n"
      "v_cvt_pk_bf16_f32 v8, v8, v9\n"
      "v_cvt_pk_bf16_f32 v9, v10, v11\n"
      "ds_write_b64 v217, v[8:9] offset:8192\n"
      "v_exp_f32 v141, v141\n"
      "v_pk_mul_f32 v[12:13], v[12:13], v[234:235]\n"
      "v_pk_mul_f32 v[14:15], v[14:15], v[234:235]\n"
      "v_cvt_pk_bf16_f32 v12, v12, v13\n"
      "v_cvt_pk_bf16_f32 v13, v14, v15\n"
      "ds_write_b64 v218, v[12:13] offset:8192\n"
      "v_exp_f32 v142, v142\n"
      "v_pk_mul_f32 v[16:17], v[16:17], v[234:235]\n"
      "v_pk_mul_f32 v[18:19], v[18:19], v[234:235]\n"
      "v_cvt_pk_bf16_f32 v16, v16, v17\n"
      "v_cvt_pk_bf16_f32 v17, v18, v19\n"
      "ds_write_b64 v215, v[16:17] offset:8320\n"
      "v_exp_f32 v143, v143\n"
      "v_pk_mul_f32 v[20:21], v[20:21], v[234:235]\n"
      "v_pk_mul_f32 v[22:23], v[22:23], v[234:235]\n"
      "v_cvt_pk_bf16_f32 v20, v20, v21\n"
      "v_cvt_pk_bf16_f32 v21, v22, v23\n"
      "ds_write_b64 v216, v[20:21] offset:8320\n"
      "v_exp_f32 v144, v144\n"
      "v_pk_mul_f32 v[24:25], v[24:25], v[234:235]\n"
      "v_pk_mul_f32 v[26:27], v[26:27], v[234:235]\n"
      "v_cvt_pk_bf16_f32 v24, v24, v25\n"
      "v_cvt_pk_bf16_f32 v25, v26, v27\n"
      "ds_write_b64 v217, v[24:25] offset:8320\n"
      "v_exp_f32 v145, v145\n"
      "v_pk_mul_f32 v[28:29], v[28:29], v[234:235]\n"
      "v_pk_mul_f32 v[30:31], v[30:31], v[234:235]\n"
      "v_cvt_pk_bf16_f32 v28, v28, v29\n"
      "v_cvt_pk_bf16_f32 v29, v30, v31\n"
      "ds_write_b64 v218, v[28:29] offset:8320\n"
      "v_exp_f32 v146, v146\n"
      "v_accvgpr_read_b32 v236, a132\n"
      "v_cmp_nge_f32 vcc, s63, v236\n"
      "v_rcp_f32 v238, v236\n"
      "s_nop 0\n"
      "v_mov_b32 v239, v238\n"
      "s_nop 1\n"
      "s_cmp_lg_u64 vcc, 0\n"
      "s_cselect_b32 s69, s65, 0\n"
      "s_or_b32 s64, s64, s69\n"
      "v_pk_mul_f32 v[32:33], v[32:33], v[238:239]\n"
      "v_pk_mul_f32 v[34:35], v[34:35], v[238:239]\n"
      "v_cvt_pk_bf16_f32 v32, v32, v33\n"
      "v_cvt_pk_bf16_f32 v33, v34, v35\n"
      "ds_write_b64 v215, v[32:33] offset:12288\n"
      "v_pk_mul_f32 v[36:37], v[36:37], v[238:239]\n"
      "v_pk_mul_f32 v[38:39], v[38:39], v[238:239]\n"
      "v_cvt_pk_bf16_f32 v36, v36, v37\n"
      "v_cvt_pk_bf16_f32 v37, v38, v39\n"
      "ds_write_b64 v216, v[36:37] offset:12288\n"
      "v_exp_f32 v147, v147\n"
      "v_pk_mul_f32 v[40:41], v[40:41], v[238:239]\n"
      "v_pk_mul_f32 v[42:43], v[42:43], v[238:239]\n"
      "v_cvt_pk_bf16_f32 v40, v40, v41\n"
      "v_cvt_pk_bf16_f32 v41, v42, v43\n"
      "ds_write_b64 v217, v[40:41] offset:12288\n"
      "v_exp_f32 v148, v148\n"
      "v_pk_mul_f32 v[44:45], v[44:45], v[238:239]\n"
      "v_pk_mul_f32 v[46:47], v[46:47], v[238:239]\n"
      "v_cvt_pk_bf16_f32 v44, v44, v45\n"
      "v_cvt_pk_bf16_f32 v45, v46, v47\n"
      "ds_write_b64 v218, v[44:45] offset:12288\n"
      "v_exp_f32 v149, v149\n"
      "v_pk_mul_f32 v[48:49], v[48:49], v[238:239]\n"
      "v_pk_mul_f32 v[50:51], v[50:51], v[238:239]\n"
      "v_cvt_pk_bf16_f32 v48, v48, v49\n"
      "v_cvt_pk_bf16_f32 v49, v50, v51\n"
      "ds_write_b64 v215, v[48:49] offset:12416\n"
      "v_exp_f32 v150, v150\n"
      "v_pk_mul_f32 v[52:53], v[52:53], v[238:239]\n"
      "v_pk_mul_f32 v[54:55], v[54:55], v[238:239]\n"
      "v_cvt_pk_bf16_f32 v52, v52, v53\n"
      "v_cvt_pk_bf16_f32 v53, v54, v55\n"
      "ds_write_b64 v216, v[52:53] offset:12416\n"
      "v_exp_f32 v151, v151\n"
      "v_pk_mul_f32 v[56:57], v[56:57], v[238:239]\n"
      "v_pk_mul_f32 v[58:59], v[58:59], v[238:239]\n"
      "v_cvt_pk_bf16_f32 v56, v56, v57\n"
      "v_cvt_pk_bf16_f32 v57, v58, v59\n"
      "ds_write_b64 v217, v[56:57] offset:12416\n"
      "v_exp_f32 v152, v152\n"
      "v_pk_mul_f32 v[60:61], v[60:61], v[238:239]\n"
      "v_pk_mul_f32 v[62:63], v[62:63], v[238:239]\n"
      "v_cvt_pk_bf16_f32 v60, v60, v61\n"
      "v_cvt_pk_bf16_f32 v61, v62, v63\n"
      "ds_write_b64 v218, v[60:61] offset:12416\n"
      "v_exp_f32 v153, v153\n"
      "v_accvgpr_read_b32 v232, a136\n"
      "v_cmp_nge_f32 vcc, s63, v232\n"
      "v_rcp_f32 v234, v232\n"
      "s_nop 0\n"
      "v_mov_b32 v235, v234\n"
      "s_nop 1\n"
      "s_cmp_lg_u64 vcc, 0\n"
      "s_cselect_b32 s69, s65, 0\n"
      "s_or_b32 s64, s64, s69\n"
      "v_pk_mul_f32 v[64:65], v[64:65], v[234:235]\n"
      "v_pk_mul_f32 v[66:67], v[66:67], v[234:235]\n"
      "v_cvt_pk_bf16_f32 v64, v64, v65\n"
      "v_cvt_pk_bf16_f32 v65, v66, v67\n"
      "ds_write_b64 v215, v[64:65] offset:16384\n"
      "v_pk_mul_f32 v[68:69], v[68:69], v[234:235]\n"
      "v_pk_mul_f32 v[70:71], v[70:71], v[234:235]\n"
      "v_cvt_pk_bf16_f32 v68, v68, v69\n"
      "v_cvt_pk_bf16_f32 v69, v70, v71\n"
      "ds_write_b64 v216, v[68:69] offset:16384\n"
      "v_exp_f32 v154, v154\n"
      "v_pk_mul_f32 v[72:73], v[72:73], v[234:235]\n"
      "v_pk_mul_f32 v[74:75], v[74:75], v[234:235]\n"
      "v_cvt_pk_bf16_f32 v72, v72, v73\n"
      "v_cvt_pk_bf16_f32 v73, v74, v75\n"
      "ds_write_b64 v217, v[72:73] offset:16384\n"
      "v_cvt_pk_bf16_f32 v128, v128, v129\n"
      "v_pk_mul_f32 v[76:77], v[76:77], v[234:235]\n"
      "v_pk_mul_f32 v[78:79], v[78:79], v[234:235]\n"
      "v_cvt_pk_bf16_f32 v76, v76, v77\n"
      "v_cvt_pk_bf16_f32 v77, v78, v79\n"
      "ds_write_b64 v218, v[76:77] offset:16384\n"
      "v_cvt_pk_bf16_f32 v129, v130, v131\n"
      "v_pk_mul_f32 v[80:81], v[80:81], v[234:235]\n"
      "v_pk_mul_f32 v[82:83], v[82:83], v[234:235]\n"
      "v_cvt_pk_bf16_f32 v80, v80, v81\n"
      "v_cvt_pk_bf16_f32 v81, v82, v83\n"
      "ds_write_b64 v215, v[80:81] offset:16512\n"
      "v_cvt_pk_bf16_f32 v130, v132, v133\n"
      "v_pk_mul_f32 v[84:85], v[84:85], v[234:235]\n"
      "v_pk_mul_f32 v[86:87], v[86:87], v[234:235]\n"
      "v_cvt_pk_bf16_f32 v84, v84, v85\n"
      "v_cvt_pk_bf16_f32 v85, v86, v87\n"
      "ds_write_b64 v216, v[84:85] offset:16512\n"
      "v_cvt_pk_bf16_f32 v131, v134, v135\n"
      "v_pk_mul_f32 v[88:89], v[88:89], v[234:235]\n"
      "v_pk_mul_f32 v[90:91], v[90:91], v[234:235]\n"
      "v_cvt_pk_bf16_f32 v88, v88, v89\n"
      "v_cvt_pk_bf16_f32 v89, v90, v91\n"
      "ds_write_b64 v217, v[88:89] offset:16512\n"
      "v_cvt_pk_bf16_f32 v136, v136, v137\n"
      "v_pk_mul_f32 v[92:93], v[92:93], v[234:235]\n"
      "v_pk_mul_f32 v[94:95], v[94:95], v[234:235]\n"
      "v_cvt_pk_bf16_f32 v92, v92, v93\n"
      "v_cvt_pk_bf16_f32 v93, v94, v95\n"
      "ds_write_b64 v218, v[92:93] offset:16512\n"
      "v_cvt_pk_bf16_f32 v137, v138, v139\n"
      "v_accvgpr_read_b32 v236, a140\n"
      "v_cmp_nge_f32 vcc, s63, v236\n"
      "v_rcp_f32 v238, v236\n"
      "s_nop 0\n"
      "v_mov_b32 v239, v238\n"
      "s_nop 1\n"
      "s_cmp_lg_u64 vcc, 0\n"
      "s_cselect_b32 s69, s65, 0\n"
      "s_or_b32 s64, s64, s69\n"
      "v_pk_mul_f32 v[96:97], v[96:97], v[238:239]\n"
      "v_pk_mul_f32 v[98:99], v[98:99], v[238:239]\n"
      "v_cvt_pk_bf16_f32 v96, v96, v97\n"
      "v_cvt_pk_bf16_f32 v97, v98, v99\n"
      "ds_write_b64 v215, v[96:97] offset:20480\n"
      "v_pk_mul_f32 v[100:101], v[100:101], v[238:239]\n"
      "v_pk_mul_f32 v[102:103], v[102:103], v[238:239]\n"
      "v_cvt_pk_bf16_f32 v100, v100, v101\n"
      "v_cvt_pk_bf16_f32 v101, v102, v103\n"
      "ds_write_b64 v216, v[100:101] offset:20480\n"
      "v_cvt_pk_bf16_f32 v138, v140, v141\n"
      "v_pk_mul_f32 v[104:105], v[104:105], v[238:239]\n"
      "v_pk_mul_f32 v[106:107], v[106:107], v[238:239]\n"
      "v_cvt_pk_bf16_f32 v104, v104, v105\n"
      "v_cvt_pk_bf16_f32 v105, v106, v107\n"
      "ds_write_b64 v217, v[104:105] offset:20480\n"
      "v_cvt_pk_bf16_f32 v139, v142, v143\n"
      "v_pk_mul_f32 v[108:109], v[108:109], v[238:239]\n"
      "v_pk_mul_f32 v[110:111], v[110:111], v[238:239]\n"
      "v_cvt_pk_bf16_f32 v108, v108, v109\n"
      "v_cvt_pk_bf16_f32 v109, v110, v111\n"
      "ds_write_b64 v218, v[108:109] offset:20480\n"
      "v_cvt_pk_bf16_f32 v144, v144, v145\n"
      "v_pk_mul_f32 v[112:113], v[112:113], v[238:239]\n"
      "v_pk_mul_f32 v[114:115], v[114:115], v[238:239]\n"
      "v_cvt_pk_bf16_f32 v112, v112, v113\n"
      "v_cvt_pk_bf16_f32 v113, v114, v115\n"
      "ds_write_b64 v215, v[112:113] offset:20608\n"
      "v_cvt_pk_bf16_f32 v145, v146, v147\n"
      "v_pk_mul_f32 v[116:117], v[116:117], v[238:239]\n"
      "v_pk_mul_f32 v[118:119], v[118:119], v[238:239]\n"
      "v_cvt_pk_bf16_f32 v116, v116, v117\n"
      "v_cvt_pk_bf16_f32 v117, v118, v119\n"
      "ds_write_b64 v216, v[116:117] offset:20608\n"
      "v_cvt_pk_bf16_f32 v146, v148, v149\n"
      "v_pk_mul_f32 v[120:121], v[120:121], v[238:239]\n"
      "v_pk_mul_f32 v[122:123], v[122:123], v[238:239]\n"
      "v_cvt_pk_bf16_f32 v120, v120, v121\n"
      "v_cvt_pk_bf16_f32 v121, v122, v123\n"
      "ds_write_b64 v217, v[120:121] offset:20608\n"
      "v_cvt_pk_bf16_f32 v147, v150, v151\n"
      "v_pk_mul_f32 v[124:125], v[124:125], v[238:239]\n"
      "v_pk_mul_f32 v[126:127], v[126:127], v[238:239]\n"
      "v_cvt_pk_bf16_f32 v124, v124, v125\n"
      "v_cvt_pk_bf16_f32 v125, v126, v127\n"
      "ds_write_b64 v218, v[124:125] offset:20608\n"
      "v_cvt_pk_bf16_f32 v152, v152, v153\n"
      "s_mov_b32 s76, s56\n"
      "s_add_u32 s77, s56, 0x1000\n"
      "s_add_u32 s78, s56, 0x2000\n"
      "s_add_u32 s79, s56, 0x3000\n"
      "s_mov_b32 s54, 0x4000000\n"
      "s_memrealtime s[84:85]\n"
      "s_add_u32 s86, s86, 90\n"
      "s_cmp_eq_u32 s66, 2\n"
      "s_cselect_b32 s87, 0, s87\n"
      "s_add_u32 s57, s57, 0x2000000\n"
      "s_add_u32 s56, s56, 0x1000000\n"
      "s_lshl_b32 s65, s65, 1\n"
      "s_sub_u32 s66, s66, 1\n"
      "s_branch L_HEAD\n"
      "L_TAIL:\n"
      "s_waitcnt lgkmcnt(0)\n"
      "s_waitcnt vmcnt(12)\n"
      "s_barrier\n"
      "ds_read_b128 a[152:155], v220 offset:23552\n"
      "ds_read_b64_tr_b16 a[96:97], v210 offset:57344\n"
      "ds_read_b64_tr_b16 a[98:99], v210 offset:61440\n"
      "v_exp_f32 v187, v187\n"
      "ds_read_b64_tr_b16 a[100:101], v211 offset:57344\n"
      "ds_read_b64_tr_b16 a[102:103], v211 offset:61440\n"
      "v_exp_f32 v188, v188\n"
      "ds_read_b64_tr_b16 a[104:105], v210 offset:57856\n"
      "ds_read_b64_tr_b16 a[106:107], v210 offset:61952\n"
      "v_cvt_pk_bf16_f32 v185, v186, v187\n"
      "ds_read_b64_tr_b16 a[108:109], v211 offset:57856\n"
      "ds_read_b64_tr_b16 a[110:111], v211 offset:61952\n"
      "v_exp_f32 v189, v189\n"
      "ds_read_b64_tr_b16 a[112:113], v210 offset:58368\n"
      "v_exp_f32 v190, v190\n"
      "ds_read_b64_tr_b16 a[114:115], v210 offset:62464\n"
      "ds_read_b64_tr_b16 a[116:117], v211 offset:58368\n"
      "v_cvt_pk_bf16_f32 v186, v188, v189\n"
      "ds_read_b64_tr_b16 a[118:119], v211 offset:62464\n"
      "ds_read_b64_tr_b16 a[120:121], v210 offset:58880\n"
      "v_exp_f32 v191, v191\n"
      "ds_read_b64_tr_b16 a[122:123], v210 offset:62976\n"
      "ds_read_b64_tr_b16 a[124:125], v211 offset:58880\n"
      "s_nop 0\n"
      "ds_read_b64_tr_b16 a[126:127], v211 offset:62976\n"
      "v_cvt_pk_bf16_f32 v187, v190, v191\n"
      "s_waitcnt lgkmcnt(0)\n"
      "buffer_store_dwordx4 a[152:155], v214, s[52:55], s79 offen offset:3072 sc1\n"
      "v_mfma_f32_16x16x32_bf16 a[128:131], a[144:147], v[160:163], a[128:131]\n"
      "v_mfma_f32_16x16x32_bf16 v[0:3], a[96:99], v[160:163], v[0:3]\n"
      "v_mfma_f32_16x16x32_bf16 v[4:7], a[100:103], v[160:163], v[4:7]\n"
      "v_mfma_f32_16x16x32_bf16 v[8:11], a[104:107], v[160:163], v[8:11]\n"
      "v_mfma_f32_16x16x32_bf16 v[12:15], a[108:111], v[160:163], v[12:15]\n"
      "v_mfma_f32_16x16x32_bf16 v[16:19], a[112:115], v[160:163], v[16:19]\n"
      "v_mfma_f32_16x16x32_bf16 v[20:23], a[116:119], v[160:163], v[20:23]\n"
      "v_mfma_f32_16x16x32_bf16 v[24:27], a[120:123], v[160:163], v[24:27]\n"
      "v_mfma_f32_16x16x32_bf16 v[28:31], a[124:127], v[160:163], v[28:31]\n"
      "v_mfma_f32_16x16x32_bf16 a[132:135], a[144:147], v[168:171], a[132:135]\n"
      "v_mfma_f32_16x16x32_bf16 v[32:35], a[96:99], v[168:171], v[32:35]\n"
      "v_mfma_f32_16x16x32_bf16 v[36:39], a[100:103], v[168:171], v[36:39]\n"
      "v_mfma_f32_16x16x32_bf16 v[40:43], a[104:107], v[168:171], v[40:43]\n"
      "v_mfma_f32_16x16x32_bf16 v[44:47], a[108:111], v[168:171], v[44:47]\n"
      "v_mfma_f32_16x16x32_bf16 v[48:51], a[112:115], v[168:171], v[48:51]\n"
      "v_mfma_f32_16x16x32_bf16 v[52:55], a[116:119], v[168:171], v[52:55]\n"
      "v_mfma_f32_16x16x32_bf16 v[56:59], a[120:123], v[168:171], v[56:59]\n"
      "v_mfma_f32_16x16x32_bf16 v[60:63], a[124:127], v[168:171], v[60:63]\n"
      "v_mfma_f32_16x16x32_bf16 a[136:139], a[144:147], v[176:179], a[136:139]\n"
      "v_mfma_f32_16x16x32_bf16 v[64:67], a[96:99], v[176:179], v[64:67]\n"
      "v_mfma_f32_16x16x32_bf16 v[68:71], a[100:103], v[176:179], v[68:71]\n"
      "v_mfma_f32_16x16x32_bf16 v[72:75], a[104:107], v[176:179], v[72:75]\n"
      "v_mfma_f32_16x16x32_bf16 v[76:79], a[108:111], v[176:179], v[76:79]\n"
      "v_mfma_f32_16x16x32_bf16 v[80:83], a[112:115], v[176:179], v[80:83]\n"
      "v_mfma_f32_16x16x32_bf16 v[84:87], a[116:119], v[176:179], v[84:87]\n"
      "v_mfma_f32_16x16x32_bf16 v[88:91], a[120:123], v[176:179], v[88:91]\n"
      "v_mfma_f32_16x16x32_bf16 v[92:95], a[124:127], v[176:179], v[92:95]\n"
      "v_mfma_f32_16x16x32_bf16 a[140:143], a[144:147], v[184:187], a[140:143]\n"
      "v_mfma_f32_16x16x32_bf16 v[96:99], a[96:99], v[184:187], v[96:99]\n"
      "v_mfma_f32_16x16x32_bf16 v[100:103], a[100:103], v[184:187], v[100:103]\n"
      "v_mfma_f32_16x16x32_bf16 v[104:107], a[104:107], v[184:187], v[104:107]\n"
      "v_mfma_f32_16x16x32_bf16 v[108:111], a[108:111], v[184:187], v[108:111]\n"
      "v_mfma_f32_16x16x32_bf16 v[112:115], a[112:115], v[184:187], v[112:115]\n"
      "v_mfma_f32_16x16x32_bf16 v[116:119], a[116:119], v[184:187], v[116:119]\n"
      "v_mfma_f32_16x16x32_bf16 v[120:123], a[120:123], v[184:187], v[120:123]\n"
      "v_mfma_f32_16x16x32_bf16 v[124:127], a[124:127], v[184:187], v[124:127]\n"
      "s_mov_b32 s76, s56\n"
      "s_add_u32 s77, s56, 0x1000\n"
      "s_add_u32 s78, s56, 0x2000\n"
      "s_add_u32 s79, s56, 0x3000\n"
      "s_mov_b32 s54, 0x4000000\n"
      "v_accvgpr_read_b32 v232, a128\n"
      "v_cmp_nge_f32 vcc, s63, v232\n"
      "v_rcp_f32 v234, v232\n"
      "s_nop 0\n"
      "v_mov_b32 v235, v234\n"
      "s_nop 1\n"
      "s_cmp_lg_u64 vcc, 0\n"
      "s_cselect_b32 s69, s65, 0\n"
      "s_or_b32 s64, s64, s69\n"
      "v_pk_mul_f32 v[0:1], v[0:1], v[234:235]\n"
      "v_pk_mul_f32 v[2:3], v[2:3], v[234:235]\n"
      "v_cvt_pk_bf16_f32 v0, v0, v1\n"
      "v_cvt_pk_bf16_f32 v1, v2, v3\n"
      "ds_write_b64 v215, v[0:1] offset:8192\n"
      "v_pk_mul_f32 v[4:5], v[4:5], v[234:235]\n"
      "v_pk_mul_f32 v[6:7], v[6:7], v[234:235]\n"
      "v_cvt_pk_bf16_f32 v4, v4, v5\n"
      "v_cvt_pk_bf16_f32 v5, v6, v7\n"
      "ds_write_b64 v216, v[4:5] offset:8192\n"
      "v_pk_mul_f32 v[8:9], v[8:9], v[234:235]\n"
      "v_pk_mul_f32 v[10:11], v[10:11], v[234:235]\n"
      "v_cvt_pk_bf16_f32 v8, v8, v9\n"
      "v_cvt_pk_bf16_f32 v9, v10, v11\n"
      "ds_write_b64 v217, v[8:9] offset:8192\n"
      "v_pk_mul_f32 v[12:13], v[12:13], v[234:235]\n"
      "v_pk_mul_f32 v[14:15], v[14:15], v[234:235]\n"
      "v_cvt_pk_bf16_f32 v12, v12, v13\n"
      "v_cvt_pk_bf16_f32 v13, v14, v15\n"
      "ds_write_b64 v218, v[12:13] offset:8192\n"
      "v_pk_mul_f32 v[16:17], v[16:17], v[234:235]\n"
      "v_pk_mul_f32 v[18:19], v[18:19], v[234:235]\n"
      "v_cvt_pk_bf16_f32 v16, v16, v17\n"
      "v_cvt_pk_bf16_f32 v17, v18, v19\n"
      "ds_write_b64 v215, v[16:17] offset:8320\n"
      "v_pk_mul_f32 v[20:21], v[20:21], v[234:235]\n"
      "v_pk_mul_f32 v[22:23], v[22:23], v[234:235]\n"
      "v_cvt_pk_bf16_f32 v20, v20, v21\n"
      "v_cvt_pk_bf16_f32 v21, v22, v23\n"
      "ds_write_b64 v216, v[20:21] offset:8320\n"
      "v_pk_mul_f32 v[24:25], v[24:25], v[234:235]\n"
      "v_pk_mul_f32 v[26:27], v[26:27], v[234:235]\n"
      "v_cvt_pk_bf16_f32 v24, v24, v25\n"
      "v_cvt_pk_bf16_f32 v25, v26, v27\n"
      "ds_write_b64 v217, v[24:25] offset:8320\n"
      "v_pk_mul_f32 v[28:29], v[28:29], v[234:235]\n"
      "v_pk_mul_f32 v[30:31], v[30:31], v[234:235]\n"
      "v_cvt_pk_bf16_f32 v28, v28, v29\n"
      "v_cvt_pk_bf16_f32 v29, v30, v31\n"
      "ds_write_b64 v218, v[28:29] offset:8320\n"
      "ds_read_b128 v[0:3], v219 offset:8192\n"
      "ds_read_b128 v[4:7], v220 offset:9216\n"
      "ds_read_b128 v[8:11], v219 offset:10240\n"
      "ds_read_b128 v[12:15], v220 offset:11264\n"
      "v_accvgpr_read_b32 v236, a132\n"
      "v_cmp_nge_f32 vcc, s63, v236\n"
      "v_rcp_f32 v238, v236\n"
      "s_nop 0\n"
      "v_mov_b32 v239, v238\n"
      "s_nop 1\n"
      "s_cmp_lg_u64 vcc, 0\n"
      "s_cselect_b32 s69, s65, 0\n"
      "s_or_b32 s64, s64, s69\n"
      "v_pk_mul_f32 v[32:33], v[32:33], v[238:239]\n"
      "v_pk_mul_f32 v[34:35], v[34:35], v[238:239]\n"
      "v_cvt_pk_bf16_f32 v32, v32, v33\n"
      "v_cvt_pk_bf16_f32 v33, v34, v35\n"
      "ds_write_b64 v215, v[32:33] offset:12288\n"
      "v_pk_mul_f32 v[36:37], v[36:37], v[238:239]\n"
      "v_pk_mul_f32 v[38:39], v[38:39], v[238:239]\n"
      "v_cvt_pk_bf16_f32 v36, v36, v37\n"
      "v_cvt_pk_bf16_f32 v37, v38, v39\n"
      "ds_write_b64 v216, v[36:37] offset:12288\n"
      "v_pk_mul_f32 v[40:41], v[40:41], v[238:239]\n"
      "v_pk_mul_f32 v[42:43], v[42:43], v[238:239]\n"
      "v_cvt_pk_bf16_f32 v40, v40, v41\n"
      "v_cvt_pk_bf16_f32 v41, v42, v43\n"
      "ds_write_b64 v217, v[40:41] offset:12288\n"
      "v_pk_mul_f32 v[44:45], v[44:45], v[238:239]\n"
      "v_pk_mul_f32 v[46:47], v[46:47], v[238:239]\n"
      "v_cvt_pk_bf16_f32 v44, v44, v45\n"
      "v_cvt_pk_bf16_f32 v45, v46, v47\n"
      "ds_write_b64 v218, v[44:45] offset:12288\n"
      "v_pk_mul_f32 v[48:49], v[48:49], v[238:239]\n"
      "v_pk_mul_f32 v[50:51], v[50:51], v[238:239]\n"
      "v_cvt_pk_bf16_f32 v48, v48, v49\n"
      "v_cvt_pk_bf16_f32 v49, v50, v51\n"
      "ds_write_b64 v215, v[48:49] offset:12416\n"
      "v_pk_mul_f32 v[52:53], v[52:53], v[238:239]\n"
      "v_pk_mul_f32 v[54:55], v[54:55], v[238:239]\n"
      "v_cvt_pk_bf16_f32 v52, v52, v53\n"
      "v_cvt_pk_bf16_f32 v53, v54, v55\n"
      "ds_write_b64 v216, v[52:53] offset:12416\n"
      "v_pk_mul_f32 v[56:57], v[56:57], v[238:239]\n"
      "v_pk_mul_f32 v[58:59], v[58:59], v[238:239]\n"
      "v_cvt_pk_bf16_f32 v56, v56, v57\n"
      "v_cvt_pk_bf16_f32 v57, v58, v59\n"
      "ds_write_b64 v217, v[56:57] offset:12416\n"
      "v_pk_mul_f32 v[60:61], v[60:61], v[238:239]\n"
      "v_pk_mul_f32 v[62:63], v[62:63], v[238:239]\n"
      "v_cvt_pk_bf16_f32 v60, v60, v61\n"
      "v_cvt_pk_bf16_f32 v61, v62, v63\n"
      "ds_write_b64 v218, v[60:61] offset:12416\n"
      "s_waitcnt lgkmcnt(8)\n"
      "buffer_store_dwordx4 v[0:3], v214, s[52:55], s76 offen sc1\n"
      "buffer_store_dwordx4 v[4:7], v214, s[52:55], s76 offen offset:1024 sc1\n"
      "buffer_store_dwordx4 v[8:11], v214, s[52:55], s76 offen offset:2048 sc1\n"
      "buffer_store_dwordx4 v[12:15], v214, s[52:55], s76 offen offset:3072 sc1\n"
      "ds_read_b128 v[32:35], v219 offset:12288\n"
      "ds_read_b128 v[36:39], v220 offset:13312\n"
      "ds_read_b128 v[40:43], v219 offset:14336\n"
      "ds_read_b128 v[44:47], v220 offset:15360\n"
      "v_accvgpr_read_b32 v232, a136\n"
      "v_cmp_nge_f32 vcc, s63, v232\n"
      "v_rcp_f32 v234, v232\n"
      "s_nop 0\n"
      "v_mov_b32 v235, v234\n"
      "s_nop 1\n"
      "s_cmp_lg_u64 vcc, 0\n"
      "s_cselect_b32 s69, s65, 0\n"
      "s_or_b32 s64, s64, s69\n"
      "v_pk_mul_f32 v[64:65], v[64:65], v[234:235]\n"
      "v_pk_mul_f32 v[66:67], v[66:67], v[234:235]\n"
      "v_cvt_pk_bf16_f32 v64, v64, v65\n"
      "v_cvt_pk_bf16_f32 v65, v66, v67\n"
      "ds_write_b64 v215, v[64:65] offset:16384\n"
      "v_pk_mul_f32 v[68:69], v[68:69], v[234:235]\n"
      "v_pk_mul_f32 v[70:71], v[70:71], v[234:235]\n"
      "v_cvt_pk_bf16_f32 v68, v68, v69\n"
      "v_cvt_pk_bf16_f32 v69, v70, v71\n"
      "ds_write_b64 v216, v[68:69] offset:16384\n"
      "v_pk_mul_f32 v[72:73], v[72:73], v[234:235]\n"
      "v_pk_mul_f32 v[74:75], v[74:75], v[234:235]\n"
      "v_cvt_pk_bf16_f32 v72, v72, v73\n"
      "v_cvt_pk_bf16_f32 v73, v74, v75\n"
      "ds_write_b64 v217, v[72:73] offset:16384\n"
      "v_pk_mul_f32 v[76:77], v[76:77], v[234:235]\n"
      "v_pk_mul_f32 v[78:79], v[78:79], v[234:235]\n"
      "v_cvt_pk_bf16_f32 v76, v76, v77\n"
      "v_cvt_pk_bf16_f32 v77, v78, v79\n"
      "ds_write_b64 v218, v[76:77] offset:16384\n"
      "v_pk_mul_f32 v[80:81], v[80:81], v[234:235]\n"
      "v_pk_mul_f32 v[82:83], v[82:83], v[234:235]\n"
      "v_cvt_pk_bf16_f32 v80, v80, v81\n"
      "v_cvt_pk_bf16_f32 v81, v82, v83\n"
      "ds_write_b64 v215, v[80:81] offset:16512\n"
      "v_pk_mul_f32 v[84:85], v[84:85], v[234:235]\n"
      "v_pk_mul_f32 v[86:87], v[86:87], v[234:235]\n"
      "v_cvt_pk_bf16_f32 v84, v84, v85\n"
      "v_cvt_pk_bf16_f32 v85, v86, v87\n"
      "ds_write_b64 v216, v[84:85] offset:16512\n"
      "v_pk_mul_f32 v[88:89], v[88:89], v[234:235]\n"
      "v_pk_mul_f32 v[90:91], v[90:91], v[234:235]\n"
      "v_cvt_pk_bf16_f32 v88, v88, v89\n"
      "v_cvt_pk_bf16_f32 v89, v90, v91\n"
      "ds_write_b64 v217, v[88:89] offset:16512\n"
      "v_pk_mul_f32 v[92:93], v[92:93], v[234:235]\n"
      "v_pk_mul_f32 v[94:95], v[94:95], v[234:235]\n"
      "v_cvt_pk_bf16_f32 v92, v92, v93\n"
      "v_cvt_pk_bf16_f32 v93, v94, v95\n"
      "ds_write_b64 v218, v[92:93] offset:16512\n"
      "s_waitcnt lgkmcnt(8)\n"
      "buffer_store_dwordx4 v[32:35], v214, s[52:55], s77 offen sc1\n"
      "buffer_store_dwordx4 v[36:39], v214, s[52:55], s77 offen offset:1024 sc1\n"
      "buffer_store_dwordx4 v[40:43], v214, s[52:55], s77 offen offset:2048 sc1\n"
      "buffer_store_dwordx4 v[44:47], v214, s[52:55], s77 offen offset:3072 sc1\n"
      "ds_read_b128 v[64:67], v219 offset:16384\n"
      "ds_read_b128 v[68:71], v220 offset:17408\n"
      "ds_read_b128 v[72:75], v219 offset:18432\n"
      "ds_read_b128 v[76:79], v220 offset:19456\n"
      "v_accvgpr_read_b32 v236, a140\n"
      "v_cmp_nge_f32 vcc, s63, v236\n"
      "v_rcp_f32 v238, v236\n"
      "s_nop 0\n"
      "v_mov_b32 v239, v238\n"
      "s_nop 1\n"
      "s_cmp_lg_u64 vcc, 0\n"
      "s_cselect_b32 s69, s65, 0\n"
      "s_or_b32 s64, s64, s69\n"
      "v_pk_mul_f32 v[96:97], v[96:97], v[238:239]\n"
      "v_pk_mul_f32 v[98:99], v[98:99], v[238:239]\n"
      "v_cvt_pk_bf16_f32 v96, v96, v97\n"
      "v_cvt_pk_bf16_f32 v97, v98, v99\n"
      "ds_write_b64 v215, v[96:97] offset:20480\n"
      "v_pk_mul_f32 v[100:101], v[100:101], v[238:239]\n"
      "v_pk_mul_f32 v[102:103], v[102:103], v[238:239]\n"
      "v_cvt_pk_bf16_f32 v100, v100, v101\n"
      "v_cvt_pk_bf16_f32 v101, v102, v103\n"
      "ds_write_b64 v216, v[100:101] offset:20480\n"
      "v_pk_mul_f32 v[104:105], v[104:105], v[238:239]\n"
      "v_pk_mul_f32 v[106:107], v[106:107], v[238:239]\n"
      "v_cvt_pk_bf16_f32 v104, v104, v105\n"
      "v_cvt_pk_bf16_f32 v105, v106, v107\n"
      "ds_write_b64 v217, v[104:105] offset:20480\n"
      "v_pk_mul_f32 v[108:109], v[108:109], v[238:239]\n"
      "v_pk_mul_f32 v[110:111], v[110:111], v[238:239]\n"
      "v_cvt_pk_bf16_f32 v108, v108, v109\n"
      "v_cvt_pk_bf16_f32 v109, v110, v111\n"
      "ds_write_b64 v218, v[108:109] offset:20480\n"
      "v_pk_mul_f32 v[112:113], v[112:113], v[238:239]\n"
      "v_pk_mul_f32 v[114:115], v[114:115], v[238:239]\n"
      "v_cvt_pk_bf16_f32 v112, v112, v113\n"
      "v_cvt_pk_bf16_f32 v113, v114, v115\n"
      "ds_write_b64 v215, v[112:113] offset:20608\n"
      "v_pk_mul_f32 v[116:117], v[116:117], v[238:239]\n"
      "v_pk_mul_f32 v[118:119], v[118:119], v[238:239]\n"
      "v_cvt_pk_bf16_f32 v116, v116, v117\n"
      "v_cvt_pk_bf16_f32 v117, v118, v119\n"
      "ds_write_b64 v216, v[116:117] offset:20608\n"
      "v_pk_mul_f32 v[120:121], v[120:121], v[238:239]\n"
      "v_pk_mul_f32 v[122:123], v[122:123], v[238:239]\n"
      "v_cvt_pk_bf16_f32 v120, v120, v121\n"
      "v_cvt_pk_bf16_f32 v121, v122, v123\n"
      "ds_write_b64 v217, v[120:121] offset:20608\n"
      "v_pk_mul_f32 v[124:125], v[124:125], v[238:239]\n"
      "v_pk_mul_f32 v[126:127], v[126:127], v[238:239]\n"
      "v_cvt_pk_bf16_f32 v124, v124, v125\n"
      "v_cvt_pk_bf16_f32 v125, v126, v127\n"
      "ds_write_b64 v218, v[124:125] offset:20608\n"
      "s_waitcnt lgkmcnt(8)\n"
      "buffer_store_dwordx4 v[64:67], v214, s[52:55], s78 offen sc1\n"
      "buffer_store_dwordx4 v[68:71], v214, s[52:55], s78 offen offset:1024 sc1\n"
      "buffer_store_dwordx4 v[72:75], v214, s[52:55], s78 offen offset:2048 sc1\n"
      "buffer_store_dwordx4 v[76:79], v214, s[52:55], s78 offen offset:3072 sc1\n"
      "ds_read_b128 v[96:99], v219 offset:20480\n"
      "ds_read_b128 v[100:103], v220 offset:21504\n"
      "ds_read_b128 v[104:107], v219 offset:22528\n"
      "ds_read_b128 v[108:111], v220 offset:23552\n"
      "s_waitcnt lgkmcnt(0)\n"
      "buffer_store_dwordx4 v[96:99], v214, s[52:55], s79 offen sc1\n"
      "buffer_store_dwordx4 v[100:103], v214, s[52:55], s79 offen offset:1024 sc1\n"
      "buffer_store_dwordx4 v[104:107], v214, s[52:55], s79 offen offset:2048 sc1\n"
      "buffer_store_dwordx4 v[108:111], v214, s[52:55], s79 offen offset:3072 sc1\n"
      "s_waitcnt vmcnt(0) lgkmcnt(0)\n"
      "s_cmp_lg_u32 s64, 0\n"
      "s_cbranch_scc1 L_SLOWEXIT\n"
      "s_endpgm\n"
      "L_SLOWEXIT:\n"
      "L_CODEEND:\n"
      "s_mov_b32 %0, s64\n"
      : "=&s"(flags)
      : "s"(__builtin_amdgcn_kernarg_segment_ptr()), "n"(0), "n"(0), "n"(0), "s"(head0), "s"(wave), "s"(ldsb)
      :
        "v0", "v1", "v2", "v3", "v4", "v5", "v6", "v7", "v8", "v9", "v10", "v11", "v12", "v13", "v14", "v15", "v16", "v17", "v18", "v19", "v20", "v21", "v22", "v23",
        "v24", "v25", "v26", "v27", "v28", "v29", "v30", "v31", "v32", "v33", "v34", "v35", "v36", "v37", "v38", "v39", "v40", "v41", "v42", "v43", "v44", "v45", "v46", "v47",
        "v48", "v49", "v50", "v51", "v52", "v53", "v54", "v55", "v56", "v57", "v58", "v59", "v60", "v61", "v62", "v63", "v64", "v65", "v66", "v67", "v68", "v69", "v70", "v71",
        "v72", "v73", "v74", "v75", "v76", "v77", "v78", "v79", "v80", "v81", "v82", "v83", "v84", "v85", "v86", "v87", "v88", "v89", "v90", "v91", "v92", "v93", "v94", "v95",
        "v96", "v97", "v98", "v99", "v100", "v101", "v102", "v103", "v104", "v105", "v106", "v107", "v108", "v109", "v110", "v111", "v112", "v113", "v114", "v115", "v116", "v117", "v118", "v119",
        "v120", "v121", "v122", "v123", "v124", "v125", "v126", "v127", "v128", "v129", "v130", "v131", "v132", "v133", "v134", "v135", "v136", "v137", "v138", "v139", "v140", "v141", "v142", "v143",
        "v144", "v145", "v146", "v147", "v148", "v149", "v150", "v151", "v152", "v153", "v154", "v155", "v156", "v157", "v158", "v159", "v160", "v161", "v162", "v163", "v164", "v165", "v166", "v167",
        "v168", "v169", "v170", "v171", "v172", "v173", "v174", "v175", "v176", "v177", "v178", "v179", "v180", "v181", "v182", "v183", "v184", "v185", "v186", "v187", "v188", "v189", "v190", "v191",
        "v192", "v193", "v194", "v195", "v196", "v197", "v198", "v199", "v200", "v201", "v202", "v203", "v204", "v205", "v206", "v207", "v208", "v209", "v210", "v211", "v212", "v213", "v214", "v215",
        "v216", "v217", "v218", "v219", "v220", "v221", "v222", "v223", "v224", "v225", "v226", "v227", "v228", "v229", "v230", "v231", "v232", "v233", "v234", "v235", "v236", "v237", "v238", "v239",
        "v240", "v241", "v242", "v243", "v244", "v245", "v246", "v247", "v248", "v249", "v250", "v251", "v252", "v253", "v254", "v255", "a0", "a1", "a2", "a3", "a4", "a5", "a6", "a7",
        "a8", "a9", "a10", "a11", "a12", "a13", "a14", "a15", "a16", "a17", "a18", "a19", "a20", "a21", "a22", "a23", "a24", "a25", "a26", "a27", "a28", "a29", "a30", "a31",
        "a32", "a33", "a34", "a35", "a36", "a37", "a38", "a39", "a40", "a41", "a42", "a43", "a44", "a45", "a46", "a47", "a48", "a49", "a50", "a51", "a52", "a53", "a54", "a55",
        "a56", "a57", "a58", "a59", "a60", "a61", "a62", "a63", "a64", "a65", "a66", "a67", "a68", "a69", "a70", "a71", "a72", "a73", "a74", "a75", "a76", "a77", "a78", "a79",
        "a80", "a81", "a82", "a83", "a84", "a85", "a86", "a87", "a88", "a89", "a90", "a91", "a92", "a93", "a94", "a95", "a96", "a97", "a98", "a99", "a100", "a101", "a102", "a103",
        "a104", "a105", "a106", "a107", "a108", "a109", "a110", "a111", "a112", "a113", "a114", "a115", "a116", "a117", "a118", "a119", "a120", "a121", "a122", "a123", "a124", "a125", "a126", "a127",
        "a128", "a129", "a130", "a131", "a132", "a133", "a134", "a135", "a136", "a137", "a138", "a139", "a140", "a141", "a142", "a143", "a144", "a145", "a146", "a147", "a148", "a149", "a150", "a151",
        "a152", "a153", "a154", "a155", "a156", "a157", "a158", "a159", "a160", "a161", "a162", "a163", "a164", "a165", "a166", "a167", "a168", "a169", "a170", "a171", "a172", "a173", "a174", "a175",
        "a176", "a177", "a178", "a179", "a180", "a181", "a182", "a183", "a184", "a185", "a186", "a187", "a188", "a189", "a190", "a191", "a192", "a193", "a194", "a195", "a196", "a197", "a198", "a199",
        "a200", "a201", "a202", "a203", "a204", "a205", "a206", "a207", "a208", "a209", "a210", "a211", "a212", "a213", "a214", "a215", "a216", "a217", "a218", "a219", "a220", "a221", "a222", "a223",
        "a224", "a225", "a226", "a227", "a228", "a229", "a230", "a231", "a232", "a233", "a234", "a235", "a236", "a237", "a238", "a239", "a240", "a241", "a242", "a243", "a244", "a245", "a246", "a247",
        "a248", "a249", "a250", "a251", "a252", "a253", "a254", "a255", "s40", "s41", "s42", "s43", "s44", "s45", "s46", "s47", "s48", "s49", "s50", "s51", "s52", "s53", "s54", "s55",
        "s56", "s57", "s58", "s59", "s60", "s61", "s62", "s63", "s64", "s65", "s66", "s67", "s68", "s69", "s70", "s71", "s72", "s73", "s74", "s75", "s76", "s77", "s78", "s79",
        "s80", "s81", "s82", "s83", "s84", "s85", "s86", "s87", "s88", "s89", "s90", "s91", "s92", "s93", "s94", "s95", "s96", "s97", "s98", "s99", "vcc", "scc", "memory");
  if (flags) {
    for (unsigned i = 0; i < 4; ++i)
      if ((flags >> i) & 1u) slow_rows(q, k, v, o, head0 + GRID * i, wave);
  }
}

extern "C" void kernel_launch(void* const* d_in, const int* in_sizes, int n_in, void* d_out, int out_size, void* d_ws, size_t ws_size, hipStream_t stream) {
  (void)in_sizes; (void)n_in; (void)out_size; (void)d_ws; (void)ws_size;
  (void)hipFuncSetAttribute((const void*)mha_fwd_kernel, hipFuncAttributeMaxDynamicSharedMemorySize, LDS_BYTES);
  hipLaunchKernelGGL(mha_fwd_kernel, dim3(GRID), dim3(256), LDS_BYTES, stream, (const unsigned*)d_in[0], (const unsigned*)d_in[1], (const unsigned*)d_in[2], (unsigned*)d_out);
}
```
